```python
import torch

def _softmax(x, axis=-1):
    return torch.softmax(x, dim=axis)
def _gelu(x):
    return torch.nn.functional.gelu(x, approximate="tanh")
def _silu(x):
    return torch.nn.functional.silu(x)
def _sigmoid(x):
    return torch.sigmoid(x)
def _layernorm(x, axis=-1, eps=1e-5):
    m = x.mean(dim=axis, keepdim=True)
    v = x.var(dim=axis, keepdim=True, unbiased=False)
    return (x - m) / torch.sqrt(v + eps)
def _scatter_add(tgt, idx, src, axis=0):
    return tgt.clone().index_add_(axis, idx, src)
def _segment_sum(src, seg, n):
    out = torch.zeros((n,) + src.shape[1:], dtype=src.dtype)
    return out.index_add_(0, seg, src)
def _pad(x, pw):
    flat = tuple(v for pair in reversed(pw) for v in pair)
    return torch.nn.functional.pad(x, flat)

kernel_name = 'standalone__dcn_v2_cross__recsys'

def setup_inputs():

    _w = (lambda a: (lambda t: t.to(torch.bfloat16) if t.is_floating_point() else t)(torch.as_tensor(a)))
    B, F, D, L = 4096, 100, 32, 3
    Din = min(F * D, 1024)
    return {
        "x0": _w((lambda *s: torch.randn(*s, dtype=torch.bfloat16) * 0.05)(B, Din)),
        "ws": _w((lambda *s: torch.randn(*s, dtype=torch.bfloat16) * 0.05)(L, Din, Din)),
        "bs": _w((lambda *s: torch.randn(*s, dtype=torch.bfloat16) * 0.05)(L, Din)),
    }

def reference(x0, ws, bs):
    x = x0
    for l in range(ws.shape[0]):
        x = x0 * (x @ ws[l] + bs[l]) + x
    return x
```

```cpp
#include <hip/hip_runtime.h>
#include <hip/hip_bf16.h>
typedef __attribute__((ext_vector_type(8))) __bf16 bf16x8;
typedef __attribute__((ext_vector_type(16))) float f32x16;
typedef __attribute__((ext_vector_type(4))) unsigned u32x4;

static __device__ __forceinline__ float bf2f(unsigned short v) { return __uint_as_float(((unsigned)v) << 16); }

__global__ __launch_bounds__(256, 2) void dcn_layer(const unsigned short* __restrict__ x0, const unsigned short* __restrict__ x,
                                                    const unsigned short* __restrict__ W, const unsigned short* __restrict__ b,
                                                    unsigned short* __restrict__ out) {
  __shared__ __attribute__((aligned(16))) unsigned short sx[128 * 40];
  __shared__ __attribute__((aligned(16))) unsigned short sw[128 * 40];
  int bid = blockIdx.x, xcd = bid & 7, idx = bid >> 3;
  int panel = xcd * 4 + (idx >> 3), j = idx & 7;
  int tid = threadIdx.x, lane = tid & 63, wid = tid >> 6;
  int wm = wid >> 1, wn = wid & 1;
  int r = lane & 31, h = lane >> 5;
  f32x16 acc[2][2];
  for (int a = 0; a < 2; ++a) for (int c = 0; c < 2; ++c) for (int i = 0; i < 16; ++i) acc[a][c][i] = 0.f;
  for (int k0 = 0; k0 < 1024; k0 += 32) {
    for (int c = tid; c < 512; c += 256) {
      int row = c >> 2, ch = c & 3;
      u32x4 v = *(const u32x4*)(x + (size_t)(panel * 128 + row) * 1024 + k0 + ch * 8);
      *(u32x4*)(sx + row * 40 + ch * 8) = v;
    }
    for (int c = tid; c < 512; c += 256) {
      int k = c >> 4, ch = c & 15;
      u32x4 v = *(const u32x4*)(W + (size_t)(k0 + k) * 1024 + j * 128 + ch * 8);
      const unsigned short* p = (const unsigned short*)&v;
      for (int e = 0; e < 8; ++e) sw[(ch * 8 + e) * 40 + k] = p[e];
    }
    __syncthreads();
    for (int kk = 0; kk < 32; kk += 16) {
      bf16x8 af[2], bf[2];
      for (int a = 0; a < 2; ++a) af[a] = *(const bf16x8*)(sx + (wm * 64 + a * 32 + r) * 40 + kk + h * 8);
      for (int c = 0; c < 2; ++c) bf[c] = *(const bf16x8*)(sw + (wn * 64 + c * 32 + r) * 40 + kk + h * 8);
      for (int a = 0; a < 2; ++a) for (int c = 0; c < 2; ++c)
        acc[a][c] = __builtin_amdgcn_mfma_f32_32x32x16_bf16(af[a], bf[c], acc[a][c], 0, 0, 0);
    }
    __syncthreads();
  }
  for (int a = 0; a < 2; ++a) for (int c = 0; c < 2; ++c) {
    int n = j * 128 + wn * 64 + c * 32 + r;
    float bias = bf2f(b[n]);
    for (int i = 0; i < 16; ++i) {
      int m = panel * 128 + wm * 64 + a * 32 + (i & 3) + 8 * (i >> 2) + 4 * h;
      size_t o = (size_t)m * 1024 + n;
      float v = bf2f(x0[o]) * (acc[a][c][i] + bias) + bf2f(x[o]);
      __hip_bfloat16 hv = __float2bfloat16(v);
      out[o] = *(unsigned short*)&hv;
    }
  }
}

extern "C" void kernel_launch(void* const* d_in, const int* in_sizes, int n_in, void* d_out, int out_size, void* d_ws,
                              size_t ws_size, hipStream_t stream) {
  const unsigned short* x0 = (const unsigned short*)d_in[0];
  const unsigned short* ws = (const unsigned short*)d_in[1];
  const unsigned short* bs = (const unsigned short*)d_in[2];
  unsigned short* t1 = (unsigned short*)d_ws;
  unsigned short* t2 = t1 + 4096 * 1024;
  dcn_layer<<<256, 256, 0, stream>>>(x0, x0, ws, bs, t1);
  dcn_layer<<<256, 256, 0, stream>>>(x0, t1, ws + 1024 * 1024, bs + 1024, t2);
  dcn_layer<<<256, 256, 0, stream>>>(x0, t2, ws + 2 * 1024 * 1024, bs + 2048, (unsigned short*)d_out);
}
```

```cpp
#include <hip/hip_runtime.h>
#include <hip/hip_bf16.h>
static __device__ __forceinline__ float bf2f(unsigned short v) { return __uint_as_float(((unsigned)v) << 16); }
static __device__ __forceinline__ unsigned short f2bf(float f) { unsigned u = __float_as_uint(f); u += 0x7fffu + ((u >> 16) & 1u); return (unsigned short)(u >> 16); }
__global__ __launch_bounds__(256, 1) void dcn_naive(const unsigned short* __restrict__ x0, const unsigned short* __restrict__ W,
                                                    const unsigned short* __restrict__ bs, unsigned short* __restrict__ out) {
  __shared__ float xs[16384];
  unsigned long long t0;
  asm volatile("s_memrealtime %0\n s_waitcnt lgkmcnt(0)" : "=s"(t0));
  const int t = threadIdx.x, r0 = blockIdx.x * 16;
  float x0r[16][4], xr[16][4];
  for (int r = 0; r < 16; ++r) for (int c = 0; c < 4; ++c) { x0r[r][c] = bf2f(x0[(r0 + r) * 1024 + t * 4 + c]); xr[r][c] = x0r[r][c]; }
  for (int l = 0; l < 3; ++l) {
    __syncthreads();
    for (int r = 0; r < 16; ++r) for (int c = 0; c < 4; ++c) xs[(t * 4 + c) * 16 + r] = xr[r][c];
    __syncthreads();
    float acc[16][4];
    for (int r = 0; r < 16; ++r) for (int c = 0; c < 4; ++c) acc[r][c] = 0.f;
    for (int k = 0; k < 1024; ++k) {
      uint2 wv = *(const uint2*)(W + (size_t)l * 1048576 + k * 1024 + t * 4);
      float w0 = __uint_as_float(wv.x << 16), w1 = __uint_as_float(wv.x & 0xffff0000u), w2 = __uint_as_float(wv.y << 16), w3 = __uint_as_float(wv.y & 0xffff0000u);
      for (int r = 0; r < 16; ++r) { float xv = xs[k * 16 + r]; acc[r][0] += xv * w0; acc[r][1] += xv * w1; acc[r][2] += xv * w2; acc[r][3] += xv * w3; }
    }
    for (int r = 0; r < 16; ++r) for (int c = 0; c < 4; ++c) xr[r][c] = bf2f(f2bf(x0r[r][c] * (acc[r][c] + bf2f(bs[l * 1024 + t * 4 + c])) + xr[r][c]));
  }
  for (int r = 0; r < 16; ++r) for (int c = 0; c < 4; ++c) out[(r0 + r) * 1024 + t * 4 + c] = f2bf(xr[r][c]);


  unsigned long long t1;
  int n = 0;
  do { asm volatile("s_sleep 1\n s_memrealtime %0\n s_waitcnt lgkmcnt(0)" : "=s"(t1)); } while ((unsigned)(t1 - t0) < 200000u && ++n < 2000000);
}
extern "C" void kernel_launch(void* const* d_in, const int* in_sizes, int n_in, void* d_out, int out_size, void* d_ws, size_t ws_size, hipStream_t stream) {
  dcn_naive<<<256, 256, 0, stream>>>((const unsigned short*)d_in[0], (const unsigned short*)d_in[1], (const unsigned short*)d_in[2], (unsigned short*)d_out);
}
```

```cpp
#include <hip/hip_runtime.h>
#include <hip/hip_bf16.h>

__global__ __launch_bounds__(256, 1) void dcn_fused(const void* w0, unsigned w0n, unsigned w0f, const void* x0, unsigned x0n, unsigned x0f,
                                                    void* ws, unsigned wsn, unsigned wsf, void* out, unsigned outn, unsigned outf,
                                                    const void* w1, unsigned w1n, unsigned w1f, const void* w2, unsigned w2n, unsigned w2f,
                                                    const void* bs, unsigned bsn, unsigned bsf) {
  __shared__ __attribute__((aligned(1024))) unsigned char lds[157696];
  asm volatile(
R"ASM(
.macro mqa d,x,y
v_mfma_f32_16x16x32_bf16 v[\d:\d+3], a[\x:\x+3], a[\y:\y+3], v[\d:\d+3]
.endm
.macro mqv d,x,y
v_mfma_f32_16x16x32_bf16 v[\d:\d+3], a[\x:\x+3], v[\y:\y+3], v[\d:\d+3]
.endm
.macro tq d,r,o
ds_read_b64_tr_b16 a[\d:\d+1], v\r offset:\o
.endm
s_load_dwordx16 s[20:35], s[0:1], 0
s_load_dwordx8 s[72:79], s[0:1], 64
s_load_dwordx4 s[36:39], s[0:1], 96
s_getpc_b64 s[66:67]
.Ldcn_pc:
v_and_b32 v1, 0xf0, v0
v_lshlrev_b32 v1, 3, v1
global_load_dword v25, v1, s[66:67] offset:128
s_lshr_b32 s5, s2, 6
s_lshr_b32 s6, s2, 3
s_add_u32 s96, s5, s6
s_and_b32 s96, s96, 3
s_mul_i32 s96, s96, 36864
v_and_b32 v2, 0xe0, v0
v_add_u32 v2, 9, v2
v_min_u32 v2, v2, v0
v_lshlrev_b32 v2, 7, v2
s_add_u32 s96, s96, .Ldcn_v0-.Ldcn_pc+4096
s_add_u32 s96, s96, s66
s_addc_u32 s97, s67, 0
global_load_dword v25, v2, s[96:97] offset:0
global_load_dword v25, v1, s[96:97] offset:-4096
v_readfirstlane_b32 s3, v0
s_and_b32 s4, s2, 7
s_and_b32 s7, s6, 7
s_lshr_b32 s3, s3, 6
s_lshl2_add_u32 s8, s4, s5
s_lshl2_add_u32 s9, s8, s3
s_lshr_b32 s15, s7, 1
s_andn2_b32 s15, s7, s15
s_and_b32 s10, s15, 1
s_lshl1_add_u32 s11, s7, s10
s_sub_u32 s15, s6, s2
s_lshl_b32 s15, s15, 2
s_lshl_b32 s15, 0x1111111, s15
s_mul_i32 s16, s4, 0x1111111
s_add_u32 s15, s15, s16
s_add_u32 s15, s15, 0x6543210
s_lshr_b32 s16, s15, 1
s_andn2_b32 s16, s15, s16
s_and_b32 s16, s16, 0x1111111
s_lshl_b32 s15, s15, 1
s_and_b32 s15, s15, 0xeeeeeee
s_or_b32 s64, s15, s16
s_and_b32 s15, s64, 15
s_lshl_b32 s42, s15, 17
s_lshl_b32 s56, s3, 11
s_mul_i32 s57, s3, 12288
v_bfe_u32 v32, v0, 3, 3
v_bfe_u32 v33, v0, 4, 2
v_and_b32 v34, 7, v0
v_and_b32 v35, 63, v0
s_lshl_b32 s15, s9, 16
v_xor_b32 v39, v33, v34
v_lshl_or_b32 v40, v32, 11, s15
v_lshl_or_b32 v246, v39, 4, v40
v_xor_b32 v39, 64, v246
v_add_u32 v247, 15360, v39
v_add_u32 v248, 30720, v246
v_add_u32 v249, 46080, v39
s_lshl_b32 s15, s3, 8
s_or_b32 s15, s15, s11
s_lshl_b32 s15, s15, 7
v_lshlrev_b32 v39, 1, v33
v_xor_b32 v39, v39, v34
v_lshl_or_b32 v40, v32, 11, s15
v_lshl_or_b32 v10, v39, 4, v40
v_add_u32 v11, 15360, v10
v_xor_b32 v30, 0x80, v10
v_add_u32 v31, 15360, v30
s_lshl_b32 s15, s7, 8
s_or_b32 s15, s15, s56
v_lshl_or_b32 v226, v35, 2, s15
s_lshl2_add_u32 s12, s6, s3
s_lshl_b32 s15, s12, 7
v_lshl_add_u32 v225, v35, 14, s15
s_mov_b32 s15, L_last_%=-.Ldcn_pc-4
v_min_u32 v225, s15, v225
s_lshl_b32 s15, s3, 12
s_add_u32 s96, s96, s15
s_addc_u32 s97, s97, 0
v_and_b32 v36, 15, v0
s_lshl2_add_u32 s15, s2, s3
s_lshl_b32 s15, s15, 11
v_lshl_add_u32 v19, v36, 7, s15
s_lshl_b32 s15, s12, 14
s_add_u32 s15, s15, 384
s_mov_b64 exec, 0xffff
v_lshl_add_u32 v19, v35, 10, s15
s_mov_b64 exec, -1
v_bfe_u32 v37, v0, 5, 1
v_lshl_or_b32 v19, v37, 30, v19
s_lshl3_add_u32 s15, s9, s7
s_lshl_b32 s15, s15, 13
s_add_u32 s15, s15, 384
v_lshl_add_u32 v228, v35, 10, s15
v_lshl_or_b32 v228, v32, 24, v228
s_lshl3_add_u32 s15, s8, s7
s_lshl2_add_u32 s15, s15, s3
s_lshl_b32 s15, s15, 8
s_add_u32 s13, s15, 16777216
s_lshl_b32 s15, s9, 8
s_add_u32 s14, s15, 17301504
s_lshl2_add_u32 s15, s7, s14
s_sub_u32 s15, s15, 1024
v_lshl_add_u32 v224, v35, 2, s13
s_mov_b32 exec_lo, 0
v_bfrev_b32 v224, 2
s_mov_b32 exec_hi, 15
v_lshl_add_u32 v224, v35, 5, s15
s_mov_b64 exec, -1
s_lshl_b32 s15, s10, 8
v_lshlrev_b32 v227, 3, v0
v_xor_b32 v227, s15, v227
v_and_b32 v24, 48, v0
v_and_b32 v39, 60, v0
v_and_b32 v40, 24, v0
v_and_b32 v41, 3, v0
v_lshlrev_b32 v39, 5, v39
v_lshl_or_b32 v39, v40, 2, v39
v_lshl_or_b32 v4, v41, 3, v39
v_xor_b32 v5, 32, v4
v_xor_b32 v6, 64, v4
v_xor_b32 v7, 0x60, v4
s_add_u32 s15, s57, 108544
v_bfe_u32 v39, v0, 1, 3
v_xor_b32 v39, v39, v37
v_lshl_add_u32 v40, v36, 7, s15
v_lshl_add_u32 v40, v39, 4, v40
v_bfe_u32 v38, v0, 4, 1
v_lshl_add_u32 v252, v38, 3, v40
v_xor_b32 v253, 32, v252
v_xor_b32 v254, 64, v252
v_xor_b32 v255, 0x60, v252
s_waitcnt lgkmcnt(0)
buffer_load_dword v230, v226, s[36:39], 0 offen
s_lshr_b32 s58, s42, 10
s_add_u32 m0, s57, 108544
s_nop 0
buffer_load_dwordx4 v246, s[24:27], s58 offen offset:0 lds
buffer_load_dwordx4 v247, s[24:27], s58 offen offset:1024 lds
buffer_load_dwordx4 v248, s[24:27], s58 offen offset:2048 lds
buffer_load_dwordx4 v249, s[24:27], s58 offen offset:3072 lds
s_add_u32 m0, s56, 2048
s_nop 0
buffer_load_dwordx4 v10, s[20:23], s42 offen offset:0 lds
buffer_load_dwordx4 v11, s[20:23], s42 offen offset:1024 lds
s_add_u32 m0, s56, 10240
s_nop 0
buffer_load_dwordx4 v30, s[20:23], s42 offen offset:0 lds
buffer_load_dwordx4 v31, s[20:23], s42 offen offset:1024 lds
global_load_dword v25, v225, s[66:67]
s_bfe_u32 s44, s64, 262148
s_lshl_b32 s44, s44, 17
s_xor_b32 s45, s44, 0x20000
s_lshr_b32 s58, s44, 10
s_add_u32 m0, s57, 112640
s_nop 0
buffer_load_dwordx4 v246, s[24:27], s58 offen offset:0 lds
buffer_load_dwordx4 v247, s[24:27], s58 offen offset:1024 lds
buffer_load_dwordx4 v248, s[24:27], s58 offen offset:2048 lds
buffer_load_dwordx4 v249, s[24:27], s58 offen offset:3072 lds
s_add_u32 m0, s56, 18432
s_nop 0
buffer_load_dwordx4 v10, s[20:23], s44 offen offset:0 lds
buffer_load_dwordx4 v11, s[20:23], s44 offen offset:1024 lds
s_lshl_b32 s40, s11, 17
s_xor_b32 s41, s40, 0x20000
s_xor_b32 s43, s42, 0x20000
s_lshr_b32 s80, s40, 4
s_xor_b32 s81, s80, 0x2000
s_mov_b64 s[60:61], 0xff00
s_mov_b32 s70, 0x8000
v_add_u32 v8, 0x10000, v4
v_add_u32 v9, 0x10000, v5
v_add_u32 v12, 0x10000, v6
v_add_u32 v13, 0x10000, v7
v_add_u32 v16, 0x20000, v4
v_add_u32 v17, 0x20000, v5
v_add_u32 v28, 0x20000, v6
v_add_u32 v29, 0x20000, v7
s_lshl_b32 s15, s9, 17
v_lshl_add_u32 v14, v35, 4, s15
v_add_u32 v15, 0x1200000, v14
s_lshl_b32 s15, s9, 9
s_or_b32 s15, s15, s11
s_lshl_b32 s15, s15, 7
v_lshl_add_u32 v39, v36, 11, s15
v_lshl_add_u32 v39, v38, 5, v39
v_lshl_add_u32 v18, v37, 4, v39
v_xor_b32 v3, 0x80, v18
v_bfrev_b32 v20, 2
v_bfrev_b32 v21, 2
v_mov_b32 v22, 0
s_lshl_b32 s16, 1, s7
s_xor_b32 s16, s16, 0xff
s_mul_i32 s15, s7, 1020
s_sub_u32 s15, s13, s15
s_mov_b32 exec_hi, 0
s_mov_b32 exec_lo, s16
v_lshl_add_u32 v20, v34, 2, s13
v_mov_b32 v22, -1
s_lshl_b32 exec_lo, s16, 8
v_lshl_add_u32 v20, v34, 2, s14
v_lshl_add_u32 v21, v34, 10, s15
s_mov_b64 exec, -1
s_mov_b64 exec, 0
s_setpc_b64 s[96:97]
.p2align 12
.Ldcn_v0:
L_var0_%=:
L_ret_0_%=:
s_mov_b64 exec, -1
s_bfe_u32 s46, s64, 262152
s_lshl_b32 s46, s46, 17
s_xor_b32 s47, s46, 0x20000
s_add_u32 m0, s56, 26624
s_nop 0
buffer_load_dwordx4 v10, s[20:23], s46 offen offset:0 lds
buffer_load_dwordx4 v11, s[20:23], s46 offen offset:1024 lds
buffer_load_dword v25, v19, s[20:23], 0 offen
buffer_load_dword v25, v228, s[24:27], 0 offen
buffer_load_dword v229, v224, s[28:31], 0 offen sc1
s_waitcnt vmcnt(20)
v_lshlrev_b32 v231, 16, v230
v_and_b32 v23, 0xffff0000, v230
ds_write_b32 v227, v231
ds_write_b32 v227, v23 offset:4
s_waitcnt vmcnt(16)
ds_read_b64 a[0:1], v252 offset:0
ds_read_b64 a[2:3], v253 offset:0
ds_read_b64 a[4:5], v252 offset:2048
ds_read_b64 a[6:7], v253 offset:2048
ds_read_b64 a[8:9], v254 offset:0
ds_read_b64 a[10:11], v255 offset:0
ds_read_b64 a[12:13], v254 offset:2048
ds_read_b64 a[14:15], v255 offset:2048
s_waitcnt lgkmcnt(8)
s_waitcnt vmcnt(12)
s_barrier
ds_read_b128 v[64:67], v24 offset:0
ds_read_b128 v[68:71], v24 offset:64
ds_read_b128 v[72:75], v24 offset:0
ds_read_b128 v[76:79], v24 offset:64
ds_read_b128 v[80:83], v24 offset:128
ds_read_b128 v[84:87], v24 offset:192
ds_read_b128 v[88:91], v24 offset:128
ds_read_b128 v[92:95], v24 offset:192
tq 224,4,2048
tq 226,4,4096
tq 228,5,2048
tq 230,5,4096
tq 232,4,6144
tq 234,4,8192
tq 236,5,6144
tq 238,5,8192
tq 240,6,2048
tq 242,6,4096
tq 244,7,2048
tq 246,7,4096
tq 248,6,6144
tq 250,6,8192
s_nop 1
s_waitcnt lgkmcnt(6)
mqa 64,224,0
tq 252,7,6144
s_lshr_b32 s58, s46, 10
s_add_u32 m0, s57, 116736
mqa 72,224,4
tq 254,7,8192
buffer_load_dwordx4 v246, s[24:27], s58 offen offset:0 lds
mqa 76,228,4
tq 224,4,10240
ds_read_b128 v[96:99], v24 offset:256
ds_read_b128 v[100:103], v24 offset:320
ds_read_b128 v[104:107], v24 offset:256
mqa 68,228,0
tq 226,4,12288
buffer_load_dwordx4 v247, s[24:27], s58 offen offset:1024 lds
ds_read_b128 v[108:111], v24 offset:320
mqa 64,232,8
tq 228,5,10240
ds_read_b128 v[112:115], v24 offset:384
ds_read_b128 v[116:119], v24 offset:448
ds_read_b128 v[120:123], v24 offset:384
mqa 72,232,12
tq 230,5,12288
buffer_load_dwordx4 v248, s[24:27], s58 offen offset:2048 lds
ds_read_b128 v[124:127], v24 offset:448
mqa 76,236,12
tq 232,4,14336
mqa 68,236,8
tq 234,4,16384
buffer_load_dwordx4 v249, s[24:27], s58 offen offset:3072 lds
s_waitcnt lgkmcnt(14)
mqa 80,240,0
tq 236,5,14336
s_bfe_u32 s48, s64, 262156
s_lshl_b32 s48, s48, 17
s_xor_b32 s49, s48, 0x20000
mqa 88,240,4
s_add_u32 m0, s56, 34816
tq 238,5,16384
buffer_load_dwordx4 v10, s[20:23], s48 offen offset:0 lds
mqa 92,244,4
tq 240,6,10240
mqa 84,244,0
tq 242,6,12288
buffer_load_dwordx4 v11, s[20:23], s48 offen offset:1024 lds
s_waitcnt vmcnt(13)
ds_read_b64 a[32:33], v252 offset:4096
ds_read_b64 a[34:35], v253 offset:4096
mqa 80,248,8
tq 244,7,10240
s_add_u32 m0, s56, 43008
ds_read_b64 a[36:37], v252 offset:6144
mqa 88,248,12
tq 246,7,12288
buffer_load_dwordx4 v30, s[20:23], s46 offen offset:0 lds
ds_read_b64 a[38:39], v253 offset:6144
mqa 92,252,12
tq 248,6,14336
ds_read_b64 a[40:41], v254 offset:4096
ds_read_b64 a[42:43], v255 offset:4096
mqa 84,252,8
tq 250,6,16384
buffer_load_dwordx4 v31, s[20:23], s46 offen offset:1024 lds
s_waitcnt lgkmcnt(12)
mqa 96,224,0
tq 252,7,14336
tq 254,7,16384
ds_read_b64 a[44:45], v254 offset:6144
ds_read_b64 a[46:47], v255 offset:6144
mqa 104,224,4
s_lshr_b32 s58, s48, 10
s_add_u32 m0, s57, 108544
s_nop 0
buffer_load_dwordx4 v246, s[24:27], s58 offen offset:0 lds
s_waitcnt vmcnt(12)
s_barrier
mqa 108,228,4
tq 224,4,18432
mqa 100,228,0
tq 226,4,20480
buffer_load_dwordx4 v247, s[24:27], s58 offen offset:1024 lds
mqa 96,232,8
tq 228,5,18432
mqa 104,232,12
tq 230,5,20480
buffer_load_dwordx4 v248, s[24:27], s58 offen offset:2048 lds
mqa 108,236,12
tq 232,6,18432
mqa 100,236,8
tq 234,6,20480
buffer_load_dwordx4 v249, s[24:27], s58 offen offset:3072 lds
s_waitcnt lgkmcnt(8)
mqa 112,240,0
tq 236,7,18432
mqa 120,240,4
tq 238,7,20480
mqa 124,244,4
tq 240,4,22528
mqa 116,244,0
s_waitcnt lgkmcnt(9)
s_lshr_b32 s58, s40, 10
s_add_u32 m0, s57, 112640
tq 242,4,24576
buffer_load_dwordx4 v246, s[24:27], s58 offen offset:0 lds
s_waitcnt vmcnt(9)
ds_read_b64 a[64:65], v252 offset:8192
ds_read_b64 a[66:67], v253 offset:8192
mqa 112,248,8
tq 244,5,22528
ds_read_b64 a[68:69], v252 offset:10240
ds_read_b64 a[70:71], v253 offset:10240
mqa 120,248,12
tq 246,5,24576
buffer_load_dwordx4 v247, s[24:27], s58 offen offset:1024 lds
mqa 124,252,12
tq 248,6,22528
ds_read_b64 a[72:73], v254 offset:8192
ds_read_b64 a[74:75], v255 offset:8192
mqa 116,252,8
tq 250,6,24576
buffer_load_dwordx4 v248, s[24:27], s58 offen offset:2048 lds
s_waitcnt lgkmcnt(12)
mqa 64,224,32
tq 252,7,22528
ds_read_b64 a[76:77], v254 offset:10240
ds_read_b64 a[78:79], v255 offset:10240
mqa 72,224,36
tq 254,7,24576
buffer_load_dwordx4 v249, s[24:27], s58 offen offset:3072 lds
mqa 76,228,36
tq 224,4,26624
s_add_u32 m0, s56, 51200
mqa 68,228,32
tq 226,4,28672
buffer_load_dwordx4 v10, s[20:23], s40 offen offset:0 lds
mqa 80,232,32
tq 228,5,26624
mqa 88,232,36
tq 230,5,28672
buffer_load_dwordx4 v11, s[20:23], s40 offen offset:1024 lds
mqa 92,236,36
tq 232,6,26624
s_add_u32 m0, s56, 59392
mqa 84,236,32
tq 234,6,28672
buffer_load_dwordx4 v30, s[20:23], s40 offen offset:0 lds
s_waitcnt lgkmcnt(6)
mqa 64,240,40
tq 236,7,26624
mqa 72,240,44
tq 238,7,28672
buffer_load_dwordx4 v31, s[20:23], s40 offen offset:1024 lds
mqa 76,244,44
tq 240,4,30720
mqa 68,244,40
tq 242,4,32768
mqa 80,248,40
tq 244,5,30720
mqa 88,248,44
tq 246,5,32768
mqa 92,252,44
tq 248,6,30720
mqa 84,252,40
tq 250,6,32768
s_waitcnt lgkmcnt(6)
mqa 64,224,64
tq 252,7,30720
tq 254,7,32768
mqa 72,224,68
v_readlane_b32 s63, v229, 32
L_nonce_0_%=:
s_add_u32 s63, s63, 1
v_cmp_eq_u32 vcc, s63, v229
s_cbranch_vccnz L_nonce_0_%=
v_mov_b32 v230, s63
s_mov_b32 exec_lo, 0
s_mov_b32 exec_hi, 15
buffer_store_dword v230, v224, s[28:31], 0 offen sc1
s_mov_b64 exec, -1
v_and_b32 v22, v22, v230
s_waitcnt vmcnt(13)
s_barrier
mqa 76,228,68
tq 224,4,43008
mqa 68,228,64
tq 226,4,45056
mqa 80,232,64
tq 228,5,43008
mqa 88,232,68
tq 230,5,45056
mqa 92,236,68
tq 232,6,43008
mqa 84,236,64
tq 234,6,45056
s_waitcnt lgkmcnt(6)
mqa 64,240,72
tq 236,7,43008
mqa 72,240,76
tq 238,7,45056
mqa 76,244,76
tq 240,4,47104
s_bfe_u32 s50, s64, 262160
s_lshl_b32 s50, s50, 17
s_xor_b32 s51, s50, 0x20000
mqa 68,244,72
s_lshr_b32 s58, s50, 10
s_add_u32 m0, s57, 116736
tq 242,4,49152
buffer_load_dwordx4 v246, s[24:27], s58 offen offset:0 lds
s_waitcnt vmcnt(10)
ds_read_b64 a[96:97], v252 offset:0
ds_read_b64 a[98:99], v253 offset:0
mqa 80,248,72
tq 244,5,47104
ds_read_b64 a[100:101], v252 offset:2048
ds_read_b64 a[102:103], v253 offset:2048
mqa 88,248,76
tq 246,5,49152
buffer_load_dwordx4 v247, s[24:27], s58 offen offset:1024 lds
mqa 92,252,76
tq 248,6,47104
ds_read_b64 a[104:105], v254 offset:0
ds_read_b64 a[106:107], v255 offset:0
mqa 84,252,72
tq 250,6,49152
buffer_load_dwordx4 v248, s[24:27], s58 offen offset:2048 lds
s_waitcnt lgkmcnt(12)
mqa 96,224,64
tq 252,7,47104
ds_read_b64 a[108:109], v254 offset:2048
ds_read_b64 a[110:111], v255 offset:2048
mqa 104,224,68
tq 254,7,49152
buffer_load_dwordx4 v249, s[24:27], s58 offen offset:3072 lds
mqa 108,228,68
tq 224,4,34816
s_bfe_u32 s52, s64, 262164
s_lshl_b32 s52, s52, 17
s_xor_b32 s53, s52, 0x20000
mqa 100,228,64
s_add_u32 m0, s56, 67584
tq 226,4,36864
buffer_load_dwordx4 v10, s[20:23], s52 offen offset:0 lds
mqa 112,232,64
tq 228,5,34816
mqa 120,232,68
tq 230,5,36864
buffer_load_dwordx4 v11, s[20:23], s52 offen offset:1024 lds
mqa 124,236,68
tq 232,6,34816
s_add_u32 m0, s56, 75776
mqa 116,236,64
tq 234,6,36864
buffer_load_dwordx4 v10, s[20:23], s50 offen offset:0 lds
s_waitcnt lgkmcnt(6)
mqa 96,240,72
tq 236,7,34816
mqa 104,240,76
tq 238,7,36864
buffer_load_dwordx4 v11, s[20:23], s50 offen offset:1024 lds
mqa 108,244,76
tq 240,4,38912
mqa 100,244,72
s_lshr_b32 s58, s52, 10
s_add_u32 m0, s57, 108544
tq 242,4,40960
buffer_load_dwordx4 v246, s[24:27], s58 offen offset:0 lds
s_waitcnt vmcnt(14)
ds_read_b64 v[32:33], v252 offset:4096
ds_read_b64 v[34:35], v253 offset:4096
mqa 112,248,72
tq 244,5,38912
ds_read_b64 v[36:37], v252 offset:6144
ds_read_b64 v[38:39], v253 offset:6144
mqa 120,248,76
tq 246,5,40960
buffer_load_dwordx4 v247, s[24:27], s58 offen offset:1024 lds
mqa 124,252,76
tq 248,6,38912
ds_read_b64 v[40:41], v254 offset:4096
ds_read_b64 v[42:43], v255 offset:4096
mqa 116,252,72
tq 250,6,40960
buffer_load_dwordx4 v248, s[24:27], s58 offen offset:2048 lds
s_waitcnt lgkmcnt(12)
mqa 64,224,96
tq 252,7,38912
tq 254,7,40960
ds_read_b64 v[44:45], v254 offset:6144
ds_read_b64 v[46:47], v255 offset:6144
mqa 72,224,100
buffer_load_dwordx4 v249, s[24:27], s58 offen offset:3072 lds
s_waitcnt vmcnt(13)
s_barrier
mqa 76,228,100
tq 224,4,51200
mqa 68,228,96
tq 226,4,53248
mqa 80,232,96
tq 228,5,51200
mqa 88,232,100
tq 230,5,53248
mqa 92,236,100
tq 232,6,51200
mqa 84,236,96
tq 234,6,53248
s_waitcnt lgkmcnt(8)
mqa 64,240,104
tq 236,7,51200
mqa 72,240,108
tq 238,7,53248
mqa 76,244,108
tq 240,4,55296
s_bfe_u32 s54, s64, 262168
s_lshl_b32 s54, s54, 17
s_xor_b32 s55, s54, 0x20000
mqa 68,244,104
s_waitcnt lgkmcnt(9)
s_lshr_b32 s58, s54, 10
s_add_u32 m0, s57, 112640
tq 242,4,57344
buffer_load_dwordx4 v246, s[24:27], s58 offen offset:0 lds
mqa 80,248,104
tq 244,5,55296
mqa 88,248,108
tq 246,5,57344
buffer_load_dwordx4 v247, s[24:27], s58 offen offset:1024 lds
mqa 92,252,108
tq 248,6,55296
mqa 84,252,104
tq 250,6,57344
buffer_load_dwordx4 v248, s[24:27], s58 offen offset:2048 lds
s_waitcnt lgkmcnt(6)
mqv 64,224,32
tq 252,7,55296
mqv 72,224,36
tq 254,7,57344
buffer_load_dwordx4 v249, s[24:27], s58 offen offset:3072 lds
mqv 76,228,36
tq 224,4,59392
s_add_u32 m0, s56, 83968
mqv 68,228,32
tq 226,4,61440
buffer_load_dwordx4 v10, s[20:23], s54 offen offset:0 lds
mqv 80,232,32
tq 228,5,59392
v_lshlrev_b32 v128, 16, v32
mqv 88,232,36
tq 230,5,61440
buffer_load_dwordx4 v11, s[20:23], s54 offen offset:1024 lds
mqv 92,236,36
tq 232,6,59392
s_add_u32 m0, s56, 92160
mqv 84,236,32
tq 234,6,61440
buffer_load_dwordx4 v10, s[20:23], s41 offen offset:0 lds
s_waitcnt lgkmcnt(6)
mqv 64,240,40
tq 236,7,59392
v_and_b32 v129, 0xffff0000, v32
mqv 72,240,44
tq 238,7,61440
buffer_load_dwordx4 v11, s[20:23], s41 offen offset:1024 lds
mqv 76,244,44
tq 240,4,63488
mqv 68,244,40
tq 242,8,0
s_waitcnt vmcnt(16)
ds_read_b64 a[128:129], v252 offset:8192
ds_read_b64 a[130:131], v253 offset:8192
mqv 80,248,40
tq 244,5,63488
ds_read_b64 a[132:133], v252 offset:10240
mqv 88,248,44
tq 246,9,0
ds_read_b64 a[134:135], v253 offset:10240
mqv 92,252,44
tq 248,6,63488
ds_read_b64 a[136:137], v254 offset:8192
ds_read_b64 a[138:139], v255 offset:8192
mqv 84,252,40
tq 250,12,0
s_waitcnt lgkmcnt(12)
mqv 96,224,32
tq 252,7,63488
tq 254,13,0
ds_read_b64 a[140:141], v254 offset:10240
ds_read_b64 a[142:143], v255 offset:10240
mqv 104,224,36
s_waitcnt vmcnt(12)
s_barrier
mqv 108,228,36
tq 224,8,10240
v_lshlrev_b32 v130, 16, v33
mqv 100,228,32
tq 226,8,12288
v_and_b32 v131, 0xffff0000, v33
mqv 112,232,32
tq 228,9,10240
v_lshlrev_b32 v132, 16, v34
mqv 120,232,36
tq 230,9,12288
v_and_b32 v133, 0xffff0000, v34
mqv 124,236,36
tq 232,12,10240
mqv 116,236,32
s_waitcnt lgkmcnt(5)
s_lshr_b32 s58, s41, 10
s_add_u32 m0, s57, 116736
tq 234,12,12288
buffer_load_dwordx4 v246, s[24:27], s58 offen offset:0 lds
mqv 96,240,40
tq 236,13,10240
v_lshlrev_b32 v134, 16, v35
mqv 104,240,44
tq 238,13,12288
buffer_load_dwordx4 v247, s[24:27], s58 offen offset:1024 lds
mqv 108,244,44
tq 240,8,14336
mqv 100,244,40
tq 242,8,16384
buffer_load_dwordx4 v248, s[24:27], s58 offen offset:2048 lds
s_waitcnt vmcnt(11)
ds_read_b64 a[160:161], v252 offset:0
ds_read_b64 a[162:163], v253 offset:0
mqv 112,248,40
tq 244,9,14336
ds_read_b64 a[164:165], v252 offset:2048
ds_read_b64 a[166:167], v253 offset:2048
mqv 120,248,44
tq 246,9,16384
buffer_load_dwordx4 v249, s[24:27], s58 offen offset:3072 lds
mqv 124,252,44
tq 248,12,14336
ds_read_b64 a[168:169], v254 offset:0
ds_read_b64 a[170:171], v255 offset:0
mqv 116,252,40
tq 250,12,16384
s_waitcnt lgkmcnt(12)
mqa 64,224,128
tq 252,13,14336
s_add_u32 m0, s56, 100352
ds_read_b64 a[172:173], v254 offset:2048
mqa 72,224,132
tq 254,13,16384
buffer_load_dwordx4 v10, s[20:23], s43 offen offset:0 lds
ds_read_b64 a[174:175], v255 offset:2048
mqa 76,228,132
tq 224,8,2048
v_and_b32 v135, 0xffff0000, v35
mqa 68,228,128
tq 226,8,4096
buffer_load_dwordx4 v11, s[20:23], s43 offen offset:1024 lds
mqa 80,232,128
tq 228,9,2048
s_add_u32 m0, s56, 2048
mqa 88,232,132
tq 230,9,4096
buffer_load_dwordx4 v30, s[20:23], s41 offen offset:0 lds
mqa 92,236,132
tq 232,12,2048
mqa 84,236,128
tq 234,12,4096
buffer_load_dwordx4 v31, s[20:23], s41 offen offset:1024 lds
s_waitcnt lgkmcnt(7)
mqa 64,240,136
tq 236,13,2048
v_lshlrev_b32 v136, 16, v36
mqa 72,240,140
tq 238,13,4096
v_and_b32 v137, 0xffff0000, v36
mqa 76,244,140
tq 240,8,6144
mqa 68,244,136
s_waitcnt lgkmcnt(9)
s_lshr_b32 s58, s43, 10
s_add_u32 m0, s57, 108544
tq 242,8,8192
buffer_load_dwordx4 v246, s[24:27], s58 offen offset:0 lds
s_waitcnt vmcnt(13)
ds_read_b64 a[192:193], v252 offset:4096
ds_read_b64 a[194:195], v253 offset:4096
mqa 80,248,136
tq 244,9,6144
ds_read_b64 a[196:197], v252 offset:6144
ds_read_b64 a[198:199], v253 offset:6144
mqa 88,248,140
tq 246,9,8192
buffer_load_dwordx4 v247, s[24:27], s58 offen offset:1024 lds
mqa 92,252,140
tq 248,12,6144
ds_read_b64 a[200:201], v254 offset:4096
ds_read_b64 a[202:203], v255 offset:4096
mqa 84,252,136
tq 250,12,8192
buffer_load_dwordx4 v248, s[24:27], s58 offen offset:2048 lds
s_waitcnt lgkmcnt(12)
s_nop 0
L_hop1_0_%=:
s_cbranch_execz L_hop5_0_%=
mqa 64,224,160
tq 252,13,6144
tq 254,13,8192
ds_read_b64 a[204:205], v254 offset:6144
ds_read_b64 a[206:207], v255 offset:6144
mqa 72,224,164
buffer_load_dwordx4 v249, s[24:27], s58 offen offset:3072 lds
s_waitcnt vmcnt(12)
s_barrier
mqa 76,228,164
tq 224,8,18432
v_lshlrev_b32 v138, 16, v37
mqa 68,228,160
tq 226,8,20480
v_and_b32 v139, 0xffff0000, v37
mqa 80,232,160
tq 228,9,18432
v_lshlrev_b32 v140, 16, v38
mqa 88,232,164
tq 230,9,20480
v_and_b32 v141, 0xffff0000, v38
mqa 92,236,164
tq 232,12,18432
v_lshlrev_b32 v142, 16, v39
mqa 84,236,160
tq 234,12,20480
s_waitcnt lgkmcnt(8)
mqa 64,240,168
tq 236,13,18432
v_and_b32 v143, 0xffff0000, v39
mqa 72,240,172
tq 238,13,20480
v_lshlrev_b32 v144, 16, v40
mqa 76,244,172
tq 240,8,22528
mqa 68,244,168
s_waitcnt lgkmcnt(9)
s_lshr_b32 s58, s45, 10
s_add_u32 m0, s57, 112640
tq 242,8,24576
buffer_load_dwordx4 v246, s[24:27], s58 offen offset:0 lds
s_waitcnt vmcnt(9)
ds_read_b64 v[48:49], v252 offset:8192
ds_read_b64 v[50:51], v253 offset:8192
mqa 80,248,168
tq 244,9,22528
ds_read_b64 v[52:53], v252 offset:10240
ds_read_b64 v[54:55], v253 offset:10240
mqa 88,248,172
tq 246,9,24576
buffer_load_dwordx4 v247, s[24:27], s58 offen offset:1024 lds
mqa 92,252,172
tq 248,12,22528
ds_read_b64 v[56:57], v254 offset:8192
ds_read_b64 v[58:59], v255 offset:8192
mqa 84,252,168
tq 250,12,24576
buffer_load_dwordx4 v248, s[24:27], s58 offen offset:2048 lds
s_waitcnt lgkmcnt(12)
mqa 64,224,192
tq 252,13,22528
ds_read_b64 v[60:61], v254 offset:10240
ds_read_b64 v[62:63], v255 offset:10240
mqa 72,224,196
tq 254,13,24576
buffer_load_dwordx4 v249, s[24:27], s58 offen offset:3072 lds
mqa 76,228,196
tq 224,8,26624
s_add_u32 m0, s56, 10240
mqa 68,228,192
tq 226,8,28672
buffer_load_dwordx4 v10, s[20:23], s45 offen offset:0 lds
mqa 80,232,192
tq 228,9,26624
v_and_b32 v145, 0xffff0000, v40
mqa 88,232,196
tq 230,9,28672
buffer_load_dwordx4 v11, s[20:23], s45 offen offset:1024 lds
mqa 92,236,196
tq 232,12,26624
s_add_u32 m0, s56, 18432
mqa 84,236,192
tq 234,12,28672
buffer_load_dwordx4 v10, s[20:23], s47 offen offset:0 lds
s_waitcnt lgkmcnt(6)
mqa 64,240,200
tq 236,13,26624
v_lshlrev_b32 v146, 16, v41
mqa 72,240,204
tq 238,13,28672
buffer_load_dwordx4 v11, s[20:23], s47 offen offset:1024 lds
mqa 76,244,204
tq 240,8,30720
mqa 68,244,200
s_lshr_b32 s58, s47, 10
s_add_u32 m0, s57, 116736
tq 242,8,32768
buffer_load_dwordx4 v246, s[24:27], s58 offen offset:0 lds
mqa 80,248,200
tq 244,9,30720
v_and_b32 v147, 0xffff0000, v41
mqa 88,248,204
tq 246,9,32768
buffer_load_dwordx4 v247, s[24:27], s58 offen offset:1024 lds
mqa 92,252,204
tq 248,12,30720
mqa 84,252,200
tq 250,12,32768
buffer_load_dwordx4 v248, s[24:27], s58 offen offset:2048 lds
s_waitcnt lgkmcnt(6)
mqv 64,224,48
tq 252,13,30720
tq 254,13,32768
mqv 72,224,52
buffer_load_dwordx4 v249, s[24:27], s58 offen offset:3072 lds
s_waitcnt vmcnt(16)
s_barrier
mqv 76,228,52
tq 224,4,2048
v_lshlrev_b32 v148, 16, v42
mqv 68,228,48
tq 226,4,4096
v_and_b32 v149, 0xffff0000, v42
mqv 80,232,48
tq 228,5,2048
v_lshlrev_b32 v150, 16, v43
mqv 88,232,52
tq 230,5,4096
v_and_b32 v151, 0xffff0000, v43
mqv 92,236,52
tq 232,6,2048
v_lshlrev_b32 v152, 16, v44
mqv 84,236,48
tq 234,6,4096
s_waitcnt lgkmcnt(6)
mqv 64,240,56
tq 236,7,2048
v_and_b32 v153, 0xffff0000, v44
mqv 72,240,60
tq 238,7,4096
v_lshlrev_b32 v154, 16, v45
mqv 76,244,60
tq 240,4,6144
mqv 68,244,56
tq 242,4,8192
s_waitcnt vmcnt(12)
ds_read_b64 a[16:17], v252 offset:0
ds_read_b64 a[18:19], v253 offset:0
mqv 80,248,56
tq 244,5,6144
ds_read_b64 a[20:21], v252 offset:2048
mqv 88,248,60
tq 246,5,8192
ds_read_b64 a[22:23], v253 offset:2048
mqv 92,252,60
tq 248,6,6144
ds_read_b64 a[24:25], v254 offset:0
ds_read_b64 a[26:27], v255 offset:0
mqv 84,252,56
tq 250,6,8192
s_waitcnt lgkmcnt(12)
mqv 96,224,48
tq 252,7,6144
ds_read_b64 a[28:29], v254 offset:2048
ds_read_b64 a[30:31], v255 offset:2048
mqv 104,224,52
tq 254,7,8192
buffer_load_dword v23, v20, s[28:31], 0 offen sc1
s_add_u32 m0, s56, 26624
s_nop 0
buffer_load_dwordx4 v10, s[20:23], s51 offen offset:0 lds
mqv 108,228,52
tq 224,8,34816
v_and_b32 v155, 0xffff0000, v45
mqv 100,228,48
tq 226,8,36864
buffer_load_dwordx4 v11, s[20:23], s51 offen offset:1024 lds
mqv 112,232,48
tq 228,9,34816
s_add_u32 m0, s56, 43008
mqv 120,232,52
tq 230,9,36864
buffer_load_dwordx4 v10, s[20:23], s49 offen offset:0 lds
mqv 124,236,52
tq 232,12,34816
mqv 116,236,48
tq 234,12,36864
buffer_load_dwordx4 v11, s[20:23], s49 offen offset:1024 lds
s_waitcnt lgkmcnt(6)
mqv 96,240,56
tq 236,13,34816
mqv 104,240,60
s_lshr_b32 s58, s49, 10
s_add_u32 m0, s57, 108544
tq 238,13,36864
buffer_load_dwordx4 v246, s[24:27], s58 offen offset:0 lds
mqv 108,244,60
tq 240,8,38912
mqv 100,244,56
tq 242,8,40960
buffer_load_dwordx4 v247, s[24:27], s58 offen offset:1024 lds
s_waitcnt vmcnt(15)
ds_read_b64 a[48:49], v252 offset:4096
ds_read_b64 a[50:51], v253 offset:4096
mqv 112,248,56
tq 244,9,38912
ds_read_b64 a[52:53], v252 offset:6144
ds_read_b64 a[54:55], v253 offset:6144
mqv 120,248,60
tq 246,9,40960
buffer_load_dwordx4 v248, s[24:27], s58 offen offset:2048 lds
mqv 124,252,60
tq 248,12,38912
ds_read_b64 a[56:57], v254 offset:4096
ds_read_b64 a[58:59], v255 offset:4096
mqv 116,252,56
tq 250,12,40960
buffer_load_dwordx4 v249, s[24:27], s58 offen offset:3072 lds
s_waitcnt lgkmcnt(12)
mqa 64,224,16
tq 252,13,38912
tq 254,13,40960
ds_read_b64 a[60:61], v254 offset:6144
ds_read_b64 a[62:63], v255 offset:6144
mqa 72,224,20
s_waitcnt vmcnt(13)
s_barrier
mqa 76,228,20
tq 224,4,10240
v_lshlrev_b32 v156, 16, v46
mqa 68,228,16
tq 226,4,12288
v_and_b32 v157, 0xffff0000, v46
mqa 80,232,16
tq 228,5,10240
v_lshlrev_b32 v158, 16, v47
mqa 88,232,20
tq 230,5,12288
v_and_b32 v159, 0xffff0000, v47
mqa 92,236,20
tq 232,6,10240
mqa 84,236,16
s_waitcnt lgkmcnt(5)
s_lshr_b32 s58, s51, 10
s_add_u32 m0, s57, 112640
tq 234,6,12288
buffer_load_dwordx4 v246, s[24:27], s58 offen offset:0 lds
mqa 64,240,24
tq 236,7,10240
v_lshlrev_b32 v160, 16, v48
mqa 72,240,28
tq 238,7,12288
buffer_load_dwordx4 v247, s[24:27], s58 offen offset:1024 lds
mqa 76,244,28
tq 240,4,14336
mqa 68,244,24
tq 242,4,16384
buffer_load_dwordx4 v248, s[24:27], s58 offen offset:2048 lds
s_waitcnt vmcnt(12)
ds_read_b64 a[80:81], v252 offset:8192
ds_read_b64 a[82:83], v253 offset:8192
mqa 80,248,24
tq 244,5,14336
ds_read_b64 a[84:85], v252 offset:10240
ds_read_b64 a[86:87], v253 offset:10240
mqa 88,248,28
tq 246,5,16384
buffer_load_dwordx4 v249, s[24:27], s58 offen offset:3072 lds
mqa 92,252,28
tq 248,6,14336
ds_read_b64 a[88:89], v254 offset:8192
ds_read_b64 a[90:91], v255 offset:8192
mqa 84,252,24
tq 250,6,16384
s_waitcnt lgkmcnt(12)
mqa 64,224,48
tq 252,7,14336
s_add_u32 m0, s56, 34816
ds_read_b64 a[92:93], v254 offset:10240
mqa 72,224,52
tq 254,7,16384
buffer_load_dwordx4 v10, s[20:23], s53 offen offset:0 lds
ds_read_b64 a[94:95], v255 offset:10240
mqa 76,228,52
tq 224,4,18432
v_and_b32 v161, 0xffff0000, v48
mqa 68,228,48
tq 226,4,20480
buffer_load_dwordx4 v11, s[20:23], s53 offen offset:1024 lds
mqa 80,232,48
tq 228,5,18432
s_add_u32 m0, s56, 51200
mqa 88,232,52
tq 230,5,20480
buffer_load_dwordx4 v10, s[20:23], s55 offen offset:0 lds
mqa 92,236,52
tq 232,6,18432
mqa 84,236,48
tq 234,6,20480
buffer_load_dwordx4 v11, s[20:23], s55 offen offset:1024 lds
s_waitcnt lgkmcnt(7)
mqa 64,240,56
tq 236,7,18432
mqa 72,240,60
s_waitcnt lgkmcnt(7)
s_lshr_b32 s58, s53, 10
s_add_u32 m0, s57, 116736
tq 238,7,20480
buffer_load_dwordx4 v246, s[24:27], s58 offen offset:0 lds
mqa 76,244,60
tq 240,4,22528
mqa 68,244,56
tq 242,4,24576
buffer_load_dwordx4 v247, s[24:27], s58 offen offset:1024 lds
s_waitcnt vmcnt(10)
ds_read_b64 a[112:113], v252 offset:0
ds_read_b64 a[114:115], v253 offset:0
mqa 80,248,56
tq 244,5,22528
ds_read_b64 a[116:117], v252 offset:2048
ds_read_b64 a[118:119], v253 offset:2048
mqa 88,248,60
tq 246,5,24576
buffer_load_dwordx4 v248, s[24:27], s58 offen offset:2048 lds
mqa 92,252,60
tq 248,6,22528
ds_read_b64 a[120:121], v254 offset:0
ds_read_b64 a[122:123], v255 offset:0
mqa 84,252,56
tq 250,6,24576
buffer_load_dwordx4 v249, s[24:27], s58 offen offset:3072 lds
s_waitcnt lgkmcnt(12)
mqa 64,224,80
tq 252,7,22528
tq 254,7,24576
ds_read_b64 a[124:125], v254 offset:2048
ds_read_b64 a[126:127], v255 offset:2048
mqa 72,224,84
s_barrier
mqa 76,228,84
tq 224,4,43008
v_lshlrev_b32 v162, 16, v49
mqa 68,228,80
tq 226,4,45056
v_and_b32 v163, 0xffff0000, v49
mqa 80,232,80
tq 228,5,43008
v_lshlrev_b32 v164, 16, v50
mqa 88,232,84
tq 230,5,45056
v_and_b32 v165, 0xffff0000, v50
mqa 92,236,84
tq 232,6,43008
mqa 84,236,80
s_waitcnt lgkmcnt(5)
s_lshr_b32 s58, s55, 10
s_add_u32 m0, s57, 108544
tq 234,6,45056
buffer_load_dwordx4 v246, s[24:27], s58 offen offset:0 lds
mqa 64,240,88
tq 236,7,43008
v_lshlrev_b32 v166, 16, v51
mqa 72,240,92
tq 238,7,45056
buffer_load_dwordx4 v247, s[24:27], s58 offen offset:1024 lds
mqa 76,244,92
tq 240,4,47104
mqa 68,244,88
tq 242,4,49152
buffer_load_dwordx4 v248, s[24:27], s58 offen offset:2048 lds
s_waitcnt vmcnt(11)
ds_read_b64 a[144:145], v252 offset:4096
ds_read_b64 a[146:147], v253 offset:4096
mqa 80,248,88
tq 244,5,47104
ds_read_b64 a[148:149], v252 offset:6144
ds_read_b64 a[150:151], v253 offset:6144
mqa 88,248,92
tq 246,5,49152
buffer_load_dwordx4 v249, s[24:27], s58 offen offset:3072 lds
mqa 92,252,92
tq 248,6,47104
ds_read_b64 a[152:153], v254 offset:4096
ds_read_b64 a[154:155], v255 offset:4096
mqa 84,252,88
tq 250,6,49152
buffer_load_dword v25, v19, s[72:75], 0 offen
s_waitcnt lgkmcnt(12)
mqa 64,224,112
tq 252,7,47104
s_add_u32 m0, s56, 59392
ds_read_b64 a[156:157], v254 offset:6144
mqa 72,224,116
tq 254,7,49152
buffer_load_dwordx4 v30, s[20:23], s48 offen offset:0 lds
ds_read_b64 a[158:159], v255 offset:6144
mqa 76,228,116
tq 224,4,26624
v_and_b32 v167, 0xffff0000, v51
mqa 68,228,112
tq 226,4,28672
buffer_load_dwordx4 v31, s[20:23], s48 offen offset:1024 lds
mqa 80,232,112
tq 228,5,26624
s_add_u32 m0, s56, 75776
mqa 88,232,116
tq 230,5,28672
buffer_load_dwordx4 v30, s[20:23], s44 offen offset:0 lds
mqa 92,236,116
tq 232,6,26624
mqa 84,236,112
tq 234,6,28672
buffer_load_dwordx4 v31, s[20:23], s44 offen offset:1024 lds
s_waitcnt vmcnt(9)
ds_read_b64 a[176:177], v252 offset:8192
ds_read_b64 a[178:179], v253 offset:8192
s_waitcnt lgkmcnt(9)
mqa 64,240,120
tq 236,7,26624
ds_read_b64 a[180:181], v252 offset:10240
mqa 72,240,124
tq 238,7,28672
ds_read_b64 a[182:183], v253 offset:10240
mqa 76,244,124
tq 240,4,30720
ds_read_b64 a[184:185], v254 offset:8192
mqa 68,244,120
tq 242,4,32768
ds_read_b64 a[186:187], v255 offset:8192
mqa 80,248,120
tq 244,5,30720
ds_read_b64 a[188:189], v254 offset:10240
mqa 88,248,124
tq 246,5,32768
ds_read_b64 a[190:191], v255 offset:10240
mqa 92,252,124
tq 248,6,30720
mqa 84,252,120
tq 250,6,32768
s_waitcnt vmcnt(5)
ds_read_b64 a[208:209], v252 offset:0
ds_read_b64 a[210:211], v253 offset:0
s_waitcnt lgkmcnt(13)
mqa 64,224,144
tq 252,7,30720
tq 254,7,32768
ds_read_b64 a[212:213], v252 offset:2048
ds_read_b64 a[214:215], v253 offset:2048
mqa 72,224,148
s_barrier
mqa 76,228,148
tq 224,4,34816
ds_read_b64 a[216:217], v254 offset:0
mqa 68,228,144
tq 226,4,36864
ds_read_b64 a[218:219], v255 offset:0
mqa 80,232,144
tq 228,4,38912
ds_read_b64 a[220:221], v254 offset:2048
mqa 88,232,148
tq 230,4,40960
ds_read_b64 a[222:223], v255 offset:2048
mqa 92,236,148
tq 232,4,51200
s_add_u32 m0, s56, 67584
mqa 84,236,144
tq 234,4,53248
buffer_load_dwordx4 v30, s[20:23], s54 offen offset:0 lds
s_waitcnt lgkmcnt(12)
mqa 64,240,152
tq 236,4,55296
v_lshlrev_b32 v168, 16, v52
mqa 72,240,156
tq 238,4,57344
buffer_load_dwordx4 v31, s[20:23], s54 offen offset:1024 lds
mqa 76,244,156
tq 240,5,34816
s_add_u32 m0, s56, 83968
mqa 68,244,152
tq 242,5,36864
buffer_load_dwordx4 v30, s[20:23], s50 offen offset:0 lds
mqa 80,248,152
tq 244,5,38912
v_and_b32 v169, 0xffff0000, v52
mqa 88,248,156
tq 246,5,40960
v_lshlrev_b32 v170, 16, v53
mqa 92,252,156
tq 248,5,51200
mqa 84,252,152
tq 250,5,53248
buffer_load_dwordx4 v31, s[20:23], s50 offen offset:1024 lds
s_waitcnt lgkmcnt(6)
mqa 64,224,176
tq 252,5,55296
s_add_u32 m0, s56, 2048
mqa 72,224,180
tq 254,5,57344
buffer_load_dwordx4 v30, s[20:23], s52 offen offset:0 lds
mqa 72,228,188
tq 224,6,34816
mqa 64,228,184
tq 226,6,36864
buffer_load_dwordx4 v31, s[20:23], s52 offen offset:1024 lds
mqa 64,232,208
tq 228,6,38912
v_and_b32 v171, 0xffff0000, v53
mqa 72,232,212
tq 230,6,40960
v_lshlrev_b32 v172, 16, v54
mqa 72,236,220
tq 232,6,51200
s_add_u32 m0, s56, 92160
mqa 64,236,216
tq 234,6,53248
buffer_load_dwordx4 v30, s[20:23], s49 offen offset:0 lds
s_waitcnt lgkmcnt(6)
mqa 68,240,176
tq 236,6,55296
v_and_b32 v173, 0xffff0000, v54
mqa 76,240,180
tq 238,6,57344
v_lshlrev_b32 v174, 16, v55
mqa 76,244,188
tq 240,7,34816
v_fma_f32 v200, v136, v72, v136
v_fma_f32 v201, v137, v73, v137
mqa 68,244,184
tq 242,7,36864
v_fma_f32 v202, v138, v74, v138
mqa 68,248,208
tq 244,7,38912
v_fma_f32 v203, v139, v75, v139
v_fma_f32 v192, v128, v64, v128
mqa 76,248,212
tq 246,7,40960
v_fma_f32 v193, v129, v65, v129
mqa 76,252,220
tq 248,7,51200
v_fma_f32 v194, v130, v66, v130
mqa 68,252,216
tq 250,7,53248
buffer_load_dwordx4 v31, s[20:23], s49 offen offset:1024 lds
L_hop2_0_%=:
s_cbranch_execz L_hop6_0_%=
s_waitcnt lgkmcnt(6)
mqa 80,224,176
tq 252,7,55296
tq 254,7,57344
v_fma_f32 v195, v131, v67, v131
mqa 88,224,180
s_waitcnt vmcnt(8)
s_barrier
mqa 88,228,188
tq 224,8,10240
s_add_u32 m0, s56, 10240
v_cvt_pk_bf16_f32 v36, v200, v201
mqa 80,228,184
tq 226,8,12288
buffer_load_dwordx4 v30, s[20:23], s43 offen offset:0 lds
v_cvt_pk_bf16_f32 v37, v202, v203
mqa 80,232,208
tq 228,9,10240
v_cvt_pk_bf16_f32 v32, v192, v193
v_cvt_pk_bf16_f32 v33, v194, v195
mqa 88,232,212
tq 230,9,12288
v_fma_f32 v204, v140, v76, v140
v_fma_f32 v205, v141, v77, v141
mqa 88,236,220
tq 232,12,10240
v_fma_f32 v206, v142, v78, v142
v_fma_f32 v207, v143, v79, v143
mqa 80,236,216
tq 234,12,12288
buffer_load_dwordx4 v31, s[20:23], s43 offen offset:1024 lds
s_waitcnt lgkmcnt(6)
mqa 84,240,176
tq 236,13,10240
v_fma_f32 v196, v132, v68, v132
v_fma_f32 v197, v133, v69, v133
v_fma_f32 v198, v134, v70, v134
mqa 92,240,180
tq 238,13,12288
v_fma_f32 v199, v135, v71, v135
v_cndmask_b32 v22, v22, v23, s[60:61]
mqa 92,244,188
tq 240,8,14336
s_add_u32 m0, s56, 100352
v_cvt_pk_bf16_f32 v38, v204, v205
mqa 84,244,184
tq 242,8,16384
buffer_load_dwordx4 v10, s[72:75], s40 offen offset:0 lds
v_cvt_pk_bf16_f32 v39, v206, v207
mqa 84,248,208
tq 244,9,14336
mqa 92,248,212
tq 246,9,16384
buffer_store_dwordx4 v[36:39], v14, s[28:31], s80 offen offset:1024 sc1
v_cvt_pk_bf16_f32 v34, v196, v197
v_cvt_pk_bf16_f32 v35, v198, v199
buffer_store_dwordx4 v[32:35], v14, s[28:31], s80 offen offset:0 sc1
mqa 92,252,220
tq 248,12,14336
v_fma_f32 v216, v152, v88, v152
v_fma_f32 v217, v153, v89, v153
mqa 84,252,216
tq 250,12,16384
buffer_load_dwordx4 v11, s[72:75], s40 offen offset:1024 lds
s_waitcnt lgkmcnt(6)
mqa 96,224,32
tq 252,13,14336
v_fma_f32 v218, v154, v90, v154
v_fma_f32 v219, v155, v91, v155
mqa 104,224,36
tq 254,13,16384
v_fma_f32 v208, v144, v80, v144
v_fma_f32 v209, v145, v81, v145
mqa 108,228,36
tq 224,4,59392
s_add_u32 m0, s56, 18432
v_fma_f32 v210, v146, v82, v146
mqa 100,228,32
tq 226,4,61440
buffer_load_dwordx4 v30, s[20:23], s45 offen offset:0 lds
v_fma_f32 v211, v147, v83, v147
mqa 112,232,32
tq 228,5,59392
v_cvt_pk_bf16_f32 v44, v216, v217
v_cvt_pk_bf16_f32 v45, v218, v219
mqa 120,232,36
tq 230,5,61440
v_cvt_pk_bf16_f32 v40, v208, v209
v_cvt_pk_bf16_f32 v41, v210, v211
mqa 124,236,36
tq 232,6,59392
v_fma_f32 v220, v156, v92, v156
v_fma_f32 v221, v157, v93, v157
mqa 116,236,32
tq 234,6,61440
buffer_load_dwordx4 v31, s[20:23], s45 offen offset:1024 lds
s_waitcnt lgkmcnt(6)
mqa 96,240,40
tq 236,7,59392
v_fma_f32 v222, v158, v94, v158
v_fma_f32 v223, v159, v95, v159
mqa 104,240,44
tq 238,7,61440
v_fma_f32 v212, v148, v84, v148
v_fma_f32 v213, v149, v85, v149
mqa 108,244,44
tq 240,4,63488
s_add_u32 m0, s56, 43008
v_fma_f32 v214, v150, v86, v150
mqa 100,244,40
tq 242,8,0
buffer_load_dwordx4 v30, s[20:23], s47 offen offset:0 lds
v_fma_f32 v215, v151, v87, v151
mqa 112,248,40
tq 244,5,63488
v_cvt_pk_bf16_f32 v46, v220, v221
v_cvt_pk_bf16_f32 v47, v222, v223
mqa 120,248,44
tq 246,9,0
buffer_store_dwordx4 v[44:47], v14, s[28:31], s80 offen offset:3072 sc1
v_cvt_pk_bf16_f32 v42, v212, v213
mqa 124,252,44
tq 248,6,63488
v_cvt_pk_bf16_f32 v43, v214, v215
mqa 116,252,40
tq 250,12,0
buffer_load_dwordx4 v31, s[20:23], s47 offen offset:1024 lds
buffer_store_dwordx4 v[40:43], v14, s[28:31], s80 offen offset:2048 sc1
s_waitcnt lgkmcnt(6)
mqa 96,224,96
tq 252,7,63488
tq 254,13,0
mqa 104,224,100
s_waitcnt vmcnt(14)
s_barrier
mqa 108,228,100
tq 224,8,18432
s_add_u32 m0, s56, 26624
mqa 100,228,96
tq 226,8,20480
buffer_load_dwordx4 v10, s[72:75], s42 offen offset:0 lds
mqa 112,232,96
tq 228,9,18432
v_and_b32 v175, 0xffff0000, v55
mqa 120,232,100
tq 230,9,20480
v_lshlrev_b32 v176, 16, v56
mqa 124,236,100
tq 232,12,18432
mqa 116,236,96
tq 234,12,20480
buffer_load_dwordx4 v11, s[72:75], s42 offen offset:1024 lds
s_waitcnt lgkmcnt(6)
mqa 96,240,104
tq 236,13,18432
s_add_u32 m0, s56, 108544
mqa 104,240,108
tq 238,13,20480
buffer_load_dwordx4 v30, s[20:23], s51 offen offset:0 lds
mqa 108,244,108
tq 240,8,22528
v_and_b32 v177, 0xffff0000, v56
mqa 100,244,104
tq 242,8,24576
buffer_load_dwordx4 v31, s[20:23], s51 offen offset:1024 lds
mqa 112,248,104
tq 244,9,22528
v_lshlrev_b32 v178, 16, v57
mqa 120,248,108
tq 246,9,24576
v_and_b32 v179, 0xffff0000, v57
mqa 124,252,108
tq 248,12,22528
s_add_u32 m0, s56, 116736
mqa 116,252,104
tq 250,12,24576
buffer_load_dwordx4 v30, s[20:23], s53 offen offset:0 lds
s_waitcnt lgkmcnt(6)
mqa 96,224,128
tq 252,13,22528
v_lshlrev_b32 v180, 16, v58
mqa 104,224,132
)ASM"
R"ASM(
tq 254,13,24576
v_and_b32 v181, 0xffff0000, v58
mqa 108,228,132
tq 224,4,2048
v_lshlrev_b32 v182, 16, v59
mqa 100,228,128
tq 226,4,4096
buffer_load_dwordx4 v31, s[20:23], s53 offen offset:1024 lds
mqa 112,232,128
tq 228,5,2048
v_and_b32 v183, 0xffff0000, v59
mqa 120,232,132
tq 230,5,4096
v_lshlrev_b32 v184, 16, v60
mqa 124,236,132
tq 232,6,2048
s_add_u32 m0, s56, 124928
mqa 116,236,128
tq 234,6,4096
buffer_load_dwordx4 v10, s[72:75], s48 offen offset:0 lds
s_waitcnt lgkmcnt(6)
mqa 96,240,136
tq 236,7,2048
v_and_b32 v185, 0xffff0000, v60
mqa 104,240,140
tq 238,7,4096
v_lshlrev_b32 v186, 16, v61
mqa 108,244,140
tq 240,4,6144
v_and_b32 v187, 0xffff0000, v61
mqa 100,244,136
tq 242,4,8192
buffer_load_dwordx4 v11, s[72:75], s48 offen offset:1024 lds
mqa 112,248,136
tq 244,5,6144
v_lshlrev_b32 v188, 16, v62
mqa 120,248,140
tq 246,5,8192
v_and_b32 v189, 0xffff0000, v62
mqa 124,252,140
tq 248,6,6144
s_add_u32 m0, s56, 133120
mqa 116,252,136
tq 250,6,8192
buffer_load_dwordx4 v30, s[20:23], s55 offen offset:0 lds
s_waitcnt lgkmcnt(6)
mqa 96,224,160
tq 252,7,6144
tq 254,7,8192
mqa 104,224,164
s_waitcnt vmcnt(19)
s_barrier
mqa 108,228,164
tq 224,8,2048
v_lshlrev_b32 v190, 16, v63
mqa 100,228,160
tq 226,8,4096
buffer_load_dwordx4 v31, s[20:23], s55 offen offset:1024 lds
mqa 112,232,160
tq 228,9,2048
v_and_b32 v191, 0xffff0000, v63
mqa 120,232,164
tq 230,9,4096
ds_read_b128 v[64:67], v24 offset:512
mqa 124,236,164
tq 232,12,2048
s_add_u32 m0, s56, 141312
mqa 116,236,160
tq 234,12,4096
buffer_load_dwordx4 v30, s[72:75], s40 offen offset:0 lds
s_waitcnt lgkmcnt(7)
mqa 96,240,168
tq 236,13,2048
ds_read_b128 v[72:75], v24 offset:512
mqa 104,240,172
tq 238,13,4096
ds_read_b128 v[68:71], v24 offset:576
mqa 108,244,172
tq 240,8,6144
ds_read_b128 v[76:79], v24 offset:576
mqa 100,244,168
tq 242,8,8192
buffer_load_dwordx4 v31, s[72:75], s40 offen offset:1024 lds
mqa 112,248,168
tq 244,9,6144
ds_read_b128 v[80:83], v24 offset:640
mqa 120,248,172
tq 246,9,8192
ds_read_b128 v[88:91], v24 offset:640
mqa 124,252,172
tq 248,12,6144
mqa 116,252,168
tq 250,12,8192
s_waitcnt vmcnt(12)
buffer_store_dword v22, v21, s[28:31], 0 offen offset:0 sc1
s_waitcnt lgkmcnt(10)
mqa 96,224,192
tq 252,13,6144
s_add_u32 m0, s56, 149504
mqa 104,224,196
tq 254,13,8192
buffer_load_dwordx4 v30, s[72:75], s46 offen offset:0 lds
mqa 108,228,196
tq 224,4,10240
ds_read_b128 v[84:87], v24 offset:704
mqa 100,228,192
tq 226,4,12288
buffer_load_dwordx4 v31, s[72:75], s46 offen offset:1024 lds
mqa 112,232,192
tq 228,5,10240
ds_read_b128 v[92:95], v24 offset:704
mqa 120,232,196
tq 230,5,12288
mqa 124,236,196
tq 232,6,10240
s_add_u32 m0, s56, 34816
mqa 116,236,192
tq 234,6,12288
buffer_load_dwordx4 v10, s[72:75], s41 offen offset:0 lds
s_waitcnt lgkmcnt(8)
mqa 96,240,200
tq 236,7,10240
mqa 104,240,204
tq 238,7,12288
mqa 108,244,204
tq 240,4,14336
mqa 100,244,200
tq 242,4,16384
buffer_load_dwordx4 v11, s[72:75], s41 offen offset:1024 lds
mqa 112,248,200
tq 244,5,14336
mqa 120,248,204
tq 246,5,16384
mqa 124,252,204
tq 248,6,14336
s_add_u32 m0, s56, 51200
mqa 116,252,200
tq 250,6,16384
buffer_load_dwordx4 v30, s[72:75], s41 offen offset:0 lds
s_waitcnt lgkmcnt(6)
mqa 96,224,16
tq 252,7,14336
tq 254,7,16384
mqa 104,224,20
s_barrier
mqa 108,228,20
tq 224,4,18432
mqa 100,228,16
tq 226,4,20480
buffer_load_dwordx4 v31, s[72:75], s41 offen offset:1024 lds
mqa 112,232,16
tq 228,5,18432
mqa 120,232,20
tq 230,5,20480
s_lshr_b32 s82, s42, 4
mqa 124,236,20
tq 232,6,18432
s_add_u32 m0, s56, 75776
mqa 116,236,16
tq 234,6,20480
buffer_load_dwordx4 v10, s[72:75], s44 offen offset:0 lds
s_waitcnt lgkmcnt(6)
mqa 96,240,24
tq 236,7,18432
mqa 104,240,28
tq 238,7,20480
s_lshr_b32 s83, s43, 4
mqa 108,244,28
tq 240,4,22528
mqa 100,244,24
tq 242,4,24576
buffer_load_dwordx4 v11, s[72:75], s44 offen offset:1024 lds
mqa 112,248,24
tq 244,5,22528
mqa 120,248,28
tq 246,5,24576
s_lshr_b32 s84, s44, 4
mqa 124,252,28
tq 248,6,22528
s_add_u32 m0, s56, 59392
mqa 116,252,24
tq 250,6,24576
buffer_load_dwordx4 v10, s[72:75], s47 offen offset:0 lds
s_waitcnt lgkmcnt(6)
mqa 96,224,48
tq 252,7,22528
mqa 104,224,52
tq 254,7,24576
s_lshr_b32 s85, s45, 4
mqa 108,228,52
tq 224,4,43008
mqa 100,228,48
tq 226,4,45056
buffer_load_dwordx4 v11, s[72:75], s47 offen offset:1024 lds
mqa 112,232,48
tq 228,5,43008
mqa 120,232,52
tq 230,5,45056
s_lshr_b32 s86, s46, 4
mqa 124,236,52
tq 232,6,43008
s_add_u32 m0, s56, 83968
mqa 116,236,48
tq 234,6,45056
buffer_load_dwordx4 v30, s[72:75], s42 offen offset:0 lds
s_waitcnt lgkmcnt(6)
mqa 96,240,56
tq 236,7,43008
mqa 104,240,60
tq 238,7,45056
s_lshr_b32 s87, s47, 4
mqa 108,244,60
tq 240,4,47104
mqa 100,244,56
tq 242,4,49152
buffer_load_dwordx4 v31, s[72:75], s42 offen offset:1024 lds
mqa 112,248,56
tq 244,5,47104
mqa 120,248,60
tq 246,5,49152
s_lshr_b32 s88, s48, 4
mqa 124,252,60
tq 248,6,47104
s_add_u32 m0, s56, 2048
mqa 116,252,56
tq 250,6,49152
buffer_load_dwordx4 v10, s[72:75], s46 offen offset:0 lds
s_waitcnt lgkmcnt(6)
mqa 96,224,80
tq 252,7,47104
tq 254,7,49152
mqa 104,224,84
s_waitcnt vmcnt(22)
s_barrier
mqa 108,228,84
tq 224,8,26624
mqa 100,228,80
tq 226,8,28672
buffer_load_dwordx4 v11, s[72:75], s46 offen offset:1024 lds
mqa 112,232,80
tq 228,9,26624
mqa 120,232,84
tq 230,9,28672
s_lshr_b32 s89, s49, 4
mqa 124,236,84
tq 232,12,26624
s_add_u32 m0, s56, 67584
mqa 116,236,80
tq 234,12,28672
buffer_load_dwordx4 v10, s[72:75], s55 offen offset:0 lds
s_waitcnt lgkmcnt(6)
mqa 96,240,88
tq 236,13,26624
mqa 104,240,92
tq 238,13,28672
s_lshr_b32 s90, s50, 4
mqa 108,244,92
tq 240,8,30720
mqa 100,244,88
tq 242,8,32768
buffer_load_dwordx4 v11, s[72:75], s55 offen offset:1024 lds
mqa 112,248,88
tq 244,9,30720
mqa 120,248,92
tq 246,9,32768
s_lshr_b32 s91, s51, 4
mqa 124,252,92
tq 248,12,30720
mqa 116,252,88
tq 250,12,32768
s_lshr_b32 s92, s52, 4
s_waitcnt lgkmcnt(6)
mqa 96,224,112
tq 252,13,30720
s_add_u32 m0, s56, 10240
mqa 104,224,116
tq 254,13,32768
buffer_load_dwordx4 v30, s[72:75], s44 offen offset:0 lds
mqa 108,228,116
tq 224,8,43008
mqa 100,228,112
tq 226,8,45056
buffer_load_dwordx4 v31, s[72:75], s44 offen offset:1024 lds
mqa 112,232,112
tq 228,9,43008
mqa 120,232,116
tq 230,9,45056
s_lshr_b32 s93, s53, 4
mqa 124,236,116
tq 232,12,43008
mqa 116,236,112
tq 234,12,45056
s_lshr_b32 s94, s54, 4
s_waitcnt lgkmcnt(6)
mqa 96,240,120
tq 236,13,43008
mqa 104,240,124
tq 238,13,45056
s_lshr_b32 s95, s55, 4
mqa 108,244,124
tq 240,8,47104
mqa 100,244,120
tq 242,8,49152
mqa 112,248,120
tq 244,9,47104
mqa 120,248,124
tq 246,9,49152
mqa 124,252,124
tq 248,12,47104
mqa 116,252,120
tq 250,12,49152
s_waitcnt lgkmcnt(6)
mqa 96,224,144
tq 252,13,47104
tq 254,13,49152
s_add_u32 m0, s56, 18432
mqa 104,224,148
buffer_load_dwordx4 v10, s[72:75], s50 offen offset:0 lds
s_waitcnt vmcnt(22)
s_barrier
mqa 108,228,148
tq 224,8,51200
mqa 100,228,144
tq 226,8,53248
buffer_load_dwordx4 v11, s[72:75], s50 offen offset:1024 lds
mqa 112,232,144
tq 228,8,55296
mqa 120,232,148
tq 230,8,57344
mqa 124,236,148
tq 232,16,2048
mqa 116,236,144
tq 234,16,4096
s_waitcnt lgkmcnt(6)
mqa 96,240,152
tq 236,16,6144
mqa 104,240,156
tq 238,16,8192
mqa 108,244,156
tq 240,9,51200
mqa 100,244,152
tq 242,9,53248
mqa 112,248,152
tq 244,9,55296
mqa 120,248,156
tq 246,9,57344
mqa 124,252,156
tq 248,17,2048
mqa 116,252,152
tq 250,17,4096
s_waitcnt lgkmcnt(6)
mqa 96,224,176
tq 252,17,6144
mqa 104,224,180
tq 254,17,8192
buffer_load_dword v23, v20, s[28:31], 0 offen offset:0 sc1
s_add_u32 m0, s56, 43008
s_nop 0
s_nop 0
L_hop3_0_%=:
s_cbranch_execz L_hop7_0_%=
buffer_load_dwordx4 v30, s[72:75], s54 offen offset:0 lds
mqa 104,228,188
tq 224,12,51200
mqa 96,228,184
tq 226,12,53248
buffer_load_dwordx4 v31, s[72:75], s54 offen offset:1024 lds
mqa 96,232,208
tq 228,12,55296
s_add_u32 m0, s56, 92160
mqa 104,232,212
tq 230,12,57344
buffer_load_dwordx4 v10, s[72:75], s52 offen offset:0 lds
mqa 104,236,220
tq 232,28,2048
mqa 96,236,216
tq 234,28,4096
s_waitcnt lgkmcnt(6)
mqa 100,240,176
tq 236,28,6144
mqa 108,240,180
tq 238,28,8192
buffer_load_dwordx4 v11, s[72:75], s52 offen offset:1024 lds
mqa 108,244,188
tq 240,13,51200
v_fma_f32 v232, v168, v104, v168
v_fma_f32 v233, v169, v105, v169
mqa 100,244,184
tq 242,13,53248
v_fma_f32 v234, v170, v106, v170
mqa 100,248,208
tq 244,13,55296
v_fma_f32 v235, v171, v107, v171
v_fma_f32 v224, v160, v96, v160
mqa 108,248,212
tq 246,13,57344
v_fma_f32 v225, v161, v97, v161
mqa 108,252,220
tq 248,29,2048
v_fma_f32 v226, v162, v98, v162
mqa 100,252,216
tq 250,29,4096
s_waitcnt lgkmcnt(6)
mqa 112,224,176
tq 252,29,6144
tq 254,29,8192
mqa 120,224,180
s_waitcnt vmcnt(26)
s_barrier
mqa 120,228,188
tq 224,8,34816
v_fma_f32 v227, v163, v99, v163
v_cvt_pk_bf16_f32 v52, v232, v233
mqa 112,228,184
tq 226,8,36864
v_cvt_pk_bf16_f32 v53, v234, v235
mqa 112,232,208
tq 228,9,34816
v_cvt_pk_bf16_f32 v48, v224, v225
v_cvt_pk_bf16_f32 v49, v226, v227
mqa 120,232,212
tq 230,9,36864
v_fma_f32 v236, v172, v108, v172
mqa 120,236,220
tq 232,8,38912
v_fma_f32 v237, v173, v109, v173
v_fma_f32 v238, v174, v110, v174
mqa 112,236,216
tq 234,8,40960
s_waitcnt lgkmcnt(6)
mqa 116,240,176
tq 236,9,38912
v_fma_f32 v239, v175, v111, v175
v_fma_f32 v228, v164, v100, v164
mqa 124,240,180
tq 238,9,40960
v_fma_f32 v229, v165, v101, v165
mqa 124,244,188
tq 240,12,34816
v_fma_f32 v230, v166, v102, v166
v_fma_f32 v231, v167, v103, v167
mqa 116,244,184
tq 242,12,36864
v_cvt_pk_bf16_f32 v54, v236, v237
v_cvt_pk_bf16_f32 v55, v238, v239
mqa 116,248,208
tq 244,13,34816
mqa 124,248,212
tq 246,13,36864
buffer_store_dwordx4 v[52:55], v14, s[28:31], s81 offen offset:1024 sc1
v_cvt_pk_bf16_f32 v50, v228, v229
v_cvt_pk_bf16_f32 v51, v230, v231
buffer_store_dwordx4 v[48:51], v14, s[28:31], s81 offen offset:0 sc1
mqa 124,252,220
tq 248,12,38912
ds_read_b128 v[96:99], v24 offset:768
v_fma_f32 v248, v184, v120, v184
v_fma_f32 v249, v185, v121, v185
mqa 116,252,216
tq 250,12,40960
v_fma_f32 v250, v186, v122, v186
s_waitcnt lgkmcnt(7)
mqv 64,224,32
tq 252,13,38912
v_fma_f32 v251, v187, v123, v187
mqv 72,224,36
tq 254,13,40960
s_waitcnt vmcnt(6)
v_cmp_ne_u32 vcc, v23, v22
s_cbranch_vccnz L_slow0_0_%=
L_back0_0_%=:
buffer_load_dwordx4 a[0:3], v14, s[28:31], s82 offen offset:0 sc1
mqv 76,228,36
tq 224,16,10240
ds_read_b128 v[104:107], v24 offset:768
v_fma_f32 v240, v176, v112, v176
mqv 68,228,32
tq 226,16,12288
v_fma_f32 v241, v177, v113, v177
v_fma_f32 v242, v178, v114, v178
mqv 64,232,40
tq 228,17,10240
v_fma_f32 v243, v179, v115, v179
v_cvt_pk_bf16_f32 v60, v248, v249
v_cvt_pk_bf16_f32 v61, v250, v251
mqv 72,232,44
tq 230,17,12288
buffer_load_dwordx4 a[4:7], v14, s[28:31], s82 offen offset:1024 sc1
ds_read_b128 v[100:103], v24 offset:832
mqv 76,236,44
tq 232,16,14336
v_cvt_pk_bf16_f32 v56, v240, v241
v_cvt_pk_bf16_f32 v57, v242, v243
ds_read_b128 v[108:111], v24 offset:832
mqv 68,236,40
tq 234,16,16384
v_fma_f32 v252, v188, v124, v188
s_waitcnt lgkmcnt(9)
mqv 80,240,32
tq 236,17,14336
v_fma_f32 v253, v189, v125, v189
v_fma_f32 v254, v190, v126, v190
v_fma_f32 v255, v191, v127, v191
mqv 88,240,36
tq 238,17,16384
buffer_load_dwordx4 a[8:11], v14, s[28:31], s82 offen offset:2048 sc1
ds_read_b128 v[112:115], v24 offset:896
mqv 92,244,36
tq 240,28,10240
v_fma_f32 v244, v180, v116, v180
v_fma_f32 v245, v181, v117, v181
mqv 84,244,32
tq 242,28,12288
v_fma_f32 v246, v182, v118, v182
v_fma_f32 v247, v183, v119, v183
mqv 80,248,40
tq 244,29,10240
v_cvt_pk_bf16_f32 v62, v252, v253
v_cvt_pk_bf16_f32 v63, v254, v255
mqv 88,248,44
tq 246,29,12288
buffer_load_dwordx4 a[12:15], v14, s[28:31], s82 offen offset:3072 sc1
buffer_store_dwordx4 v[60:63], v14, s[28:31], s81 offen offset:3072 sc1
ds_read_b128 v[120:123], v24 offset:896
mqv 92,252,44
tq 248,28,14336
v_cvt_pk_bf16_f32 v58, v244, v245
v_cvt_pk_bf16_f32 v59, v246, v247
mqv 84,252,40
tq 250,28,16384
buffer_store_dwordx4 v[56:59], v14, s[28:31], s81 offen offset:2048 sc1
ds_read_b128 v[116:119], v24 offset:960
s_waitcnt lgkmcnt(9)
mqv 96,224,32
tq 252,29,14336
tq 254,29,16384
ds_read_b128 v[124:127], v24 offset:960
mqv 104,224,36
buffer_load_dwordx4 a[32:35], v14, s[28:31], s84 offen offset:0 sc1
s_barrier
mqv 108,228,36
tq 224,4,34816
mqv 100,228,32
tq 226,4,36864
buffer_load_dwordx4 a[36:39], v14, s[28:31], s84 offen offset:1024 sc1
mqv 96,232,40
tq 228,5,34816
mqv 104,232,44
tq 230,5,36864
buffer_load_dwordx4 a[40:43], v14, s[28:31], s84 offen offset:2048 sc1
mqv 108,236,44
tq 232,6,34816
mqv 100,236,40
tq 234,6,36864
s_waitcnt lgkmcnt(6)
mqv 112,240,32
tq 236,7,34816
mqv 120,240,36
tq 238,7,36864
buffer_load_dwordx4 a[44:47], v14, s[28:31], s84 offen offset:3072 sc1
mqv 124,244,36
tq 240,4,38912
mqv 116,244,32
tq 242,4,40960
mqv 112,248,40
tq 244,5,38912
mqv 120,248,44
tq 246,5,40960
buffer_load_dwordx4 a[64:67], v14, s[28:31], s86 offen offset:0 sc1
mqv 124,252,44
tq 248,6,38912
mqv 116,252,40
tq 250,6,40960
s_waitcnt lgkmcnt(6)
mqv 64,224,48
tq 252,7,38912
mqv 72,224,52
tq 254,7,40960
buffer_load_dwordx4 a[68:71], v14, s[28:31], s86 offen offset:1024 sc1
mqv 76,228,52
tq 224,4,51200
mqv 68,228,48
tq 226,4,53248
mqv 80,232,48
tq 228,5,51200
mqv 88,232,52
tq 230,5,53248
buffer_load_dwordx4 a[72:75], v14, s[28:31], s86 offen offset:2048 sc1
mqv 92,236,52
tq 232,6,51200
mqv 84,236,48
tq 234,6,53248
s_waitcnt lgkmcnt(6)
mqv 64,240,56
tq 236,7,51200
mqv 72,240,60
tq 238,7,53248
buffer_load_dwordx4 a[76:79], v14, s[28:31], s86 offen offset:3072 sc1
mqv 76,244,60
tq 240,4,55296
mqv 68,244,56
tq 242,4,57344
mqv 80,248,56
tq 244,5,55296
s_add_u32 m0, s56, 108544
mqv 88,248,60
tq 246,5,57344
buffer_load_dwordx4 v30, s[72:75], s49 offen offset:0 lds
mqv 92,252,60
tq 248,6,55296
mqv 84,252,56
tq 250,6,57344
s_waitcnt lgkmcnt(6)
mqv 96,224,48
tq 252,7,55296
tq 254,7,57344
mqv 104,224,52
s_barrier
mqv 108,228,52
tq 224,4,26624
mqv 100,228,48
tq 226,4,28672
buffer_load_dwordx4 v31, s[72:75], s49 offen offset:1024 lds
mqv 112,232,48
tq 228,5,26624
mqv 120,232,52
tq 230,5,28672
buffer_load_dwordx4 a[96:99], v14, s[28:31], s88 offen offset:0 sc1
mqv 124,236,52
tq 232,6,26624
mqv 116,236,48
tq 234,6,28672
s_waitcnt lgkmcnt(6)
mqv 96,240,56
tq 236,7,26624
mqv 104,240,60
tq 238,7,28672
buffer_load_dwordx4 a[100:103], v14, s[28:31], s88 offen offset:1024 sc1
mqv 108,244,60
tq 240,4,30720
mqv 100,244,56
tq 242,4,32768
mqv 112,248,56
tq 244,5,30720
mqv 120,248,60
tq 246,5,32768
buffer_load_dwordx4 a[104:107], v14, s[28:31], s88 offen offset:2048 sc1
mqv 124,252,60
tq 248,6,30720
mqv 116,252,56
tq 250,6,32768
s_waitcnt vmcnt(13)
buffer_store_dword v22, v21, s[28:31], 0 offen offset:32 sc1
s_waitcnt lgkmcnt(6)
mqa 64,224,0
tq 252,7,30720
mqa 72,224,4
tq 254,7,32768
buffer_load_dwordx4 a[108:111], v14, s[28:31], s88 offen offset:3072 sc1
mqa 76,228,4
tq 224,8,10240
mqa 68,228,0
tq 226,8,12288
mqa 80,232,0
tq 228,9,10240
mqa 88,232,4
tq 230,9,12288
buffer_load_dwordx4 a[128:131], v14, s[28:31], s90 offen offset:0 sc1
mqa 92,236,4
tq 232,12,10240
mqa 84,236,0
tq 234,12,12288
s_waitcnt lgkmcnt(6)
mqa 64,240,8
tq 236,13,10240
mqa 72,240,12
tq 238,13,12288
buffer_load_dwordx4 a[132:135], v14, s[28:31], s90 offen offset:1024 sc1
mqa 76,244,12
tq 240,8,14336
mqa 68,244,8
tq 242,8,16384
mqa 80,248,8
tq 244,9,14336
mqa 88,248,12
tq 246,9,16384
buffer_load_dwordx4 a[136:139], v14, s[28:31], s90 offen offset:2048 sc1
mqa 92,252,12
tq 248,12,14336
mqa 84,252,8
tq 250,12,16384
s_waitcnt lgkmcnt(6)
s_waitcnt vmcnt(14)
mqa 64,224,32
tq 252,13,14336
tq 254,13,16384
mqa 72,224,36
s_barrier
mqa 76,228,36
tq 224,8,18432
mqa 68,228,32
tq 226,8,20480
buffer_load_dwordx4 a[140:143], v14, s[28:31], s90 offen offset:3072 sc1
mqa 80,232,32
tq 228,9,18432
s_add_u32 m0, s56, 100352
mqa 88,232,36
tq 230,9,20480
buffer_load_dwordx4 v10, s[72:75], s54 offen offset:0 lds
mqa 92,236,36
tq 232,12,18432
mqa 84,236,32
tq 234,12,20480
s_waitcnt lgkmcnt(6)
mqa 64,240,40
tq 236,13,18432
mqa 72,240,44
tq 238,13,20480
buffer_load_dwordx4 v11, s[72:75], s54 offen offset:1024 lds
mqa 76,244,44
tq 240,8,22528
mqa 68,244,40
tq 242,8,24576
mqa 80,248,40
tq 244,9,22528
mqa 88,248,44
tq 246,9,24576
buffer_load_dwordx4 a[160:163], v14, s[28:31], s92 offen offset:0 sc1
mqa 92,252,44
tq 248,12,22528
mqa 84,252,40
tq 250,12,24576
s_waitcnt lgkmcnt(6)
mqa 96,224,0
tq 252,13,22528
mqa 104,224,4
tq 254,13,24576
buffer_load_dwordx4 a[164:167], v14, s[28:31], s92 offen offset:1024 sc1
mqa 108,228,4
tq 224,4,2048
mqa 100,228,0
tq 226,4,4096
mqa 112,232,0
tq 228,5,2048
mqa 120,232,4
tq 230,5,4096
buffer_load_dwordx4 a[168:171], v14, s[28:31], s92 offen offset:2048 sc1
mqa 124,236,4
tq 232,6,2048
mqa 116,236,0
tq 234,6,4096
s_waitcnt lgkmcnt(6)
mqa 96,240,8
tq 236,7,2048
mqa 104,240,12
tq 238,7,4096
buffer_load_dwordx4 a[172:175], v14, s[28:31], s92 offen offset:3072 sc1
mqa 108,244,12
tq 240,4,6144
mqa 100,244,8
tq 242,4,8192
mqa 112,248,8
tq 244,5,6144
s_add_u32 m0, s56, 116736
mqa 120,248,12
tq 246,5,8192
buffer_load_dwordx4 v10, s[76:79], s40 offen offset:0 lds
mqa 124,252,12
tq 248,6,6144
mqa 116,252,8
tq 250,6,8192
s_waitcnt lgkmcnt(6)
s_waitcnt vmcnt(18)
mqa 64,224,64
tq 252,7,6144
tq 254,7,8192
mqa 72,224,68
s_barrier
mqa 76,228,68
tq 224,8,59392
mqa 68,228,64
tq 226,8,61440
buffer_load_dwordx4 v11, s[76:79], s40 offen offset:1024 lds
mqa 80,232,64
tq 228,9,59392
s_add_u32 m0, s56, 133120
mqa 88,232,68
tq 230,9,61440
buffer_load_dwordx4 v10, s[72:75], s43 offen offset:0 lds
mqa 92,236,68
tq 232,12,59392
mqa 84,236,64
tq 234,12,61440
s_waitcnt lgkmcnt(6)
mqa 64,240,72
tq 236,13,59392
mqa 72,240,76
tq 238,13,61440
buffer_load_dwordx4 v11, s[72:75], s43 offen offset:1024 lds
mqa 76,244,76
tq 240,8,63488
mqa 68,244,72
tq 242,16,0
mqa 80,248,72
tq 244,9,63488
s_add_u32 m0, s56, 34816
mqa 88,248,76
tq 246,17,0
buffer_load_dwordx4 v10, s[72:75], s45 offen offset:0 lds
mqa 92,252,76
tq 248,12,63488
mqa 84,252,72
tq 250,28,0
s_waitcnt lgkmcnt(6)
s_waitcnt vmcnt(15)
mqa 64,224,96
tq 252,13,63488
mqa 72,224,100
tq 254,29,0
buffer_load_dwordx4 v11, s[72:75], s45 offen offset:1024 lds
mqa 76,228,100
tq 224,4,10240
mqa 68,228,96
tq 226,4,12288
mqa 80,232,96
tq 228,5,10240
mqa 88,232,100
tq 230,5,12288
buffer_load_dwordx4 a[192:195], v14, s[28:31], s94 offen offset:0 sc1
mqa 92,236,100
tq 232,6,10240
mqa 84,236,96
tq 234,6,12288
s_waitcnt lgkmcnt(6)
mqa 64,240,104
tq 236,7,10240
mqa 72,240,108
tq 238,7,12288
buffer_load_dwordx4 a[196:199], v14, s[28:31], s94 offen offset:1024 sc1
mqa 76,244,108
tq 240,4,14336
mqa 68,244,104
tq 242,4,16384
mqa 80,248,104
tq 244,5,14336
mqa 88,248,108
tq 246,5,16384
buffer_load_dwordx4 a[200:203], v14, s[28:31], s94 offen offset:2048 sc1
mqa 92,252,108
tq 248,6,14336
mqa 84,252,104
tq 250,6,16384
s_waitcnt lgkmcnt(6)
mqa 96,224,32
tq 252,7,14336
tq 254,7,16384
mqa 104,224,36
buffer_load_dword v23, v20, s[28:31], 0 offen offset:32 sc1
s_barrier
mqa 108,228,36
tq 224,4,18432
mqa 100,228,32
tq 226,4,20480
buffer_load_dwordx4 a[204:207], v14, s[28:31], s94 offen offset:3072 sc1
mqa 112,232,32
tq 228,5,18432
s_add_u32 m0, s56, 141312
mqa 120,232,36
tq 230,5,20480
buffer_load_dwordx4 v10, s[72:75], s49 offen offset:0 lds
mqa 124,236,36
tq 232,6,18432
mqa 116,236,32
tq 234,6,20480
s_waitcnt lgkmcnt(6)
mqa 96,240,40
tq 236,7,18432
mqa 104,240,44
tq 238,7,20480
buffer_load_dwordx4 v11, s[72:75], s49 offen offset:1024 lds
mqa 108,244,44
tq 240,4,22528
mqa 100,244,40
tq 242,4,24576
mqa 112,248,40
tq 244,5,22528
s_add_u32 m0, s56, 26624
mqa 120,248,44
tq 246,5,24576
buffer_load_dwordx4 v10, s[72:75], s51 offen offset:0 lds
mqa 124,252,44
tq 248,6,22528
L_hop4_0_%=:
s_cbranch_execz L_hop8_0_%=
mqa 116,252,40
tq 250,6,24576
s_waitcnt lgkmcnt(6)
s_waitcnt vmcnt(20)
mqa 64,224,128
tq 252,7,22528
mqa 72,224,132
tq 254,7,24576
buffer_load_dwordx4 v11, s[72:75], s51 offen offset:1024 lds
mqa 76,228,132
tq 224,8,26624
mqa 68,228,128
tq 226,8,28672
mqa 80,232,128
tq 228,9,26624
s_add_u32 m0, s56, 51200
mqa 88,232,132
tq 230,9,28672
buffer_load_dwordx4 v10, s[76:79], s42 offen offset:0 lds
mqa 92,236,132
tq 232,12,26624
mqa 84,236,128
tq 234,12,28672
s_waitcnt lgkmcnt(6)
mqa 64,240,136
tq 236,13,26624
mqa 72,240,140
tq 238,13,28672
buffer_load_dwordx4 v11, s[76:79], s42 offen offset:1024 lds
mqa 76,244,140
tq 240,8,30720
mqa 68,244,136
tq 242,8,32768
mqa 80,248,136
tq 244,9,30720
s_add_u32 m0, s56, 75776
mqa 88,248,140
tq 246,9,32768
buffer_load_dwordx4 v10, s[72:75], s53 offen offset:0 lds
mqa 92,252,140
tq 248,12,30720
mqa 84,252,136
tq 250,12,32768
s_waitcnt lgkmcnt(6)
s_waitcnt vmcnt(18)
mqa 64,224,160
tq 252,13,30720
tq 254,13,32768
mqa 72,224,164
s_waitcnt vmcnt(8)
v_cmp_ne_u32 vcc, v23, v22
s_cbranch_vccnz L_slow1_0_%=
L_back1_0_%=:
s_barrier
mqa 76,228,164
tq 224,16,18432
mqa 68,228,160
tq 226,16,20480
buffer_load_dwordx4 v11, s[72:75], s53 offen offset:1024 lds
mqa 80,232,160
tq 228,17,18432
mqa 88,232,164
tq 230,17,20480
buffer_load_dwordx4 a[16:19], v14, s[28:31], s83 offen offset:0 sc1
mqa 92,236,164
tq 232,28,18432
mqa 84,236,160
tq 234,28,20480
buffer_load_dwordx4 a[20:23], v14, s[28:31], s83 offen offset:1024 sc1
s_waitcnt lgkmcnt(6)
mqa 64,240,168
tq 236,29,18432
mqa 72,240,172
tq 238,29,20480
buffer_load_dwordx4 a[24:27], v14, s[28:31], s83 offen offset:2048 sc1
mqa 76,244,172
tq 240,16,22528
mqa 68,244,168
tq 242,16,24576
buffer_load_dwordx4 a[28:31], v14, s[28:31], s83 offen offset:3072 sc1
mqa 80,248,168
tq 244,17,22528
mqa 88,248,172
tq 246,17,24576
mqa 92,252,172
tq 248,28,22528
mqa 84,252,168
tq 250,28,24576
buffer_load_dwordx4 a[48:51], v14, s[28:31], s85 offen offset:0 sc1
s_waitcnt lgkmcnt(6)
mqa 96,224,64
tq 252,29,22528
mqa 104,224,68
tq 254,29,24576
buffer_load_dwordx4 a[52:55], v14, s[28:31], s85 offen offset:1024 sc1
mqa 108,228,68
tq 224,8,34816
mqa 100,228,64
tq 226,8,36864
buffer_load_dwordx4 a[56:59], v14, s[28:31], s85 offen offset:2048 sc1
mqa 112,232,64
tq 228,9,34816
mqa 120,232,68
tq 230,9,36864
buffer_load_dwordx4 a[60:63], v14, s[28:31], s85 offen offset:3072 sc1
mqa 124,236,68
tq 232,12,34816
mqa 116,236,64
tq 234,12,36864
s_waitcnt lgkmcnt(6)
mqa 96,240,72
tq 236,13,34816
mqa 104,240,76
tq 238,13,36864
mqa 108,244,76
tq 240,8,38912
mqa 100,244,72
tq 242,8,40960
mqa 112,248,72
tq 244,9,38912
mqa 120,248,76
tq 246,9,40960
mqa 124,252,76
tq 248,12,38912
mqa 116,252,72
tq 250,12,40960
buffer_load_dwordx4 a[80:83], v14, s[28:31], s87 offen offset:0 sc1
s_waitcnt lgkmcnt(6)
s_waitcnt vmcnt(17)
mqa 64,224,192
tq 252,13,38912
tq 254,13,40960
mqa 72,224,196
buffer_load_dwordx4 a[84:87], v14, s[28:31], s87 offen offset:1024 sc1
s_barrier
mqa 76,228,196
tq 224,16,2048
mqa 68,228,192
tq 226,16,4096
buffer_load_dwordx4 a[88:91], v14, s[28:31], s87 offen offset:2048 sc1
mqa 80,232,192
tq 228,17,2048
mqa 88,232,196
tq 230,17,4096
buffer_load_dwordx4 a[92:95], v14, s[28:31], s87 offen offset:3072 sc1
mqa 92,236,196
tq 232,28,2048
mqa 84,236,192
tq 234,28,4096
s_waitcnt lgkmcnt(6)
mqa 64,240,200
tq 236,29,2048
mqa 72,240,204
tq 238,29,4096
mqa 76,244,204
tq 240,16,6144
mqa 68,244,200
tq 242,16,8192
mqa 80,248,200
tq 244,17,6144
mqa 88,248,204
tq 246,17,8192
mqa 92,252,204
tq 248,28,6144
mqa 84,252,200
tq 250,28,8192
buffer_load_dwordx4 a[112:115], v14, s[28:31], s89 offen offset:0 sc1
s_waitcnt lgkmcnt(6)
s_waitcnt vmcnt(9)
mqa 64,224,16
tq 252,29,6144
mqa 72,224,20
tq 254,29,8192
buffer_load_dwordx4 a[116:119], v14, s[28:31], s89 offen offset:1024 sc1
mqa 76,228,20
tq 224,4,34816
mqa 68,228,16
tq 226,4,36864
buffer_load_dwordx4 a[120:123], v14, s[28:31], s89 offen offset:2048 sc1
mqa 80,232,16
tq 228,5,34816
mqa 88,232,20
tq 230,5,36864
buffer_load_dwordx4 a[124:127], v14, s[28:31], s89 offen offset:3072 sc1
mqa 92,236,20
tq 232,6,34816
mqa 84,236,16
tq 234,6,36864
s_waitcnt lgkmcnt(6)
mqa 64,240,24
tq 236,7,34816
mqa 72,240,28
tq 238,7,36864
mqa 76,244,28
tq 240,4,38912
mqa 68,244,24
tq 242,4,40960
mqa 80,248,24
tq 244,5,38912
mqa 88,248,28
tq 246,5,40960
mqa 92,252,28
tq 248,6,38912
mqa 84,252,24
tq 250,6,40960
buffer_load_dwordx4 a[144:147], v14, s[28:31], s91 offen offset:0 sc1
s_waitcnt lgkmcnt(6)
s_waitcnt vmcnt(9)
mqa 64,224,48
tq 252,7,38912
tq 254,7,40960
mqa 72,224,52
buffer_load_dwordx4 a[148:151], v14, s[28:31], s91 offen offset:1024 sc1
s_barrier
mqa 76,228,52
tq 224,4,59392
mqa 68,228,48
tq 226,4,61440
buffer_load_dwordx4 a[152:155], v14, s[28:31], s91 offen offset:2048 sc1
mqa 80,232,48
tq 228,5,59392
mqa 88,232,52
tq 230,5,61440
buffer_load_dwordx4 a[156:159], v14, s[28:31], s91 offen offset:3072 sc1
mqa 92,236,52
tq 232,6,59392
mqa 84,236,48
tq 234,6,61440
s_waitcnt lgkmcnt(6)
mqa 64,240,56
tq 236,7,59392
mqa 72,240,60
tq 238,7,61440
buffer_load_dword v25, v19, s[76:79], 0 offen
mqa 76,244,60
tq 240,4,63488
mqa 68,244,56
tq 242,8,0
mqa 80,248,56
tq 244,5,63488
mqa 88,248,60
tq 246,9,0
mqa 92,252,60
tq 248,6,63488
mqa 84,252,56
tq 250,12,0
buffer_load_dwordx4 a[176:179], v14, s[28:31], s93 offen offset:0 sc1
s_waitcnt lgkmcnt(6)
s_waitcnt vmcnt(10)
mqa 64,224,80
tq 252,7,63488
mqa 72,224,84
tq 254,13,0
buffer_load_dwordx4 a[180:183], v14, s[28:31], s93 offen offset:1024 sc1
mqa 76,228,84
tq 224,16,10240
mqa 68,228,80
tq 226,16,12288
buffer_load_dwordx4 a[184:187], v14, s[28:31], s93 offen offset:2048 sc1
mqa 80,232,80
tq 228,17,10240
mqa 88,232,84
tq 230,17,12288
buffer_load_dwordx4 a[188:191], v14, s[28:31], s93 offen offset:3072 sc1
mqa 92,236,84
tq 232,28,10240
s_add_u32 m0, s56, 83968
mqa 84,236,80
tq 234,28,12288
buffer_load_dwordx4 v30, s[72:75], s48 offen offset:0 lds
s_waitcnt lgkmcnt(6)
mqa 64,240,88
tq 236,29,10240
mqa 72,240,92
tq 238,29,12288
buffer_load_dwordx4 v31, s[72:75], s48 offen offset:1024 lds
mqa 76,244,92
tq 240,16,14336
mqa 68,244,88
tq 242,16,16384
buffer_load_dwordx4 a[208:211], v14, s[28:31], s95 offen offset:0 sc1
mqa 80,248,88
tq 244,17,14336
mqa 88,248,92
tq 246,17,16384
buffer_load_dwordx4 a[212:215], v14, s[28:31], s95 offen offset:1024 sc1
mqa 92,252,92
tq 248,28,14336
mqa 84,252,88
tq 250,28,16384
buffer_load_dwordx4 a[216:219], v14, s[28:31], s95 offen offset:2048 sc1
s_waitcnt lgkmcnt(6)
s_waitcnt vmcnt(14)
mqa 64,224,112
tq 252,29,14336
tq 254,29,16384
mqa 72,224,116
buffer_load_dwordx4 a[220:223], v14, s[28:31], s95 offen offset:3072 sc1
s_barrier
mqa 76,228,116
tq 224,4,26624
mqa 68,228,112
tq 226,4,28672
mqa 80,232,112
tq 228,5,26624
mqa 88,232,116
tq 230,5,28672
mqa 92,236,116
tq 232,6,26624
mqa 84,236,112
tq 234,6,28672
s_waitcnt lgkmcnt(6)
mqa 64,240,120
tq 236,7,26624
mqa 72,240,124
tq 238,7,28672
mqa 76,244,124
tq 240,4,30720
mqa 68,244,120
tq 242,4,32768
mqa 80,248,120
tq 244,5,30720
mqa 88,248,124
tq 246,5,32768
mqa 92,252,124
tq 248,6,30720
mqa 84,252,120
tq 250,6,32768
s_waitcnt lgkmcnt(6)
s_waitcnt vmcnt(11)
mqa 64,224,144
tq 252,7,30720
s_add_u32 m0, s56, 2048
mqa 72,224,148
tq 254,7,32768
buffer_load_dwordx4 v30, s[72:75], s50 offen offset:0 lds
mqa 76,228,148
tq 224,8,10240
mqa 68,228,144
tq 226,8,12288
buffer_load_dwordx4 v31, s[72:75], s50 offen offset:1024 lds
mqa 80,232,144
tq 228,8,14336
s_add_u32 m0, s56, 124928
mqa 88,232,148
tq 230,8,16384
buffer_load_dwordx4 v30, s[72:75], s52 offen offset:0 lds
mqa 92,236,148
tq 232,8,2048
mqa 84,236,144
tq 234,8,4096
buffer_load_dwordx4 v31, s[72:75], s52 offen offset:1024 lds
s_waitcnt lgkmcnt(6)
mqa 64,240,152
tq 236,8,6144
mqa 72,240,156
tq 238,8,8192
mqa 76,244,156
tq 240,9,10240
mqa 68,244,152
tq 242,9,12288
mqa 80,248,152
tq 244,9,14336
mqa 88,248,156
tq 246,9,16384
mqa 92,252,156
tq 248,9,2048
mqa 84,252,152
tq 250,9,4096
s_waitcnt lgkmcnt(6)
s_waitcnt vmcnt(10)
mqa 64,224,176
tq 252,9,6144
tq 254,9,8192
mqa 72,224,180
s_waitcnt vmcnt(8)
s_barrier
mqa 72,228,188
tq 224,12,10240
mqa 64,228,184
tq 226,12,12288
s_waitcnt vmcnt(4)
mqa 64,232,208
tq 228,12,14336
mqa 72,232,212
tq 230,12,16384
mqa 72,236,220
tq 232,12,2048
mqa 64,236,216
tq 234,12,4096
s_waitcnt lgkmcnt(6)
mqa 68,240,176
tq 236,12,6144
mqa 76,240,180
tq 238,12,8192
mqa 76,244,188
tq 240,13,10240
v_fma_f32 v200, v136, v72, v200
v_fma_f32 v201, v137, v73, v201
mqa 68,244,184
tq 242,13,12288
v_fma_f32 v202, v138, v74, v202
mqa 68,248,208
tq 244,13,14336
v_fma_f32 v203, v139, v75, v203
v_fma_f32 v192, v128, v64, v192
mqa 76,248,212
tq 246,13,16384
v_fma_f32 v193, v129, v65, v193
mqa 76,252,220
tq 248,13,2048
v_fma_f32 v194, v130, v66, v194
v_fma_f32 v195, v131, v67, v195
mqa 68,252,216
tq 250,13,4096
s_waitcnt lgkmcnt(6)
mqa 80,224,176
tq 252,13,6144
s_add_u32 m0, s56, 10240
mqa 88,224,180
tq 254,13,8192
buffer_load_dwordx4 v30, s[72:75], s43 offen offset:0 lds
mqa 88,228,188
tq 224,8,18432
v_cvt_pk_bf16_f32 v36, v200, v201
v_cvt_pk_bf16_f32 v37, v202, v203
mqa 80,228,184
tq 226,8,20480
buffer_load_dwordx4 v31, s[72:75], s43 offen offset:1024 lds
v_cvt_pk_bf16_f32 v32, v192, v193
mqa 80,232,208
tq 228,9,18432
v_cvt_pk_bf16_f32 v33, v194, v195
v_fma_f32 v204, v140, v76, v204
mqa 88,232,212
tq 230,9,20480
v_fma_f32 v205, v141, v77, v205
v_fma_f32 v206, v142, v78, v206
mqa 88,236,220
tq 232,12,18432
v_fma_f32 v207, v143, v79, v207
v_fma_f32 v196, v132, v68, v196
mqa 80,236,216
tq 234,12,20480
v_fma_f32 v197, v133, v69, v197
s_waitcnt lgkmcnt(6)
mqa 84,240,176
tq 236,13,18432
s_add_u32 m0, s56, 18432
v_fma_f32 v198, v134, v70, v198
mqa 92,240,180
tq 238,13,20480
buffer_load_dwordx4 v10, s[76:79], s46 offen offset:0 lds
mqa 92,244,188
tq 240,8,22528
v_fma_f32 v199, v135, v71, v199
v_cvt_pk_bf16_f32 v38, v204, v205
mqa 84,244,184
tq 242,8,24576
v_cvt_pk_bf16_f32 v39, v206, v207
buffer_store_dwordx4 v[36:39], v15, s[28:31], s80 offen offset:1024 sc1
mqa 84,248,208
tq 244,9,22528
v_cvt_pk_bf16_f32 v34, v196, v197
v_cvt_pk_bf16_f32 v35, v198, v199
mqa 92,248,212
tq 246,9,24576
buffer_load_dwordx4 v11, s[76:79], s46 offen offset:1024 lds
buffer_store_dwordx4 v[32:35], v15, s[28:31], s80 offen offset:0 sc1
mqa 92,252,220
tq 248,12,22528
v_fma_f32 v216, v152, v88, v216
v_fma_f32 v217, v153, v89, v217
mqa 84,252,216
tq 250,12,24576
v_fma_f32 v218, v154, v90, v218
s_waitcnt lgkmcnt(6)
mqa 96,224,96
tq 252,13,22528
tq 254,13,24576
s_add_u32 m0, s56, 92160
mqa 104,224,100
buffer_load_dwordx4 v10, s[76:79], s50 offen offset:0 lds
s_waitcnt vmcnt(7)
s_barrier
mqa 108,228,100
tq 224,4,2048
v_fma_f32 v219, v155, v91, v219
v_fma_f32 v208, v144, v80, v208
mqa 100,228,96
tq 226,4,4096
v_fma_f32 v209, v145, v81, v209
v_fma_f32 v210, v146, v82, v210
mqa 112,232,96
tq 228,5,2048
v_fma_f32 v211, v147, v83, v211
v_cvt_pk_bf16_f32 v44, v216, v217
v_cvt_pk_bf16_f32 v45, v218, v219
mqa 120,232,100
tq 230,5,4096
buffer_load_dwordx4 v11, s[76:79], s50 offen offset:1024 lds
v_cvt_pk_bf16_f32 v40, v208, v209
mqa 124,236,100
tq 232,6,2048
v_cvt_pk_bf16_f32 v41, v210, v211
v_fma_f32 v220, v156, v92, v220
mqa 116,236,96
tq 234,6,4096
v_fma_f32 v221, v157, v93, v221
s_waitcnt lgkmcnt(6)
mqa 96,240,104
tq 236,7,2048
s_add_u32 m0, s56, 149504
v_fma_f32 v222, v158, v94, v222
mqa 104,240,108
tq 238,7,4096
buffer_load_dwordx4 v30, s[72:75], s45 offen offset:0 lds
v_fma_f32 v223, v159, v95, v223
mqa 108,244,108
tq 240,4,6144
v_fma_f32 v212, v148, v84, v212
v_fma_f32 v213, v149, v85, v213
mqa 100,244,104
tq 242,4,8192
v_fma_f32 v214, v150, v86, v214
v_fma_f32 v215, v151, v87, v215
mqa 112,248,104
tq 244,5,6144
v_cvt_pk_bf16_f32 v46, v220, v221
v_cvt_pk_bf16_f32 v47, v222, v223
mqa 120,248,108
tq 246,5,8192
buffer_load_dwordx4 v31, s[72:75], s45 offen offset:1024 lds
buffer_store_dwordx4 v[44:47], v15, s[28:31], s80 offen offset:3072 sc1
mqa 124,252,108
L_hop5_0_%=:
s_cbranch_execz L_ret_0_%=
tq 248,6,6144
v_cvt_pk_bf16_f32 v42, v212, v213
v_cvt_pk_bf16_f32 v43, v214, v215
mqa 116,252,104
tq 250,6,8192
buffer_store_dwordx4 v[40:43], v15, s[28:31], s80 offen offset:2048 sc1
s_waitcnt lgkmcnt(6)
mqa 96,224,128
tq 252,7,6144
s_add_u32 m0, s56, 100352
mqa 104,224,132
tq 254,7,8192
buffer_load_dwordx4 v30, s[72:75], s47 offen offset:0 lds
mqa 108,228,132
tq 224,8,59392
ds_read_b128 v[64:67], v24 offset:1024
mqa 100,228,128
tq 226,8,61440
buffer_load_dwordx4 v31, s[72:75], s47 offen offset:1024 lds
mqa 112,232,128
tq 228,9,59392
ds_read_b128 v[72:75], v24 offset:1024
mqa 120,232,132
tq 230,9,61440
ds_read_b128 v[68:71], v24 offset:1088
mqa 124,236,132
tq 232,12,59392
s_add_u32 m0, s56, 133120
ds_read_b128 v[76:79], v24 offset:1088
mqa 116,236,128
tq 234,12,61440
buffer_load_dwordx4 v30, s[72:75], s51 offen offset:0 lds
s_waitcnt lgkmcnt(10)
mqa 96,240,136
tq 236,13,59392
mqa 104,240,140
tq 238,13,61440
buffer_load_dwordx4 v31, s[72:75], s51 offen offset:1024 lds
mqa 108,244,140
tq 240,8,63488
ds_read_b128 v[80:83], v24 offset:1152
mqa 100,244,136
tq 242,16,0
mqa 112,248,136
tq 244,9,63488
s_add_u32 m0, s56, 34816
mqa 120,248,140
tq 246,17,0
buffer_load_dwordx4 v10, s[76:79], s54 offen offset:0 lds
mqa 124,252,140
tq 248,12,63488
ds_read_b128 v[88:91], v24 offset:1152
mqa 116,252,136
tq 250,28,0
s_waitcnt lgkmcnt(8)
mqa 96,224,160
tq 252,13,63488
tq 254,29,0
mqa 104,224,164
s_waitcnt vmcnt(15)
s_barrier
mqa 108,228,164
tq 224,4,43008
ds_read_b128 v[84:87], v24 offset:1216
mqa 100,228,160
tq 226,4,45056
buffer_load_dwordx4 v11, s[76:79], s54 offen offset:1024 lds
mqa 112,232,160
tq 228,5,43008
)ASM"
R"ASM(
s_add_u32 m0, s56, 59392
mqa 120,232,164
tq 230,5,45056
buffer_load_dwordx4 v30, s[72:75], s53 offen offset:0 lds
mqa 124,236,164
tq 232,6,43008
ds_read_b128 v[92:95], v24 offset:1216
mqa 116,236,160
tq 234,6,45056
s_waitcnt lgkmcnt(8)
mqa 96,240,168
tq 236,7,43008
mqa 104,240,172
tq 238,7,45056
buffer_load_dwordx4 v31, s[72:75], s53 offen offset:1024 lds
mqa 108,244,172
tq 240,4,47104
mqa 100,244,168
tq 242,4,49152
mqa 112,248,168
tq 244,5,47104
s_add_u32 m0, s56, 26624
mqa 120,248,172
tq 246,5,49152
buffer_load_dwordx4 v30, s[72:75], s55 offen offset:0 lds
mqa 124,252,172
tq 248,6,47104
mqa 116,252,168
tq 250,6,49152
s_waitcnt lgkmcnt(6)
mqa 96,224,192
tq 252,7,47104
mqa 104,224,196
tq 254,7,49152
buffer_load_dwordx4 v31, s[72:75], s55 offen offset:1024 lds
mqa 108,228,196
tq 224,4,10240
mqa 100,228,192
tq 226,4,12288
mqa 112,232,192
tq 228,5,10240
s_add_u32 m0, s56, 141312
mqa 120,232,196
tq 230,5,12288
buffer_load_dwordx4 v30, s[76:79], s40 offen offset:0 lds
mqa 124,236,196
tq 232,6,10240
mqa 116,236,192
tq 234,6,12288
s_waitcnt lgkmcnt(6)
mqa 96,240,200
tq 236,7,10240
mqa 104,240,204
tq 238,7,12288
buffer_load_dwordx4 v31, s[76:79], s40 offen offset:1024 lds
mqa 108,244,204
tq 240,4,14336
mqa 100,244,200
tq 242,4,16384
mqa 112,248,200
tq 244,5,14336
s_add_u32 m0, s56, 67584
mqa 120,248,204
tq 246,5,16384
buffer_load_dwordx4 v10, s[76:79], s41 offen offset:0 lds
mqa 124,252,204
tq 248,6,14336
mqa 116,252,200
tq 250,6,16384
s_waitcnt vmcnt(13)
buffer_store_dword v22, v21, s[28:31], 0 offen offset:64 sc1
s_waitcnt lgkmcnt(6)
mqa 96,224,16
tq 252,7,14336
tq 254,7,16384
mqa 104,224,20
s_waitcnt vmcnt(12)
s_barrier
mqa 108,228,20
tq 224,16,18432
mqa 100,228,16
tq 226,16,20480
buffer_load_dwordx4 v11, s[76:79], s41 offen offset:1024 lds
mqa 112,232,16
tq 228,17,18432
s_add_u32 m0, s56, 75776
mqa 120,232,20
tq 230,17,20480
buffer_load_dwordx4 v30, s[76:79], s41 offen offset:0 lds
mqa 124,236,20
tq 232,28,18432
mqa 116,236,16
tq 234,28,20480
s_waitcnt lgkmcnt(6)
mqa 96,240,24
tq 236,29,18432
mqa 104,240,28
tq 238,29,20480
buffer_load_dwordx4 v31, s[76:79], s41 offen offset:1024 lds
mqa 108,244,28
tq 240,16,22528
mqa 100,244,24
tq 242,16,24576
mqa 112,248,24
tq 244,17,22528
s_add_u32 m0, s56, 2048
mqa 120,248,28
tq 246,17,24576
buffer_load_dwordx4 v30, s[76:79], s42 offen offset:0 lds
mqa 124,252,28
tq 248,28,22528
mqa 116,252,24
tq 250,28,24576
s_waitcnt lgkmcnt(6)
mqa 96,224,48
tq 252,29,22528
mqa 104,224,52
tq 254,29,24576
buffer_load_dwordx4 v31, s[76:79], s42 offen offset:1024 lds
mqa 108,228,52
tq 224,8,34816
mqa 100,228,48
tq 226,8,36864
mqa 112,232,48
tq 228,9,34816
s_add_u32 m0, s56, 83968
mqa 120,232,52
tq 230,9,36864
buffer_load_dwordx4 v10, s[76:79], s47 offen offset:0 lds
mqa 124,236,52
tq 232,12,34816
mqa 116,236,48
tq 234,12,36864
s_waitcnt lgkmcnt(6)
mqa 96,240,56
tq 236,13,34816
mqa 104,240,60
tq 238,13,36864
buffer_load_dwordx4 v11, s[76:79], s47 offen offset:1024 lds
mqa 108,244,60
tq 240,8,38912
mqa 100,244,56
tq 242,8,40960
mqa 112,248,56
tq 244,9,38912
s_add_u32 m0, s56, 43008
mqa 120,248,60
tq 246,9,40960
buffer_load_dwordx4 v10, s[76:79], s44 offen offset:0 lds
mqa 124,252,60
tq 248,12,38912
mqa 116,252,56
tq 250,12,40960
s_waitcnt lgkmcnt(6)
mqa 96,224,80
tq 252,13,38912
tq 254,13,40960
mqa 104,224,84
s_waitcnt vmcnt(18)
s_barrier
mqa 108,228,84
tq 224,8,43008
mqa 100,228,80
tq 226,8,45056
buffer_load_dwordx4 v11, s[76:79], s44 offen offset:1024 lds
mqa 112,232,80
tq 228,9,43008
s_add_u32 m0, s56, 124928
mqa 120,232,84
tq 230,9,45056
buffer_load_dwordx4 v30, s[76:79], s44 offen offset:0 lds
mqa 124,236,84
tq 232,12,43008
mqa 116,236,80
tq 234,12,45056
s_waitcnt lgkmcnt(6)
mqa 96,240,88
tq 236,13,43008
mqa 104,240,92
tq 238,13,45056
buffer_load_dwordx4 v31, s[76:79], s44 offen offset:1024 lds
mqa 108,244,92
tq 240,8,47104
mqa 100,244,88
tq 242,8,49152
mqa 112,248,88
tq 244,9,47104
mqa 120,248,92
tq 246,9,49152
mqa 124,252,92
tq 248,12,47104
mqa 116,252,88
tq 250,12,49152
s_waitcnt lgkmcnt(6)
mqa 96,224,112
tq 252,13,47104
s_add_u32 m0, s56, 10240
mqa 104,224,116
tq 254,13,49152
buffer_load_dwordx4 v30, s[76:79], s46 offen offset:0 lds
mqa 108,228,116
tq 224,16,2048
mqa 100,228,112
tq 226,16,4096
buffer_load_dwordx4 v31, s[76:79], s46 offen offset:1024 lds
mqa 112,232,112
tq 228,17,2048
mqa 120,232,116
tq 230,17,4096
mqa 124,236,116
tq 232,28,2048
mqa 116,236,112
tq 234,28,4096
s_waitcnt lgkmcnt(6)
mqa 96,240,120
tq 236,29,2048
mqa 104,240,124
tq 238,29,4096
mqa 108,244,124
tq 240,16,6144
mqa 100,244,120
tq 242,16,8192
mqa 112,248,120
tq 244,17,6144
mqa 120,248,124
tq 246,17,8192
mqa 124,252,124
tq 248,28,6144
mqa 116,252,120
tq 250,28,8192
s_waitcnt lgkmcnt(6)
mqa 96,224,144
tq 252,29,6144
tq 254,29,8192
s_add_u32 m0, s56, 149504
mqa 104,224,148
buffer_load_dwordx4 v10, s[76:79], s48 offen offset:0 lds
s_waitcnt vmcnt(18)
s_barrier
mqa 108,228,148
tq 224,4,59392
mqa 100,228,144
tq 226,4,61440
buffer_load_dwordx4 v11, s[76:79], s48 offen offset:1024 lds
mqa 112,232,144
tq 228,4,63488
mqa 120,232,148
tq 230,8,0
mqa 124,236,148
tq 232,4,26624
mqa 116,236,144
tq 234,4,28672
s_waitcnt lgkmcnt(6)
mqa 96,240,152
tq 236,4,30720
mqa 104,240,156
tq 238,4,32768
mqa 108,244,156
tq 240,5,59392
mqa 100,244,152
tq 242,5,61440
mqa 112,248,152
tq 244,5,63488
mqa 120,248,156
tq 246,9,0
mqa 124,252,156
tq 248,5,26624
mqa 116,252,152
tq 250,5,28672
s_waitcnt lgkmcnt(6)
mqa 96,224,176
tq 252,5,30720
s_add_u32 m0, s56, 100352
mqa 104,224,180
tq 254,5,32768
buffer_load_dwordx4 v10, s[76:79], s55 offen offset:0 lds
mqa 104,228,188
tq 224,6,59392
mqa 96,228,184
tq 226,6,61440
buffer_load_dwordx4 v11, s[76:79], s55 offen offset:1024 lds
mqa 96,232,208
tq 228,6,63488
s_add_u32 m0, s56, 108544
mqa 104,232,212
tq 230,12,0
buffer_load_dwordx4 v30, s[76:79], s48 offen offset:0 lds
mqa 104,236,220
tq 232,6,26624
mqa 96,236,216
tq 234,6,28672
s_waitcnt lgkmcnt(6)
mqa 100,240,176
tq 236,6,30720
mqa 108,240,180
tq 238,6,32768
buffer_load_dwordx4 v31, s[76:79], s48 offen offset:1024 lds
mqa 108,244,188
tq 240,7,59392
v_fma_f32 v232, v168, v104, v232
v_fma_f32 v233, v169, v105, v233
mqa 100,244,184
tq 242,7,61440
v_fma_f32 v234, v170, v106, v234
mqa 100,248,208
tq 244,7,63488
v_fma_f32 v235, v171, v107, v235
v_fma_f32 v224, v160, v96, v224
mqa 108,248,212
tq 246,13,0
v_fma_f32 v225, v161, v97, v225
mqa 108,252,220
tq 248,7,26624
v_fma_f32 v226, v162, v98, v226
mqa 100,252,216
tq 250,7,28672
s_waitcnt lgkmcnt(6)
mqa 112,224,176
tq 252,7,30720
tq 254,7,32768
mqa 120,224,180
buffer_load_dword v23, v20, s[28:31], 0 offen offset:64 sc1
s_waitcnt vmcnt(22)
s_barrier
mqa 120,228,188
tq 224,8,51200
v_fma_f32 v227, v163, v99, v227
v_cvt_pk_bf16_f32 v52, v232, v233
mqa 112,228,184
tq 226,8,53248
v_cvt_pk_bf16_f32 v53, v234, v235
mqa 112,232,208
tq 228,9,51200
v_cvt_pk_bf16_f32 v48, v224, v225
v_cvt_pk_bf16_f32 v49, v226, v227
mqa 120,232,212
tq 230,9,53248
v_fma_f32 v236, v172, v108, v236
mqa 120,236,220
tq 232,8,55296
v_fma_f32 v237, v173, v109, v237
v_fma_f32 v238, v174, v110, v238
mqa 112,236,216
tq 234,8,57344
s_waitcnt lgkmcnt(6)
mqa 116,240,176
tq 236,9,55296
v_fma_f32 v239, v175, v111, v239
v_fma_f32 v228, v164, v100, v228
mqa 124,240,180
tq 238,9,57344
v_fma_f32 v229, v165, v101, v229
mqa 124,244,188
tq 240,12,51200
v_fma_f32 v230, v166, v102, v230
v_fma_f32 v231, v167, v103, v231
mqa 116,244,184
tq 242,12,53248
v_cvt_pk_bf16_f32 v54, v236, v237
v_cvt_pk_bf16_f32 v55, v238, v239
mqa 116,248,208
tq 244,13,51200
mqa 124,248,212
tq 246,13,53248
buffer_store_dwordx4 v[52:55], v15, s[28:31], s81 offen offset:1024 sc1
v_cvt_pk_bf16_f32 v50, v228, v229
v_cvt_pk_bf16_f32 v51, v230, v231
buffer_store_dwordx4 v[48:51], v15, s[28:31], s81 offen offset:0 sc1
mqa 124,252,220
tq 248,12,55296
ds_read_b128 v[96:99], v24 offset:1280
v_fma_f32 v248, v184, v120, v248
v_fma_f32 v249, v185, v121, v249
mqa 116,252,216
tq 250,12,57344
s_waitcnt lgkmcnt(7)
mqv 64,224,32
tq 252,13,55296
s_add_u32 m0, s56, 133120
v_fma_f32 v250, v186, v122, v250
mqv 72,224,36
tq 254,13,57344
buffer_load_dwordx4 v30, s[76:79], s50 offen offset:0 lds
v_fma_f32 v251, v187, v123, v251
mqv 76,228,36
tq 224,16,10240
ds_read_b128 v[104:107], v24 offset:1280
v_fma_f32 v240, v176, v112, v240
mqv 68,228,32
tq 226,16,12288
v_fma_f32 v241, v177, v113, v241
v_fma_f32 v242, v178, v114, v242
mqv 64,232,40
tq 228,17,10240
v_fma_f32 v243, v179, v115, v243
v_cvt_pk_bf16_f32 v60, v248, v249
v_cvt_pk_bf16_f32 v61, v250, v251
mqv 72,232,44
tq 230,17,12288
buffer_load_dwordx4 v31, s[76:79], s50 offen offset:1024 lds
ds_read_b128 v[100:103], v24 offset:1344
mqv 76,236,44
tq 232,16,14336
v_cvt_pk_bf16_f32 v56, v240, v241
v_cvt_pk_bf16_f32 v57, v242, v243
ds_read_b128 v[108:111], v24 offset:1344
mqv 68,236,40
tq 234,16,16384
v_fma_f32 v252, v188, v124, v252
s_waitcnt lgkmcnt(9)
mqv 80,240,32
tq 236,17,14336
v_fma_f32 v253, v189, v125, v253
v_fma_f32 v254, v190, v126, v254
mqv 88,240,36
tq 238,17,16384
v_fma_f32 v255, v191, v127, v255
ds_read_b128 v[112:115], v24 offset:1408
mqv 92,244,36
tq 240,28,10240
v_fma_f32 v244, v180, v116, v244
v_fma_f32 v245, v181, v117, v245
mqv 84,244,32
tq 242,28,12288
v_fma_f32 v246, v182, v118, v246
v_fma_f32 v247, v183, v119, v247
mqv 80,248,40
tq 244,29,10240
v_cvt_pk_bf16_f32 v62, v252, v253
v_cvt_pk_bf16_f32 v63, v254, v255
mqv 88,248,44
tq 246,29,12288
buffer_store_dwordx4 v[60:63], v15, s[28:31], s81 offen offset:3072 sc1
ds_read_b128 v[120:123], v24 offset:1408
mqv 92,252,44
tq 248,28,14336
v_cvt_pk_bf16_f32 v58, v244, v245
v_cvt_pk_bf16_f32 v59, v246, v247
mqv 84,252,40
tq 250,28,16384
buffer_store_dwordx4 v[56:59], v15, s[28:31], s81 offen offset:2048 sc1
ds_read_b128 v[116:119], v24 offset:1472
s_waitcnt lgkmcnt(9)
mqv 96,224,32
tq 252,29,14336
tq 254,29,16384
ds_read_b128 v[124:127], v24 offset:1472
mqv 104,224,36
s_waitcnt vmcnt(6)
v_cmp_ne_u32 vcc, v23, v22
s_cbranch_vccnz L_slow2_0_%=
L_back2_0_%=:
buffer_load_dwordx4 a[0:3], v15, s[28:31], s82 offen offset:0 sc1
s_barrier
mqv 108,228,36
tq 224,8,2048
mqv 100,228,32
tq 226,8,4096
buffer_load_dwordx4 a[4:7], v15, s[28:31], s82 offen offset:1024 sc1
mqv 96,232,40
tq 228,9,2048
mqv 104,232,44
s_nop 0
L_hop6_0_%=:
s_cbranch_execz L_ret_0_%=
tq 230,9,4096
buffer_load_dwordx4 a[8:11], v15, s[28:31], s82 offen offset:2048 sc1
mqv 108,236,44
tq 232,12,2048
mqv 100,236,40
tq 234,12,4096
buffer_load_dwordx4 a[12:15], v15, s[28:31], s82 offen offset:3072 sc1
s_waitcnt lgkmcnt(6)
mqv 112,240,32
tq 236,13,2048
mqv 120,240,36
tq 238,13,4096
mqv 124,244,36
tq 240,8,6144
mqv 116,244,32
tq 242,8,8192
mqv 112,248,40
tq 244,9,6144
mqv 120,248,44
tq 246,9,8192
buffer_load_dwordx4 a[32:35], v15, s[28:31], s84 offen offset:0 sc1
mqv 124,252,44
tq 248,12,6144
mqv 116,252,40
tq 250,12,8192
s_waitcnt lgkmcnt(6)
mqv 64,224,48
tq 252,13,6144
mqv 72,224,52
tq 254,13,8192
buffer_load_dwordx4 a[36:39], v15, s[28:31], s84 offen offset:1024 sc1
mqv 76,228,52
tq 224,8,10240
mqv 68,228,48
tq 226,8,12288
mqv 80,232,48
tq 228,9,10240
mqv 88,232,52
tq 230,9,12288
buffer_load_dwordx4 a[40:43], v15, s[28:31], s84 offen offset:2048 sc1
mqv 92,236,52
tq 232,12,10240
mqv 84,236,48
tq 234,12,12288
s_waitcnt lgkmcnt(6)
mqv 64,240,56
tq 236,13,10240
mqv 72,240,60
tq 238,13,12288
buffer_load_dwordx4 a[44:47], v15, s[28:31], s84 offen offset:3072 sc1
mqv 76,244,60
tq 240,8,14336
mqv 68,244,56
tq 242,8,16384
mqv 80,248,56
tq 244,9,14336
s_add_u32 m0, s56, 26624
mqv 88,248,60
tq 246,9,16384
buffer_load_dwordx4 v30, s[76:79], s49 offen offset:0 lds
mqv 92,252,60
tq 248,12,14336
mqv 84,252,56
tq 250,12,16384
s_waitcnt lgkmcnt(6)
mqv 96,224,48
tq 252,13,14336
tq 254,13,16384
mqv 104,224,52
s_barrier
mqv 108,228,52
tq 224,4,51200
mqv 100,228,48
tq 226,4,53248
buffer_load_dwordx4 v31, s[76:79], s49 offen offset:1024 lds
mqv 112,232,48
tq 228,5,51200
mqv 120,232,52
tq 230,5,53248
buffer_load_dwordx4 a[64:67], v15, s[28:31], s86 offen offset:0 sc1
mqv 124,236,52
tq 232,6,51200
mqv 116,236,48
tq 234,6,53248
s_waitcnt lgkmcnt(6)
mqv 96,240,56
tq 236,7,51200
mqv 104,240,60
tq 238,7,53248
buffer_load_dwordx4 a[68:71], v15, s[28:31], s86 offen offset:1024 sc1
mqv 108,244,60
tq 240,4,55296
mqv 100,244,56
tq 242,4,57344
mqv 112,248,56
tq 244,5,55296
mqv 120,248,60
tq 246,5,57344
buffer_load_dwordx4 a[72:75], v15, s[28:31], s86 offen offset:2048 sc1
mqv 124,252,60
tq 248,6,55296
mqv 116,252,56
tq 250,6,57344
s_waitcnt vmcnt(13)
buffer_store_dword v22, v21, s[28:31], 0 offen offset:96 sc1
s_waitcnt lgkmcnt(6)
s_waitcnt vmcnt(10)
mqa 64,224,0
tq 252,7,55296
mqa 72,224,4
tq 254,7,57344
buffer_load_dwordx4 a[76:79], v15, s[28:31], s86 offen offset:3072 sc1
mqa 76,228,4
tq 224,4,2048
mqa 68,228,0
tq 226,4,4096
mqa 80,232,0
tq 228,5,2048
mqa 88,232,4
tq 230,5,4096
buffer_load_dwordx4 a[96:99], v15, s[28:31], s88 offen offset:0 sc1
mqa 92,236,4
tq 232,6,2048
mqa 84,236,0
tq 234,6,4096
s_waitcnt lgkmcnt(6)
mqa 64,240,8
tq 236,7,2048
mqa 72,240,12
tq 238,7,4096
buffer_load_dwordx4 a[100:103], v15, s[28:31], s88 offen offset:1024 sc1
mqa 76,244,12
tq 240,4,6144
mqa 68,244,8
tq 242,4,8192
mqa 80,248,8
tq 244,5,6144
mqa 88,248,12
tq 246,5,8192
buffer_load_dwordx4 a[104:107], v15, s[28:31], s88 offen offset:2048 sc1
mqa 92,252,12
tq 248,6,6144
mqa 84,252,8
tq 250,6,8192
s_waitcnt lgkmcnt(6)
mqa 96,224,0
tq 252,7,6144
tq 254,7,8192
mqa 104,224,4
s_barrier
mqa 108,228,4
tq 224,4,43008
mqa 100,228,0
tq 226,4,45056
buffer_load_dwordx4 a[108:111], v15, s[28:31], s88 offen offset:3072 sc1
mqa 112,232,0
tq 228,5,43008
mqa 120,232,4
tq 230,5,45056
buffer_load_dwordx4 a[128:131], v15, s[28:31], s90 offen offset:0 sc1
mqa 124,236,4
tq 232,6,43008
mqa 116,236,0
tq 234,6,45056
s_waitcnt lgkmcnt(6)
mqa 96,240,8
tq 236,7,43008
mqa 104,240,12
tq 238,7,45056
buffer_load_dwordx4 a[132:135], v15, s[28:31], s90 offen offset:1024 sc1
mqa 108,244,12
tq 240,4,47104
mqa 100,244,8
tq 242,4,49152
mqa 112,248,8
tq 244,5,47104
mqa 120,248,12
tq 246,5,49152
buffer_load_dwordx4 a[136:139], v15, s[28:31], s90 offen offset:2048 sc1
mqa 124,252,12
tq 248,6,47104
mqa 116,252,8
tq 250,6,49152
s_waitcnt lgkmcnt(6)
s_waitcnt vmcnt(14)
mqa 64,224,32
tq 252,7,47104
mqa 72,224,36
tq 254,7,49152
buffer_load_dwordx4 a[140:143], v15, s[28:31], s90 offen offset:3072 sc1
mqa 76,228,36
tq 224,8,59392
mqa 68,228,32
tq 226,8,61440
mqa 80,232,32
tq 228,9,59392
s_add_u32 m0, s56, 59392
mqa 88,232,36
tq 230,9,61440
buffer_load_dwordx4 v10, s[76:79], s52 offen offset:0 lds
mqa 92,236,36
tq 232,12,59392
mqa 84,236,32
tq 234,12,61440
s_waitcnt lgkmcnt(6)
mqa 64,240,40
tq 236,13,59392
mqa 72,240,44
tq 238,13,61440
buffer_load_dwordx4 v11, s[76:79], s52 offen offset:1024 lds
mqa 76,244,44
tq 240,8,63488
mqa 68,244,40
tq 242,16,0
mqa 80,248,40
tq 244,9,63488
s_add_u32 m0, s56, 116736
mqa 88,248,44
tq 246,17,0
buffer_load_dwordx4 v30, s[76:79], s52 offen offset:0 lds
mqa 92,252,44
tq 248,12,63488
mqa 84,252,40
tq 250,28,0
s_waitcnt lgkmcnt(6)
mqa 96,224,32
tq 252,13,63488
tq 254,29,0
mqa 104,224,36
s_barrier
mqa 108,228,36
tq 224,4,18432
mqa 100,228,32
tq 226,4,20480
buffer_load_dwordx4 v31, s[76:79], s52 offen offset:1024 lds
mqa 112,232,32
tq 228,5,18432
mqa 120,232,36
tq 230,5,20480
buffer_load_dwordx4 a[160:163], v15, s[28:31], s92 offen offset:0 sc1
mqa 124,236,36
tq 232,6,18432
mqa 116,236,32
tq 234,6,20480
s_waitcnt lgkmcnt(6)
mqa 96,240,40
tq 236,7,18432
mqa 104,240,44
tq 238,7,20480
buffer_load_dwordx4 a[164:167], v15, s[28:31], s92 offen offset:1024 sc1
mqa 108,244,44
tq 240,4,22528
mqa 100,244,40
tq 242,4,24576
mqa 112,248,40
tq 244,5,22528
mqa 120,248,44
tq 246,5,24576
buffer_load_dwordx4 a[168:171], v15, s[28:31], s92 offen offset:2048 sc1
mqa 124,252,44
tq 248,6,22528
mqa 116,252,40
tq 250,6,24576
s_waitcnt lgkmcnt(6)
s_waitcnt vmcnt(15)
mqa 64,224,64
tq 252,7,22528
mqa 72,224,68
tq 254,7,24576
buffer_load_dwordx4 a[172:175], v15, s[28:31], s92 offen offset:3072 sc1
mqa 76,228,68
tq 224,4,10240
mqa 68,228,64
tq 226,4,12288
mqa 80,232,64
tq 228,5,10240
s_add_u32 m0, s56, 67584
mqa 88,232,68
tq 230,5,12288
buffer_load_dwordx4 v30, s[76:79], s54 offen offset:0 lds
mqa 92,236,68
tq 232,6,10240
mqa 84,236,64
tq 234,6,12288
s_waitcnt lgkmcnt(6)
mqa 64,240,72
tq 236,7,10240
mqa 72,240,76
tq 238,7,12288
buffer_load_dwordx4 v31, s[76:79], s54 offen offset:1024 lds
mqa 76,244,76
tq 240,4,14336
mqa 68,244,72
tq 242,4,16384
mqa 80,248,72
tq 244,5,14336
mqa 88,248,76
tq 246,5,16384
buffer_load_dwordx4 a[192:195], v15, s[28:31], s94 offen offset:0 sc1
mqa 92,252,76
tq 248,6,14336
mqa 84,252,72
tq 250,6,16384
s_waitcnt lgkmcnt(6)
mqa 96,224,64
tq 252,7,14336
tq 254,7,16384
mqa 104,224,68
s_barrier
mqa 108,228,68
tq 224,16,18432
mqa 100,228,64
tq 226,16,20480
buffer_load_dwordx4 a[196:199], v15, s[28:31], s94 offen offset:1024 sc1
mqa 112,232,64
tq 228,17,18432
mqa 120,232,68
tq 230,17,20480
buffer_load_dwordx4 a[200:203], v15, s[28:31], s94 offen offset:2048 sc1
mqa 124,236,68
tq 232,28,18432
mqa 116,236,64
tq 234,28,20480
s_waitcnt lgkmcnt(6)
mqa 96,240,72
tq 236,29,18432
mqa 104,240,76
tq 238,29,20480
buffer_load_dwordx4 a[204:207], v15, s[28:31], s94 offen offset:3072 sc1
mqa 108,244,76
tq 240,16,22528
mqa 100,244,72
tq 242,16,24576
mqa 112,248,72
tq 244,17,22528
s_add_u32 m0, s56, 141312
mqa 120,248,76
tq 246,17,24576
buffer_load_dwordx4 v10, s[76:79], s43 offen offset:0 lds
mqa 124,252,76
tq 248,28,22528
mqa 116,252,72
tq 250,28,24576
s_waitcnt lgkmcnt(6)
s_waitcnt vmcnt(19)
mqa 64,224,96
tq 252,29,22528
mqa 72,224,100
tq 254,29,24576
buffer_load_dwordx4 v11, s[76:79], s43 offen offset:1024 lds
mqa 76,228,100
tq 224,8,43008
mqa 68,228,96
tq 226,8,45056
mqa 80,232,96
tq 228,9,43008
s_add_u32 m0, s56, 51200
mqa 88,232,100
tq 230,9,45056
buffer_load_dwordx4 v10, s[76:79], s45 offen offset:0 lds
mqa 92,236,100
tq 232,12,43008
mqa 84,236,96
tq 234,12,45056
s_waitcnt lgkmcnt(6)
mqa 64,240,104
tq 236,13,43008
mqa 72,240,108
tq 238,13,45056
buffer_load_dwordx4 v11, s[76:79], s45 offen offset:1024 lds
mqa 76,244,108
tq 240,8,47104
mqa 68,244,104
tq 242,8,49152
mqa 80,248,104
tq 244,9,47104
s_add_u32 m0, s56, 75776
mqa 88,248,108
tq 246,9,49152
buffer_load_dwordx4 v10, s[76:79], s49 offen offset:0 lds
mqa 92,252,108
tq 248,12,47104
mqa 84,252,104
tq 250,12,49152
s_waitcnt lgkmcnt(6)
mqa 96,224,96
tq 252,13,47104
tq 254,13,49152
mqa 104,224,100
buffer_load_dword v23, v20, s[28:31], 0 offen offset:96 sc1
s_barrier
mqa 108,228,100
tq 224,8,26624
mqa 100,228,96
tq 226,8,28672
buffer_load_dwordx4 v11, s[76:79], s49 offen offset:1024 lds
mqa 112,232,96
tq 228,9,26624
s_add_u32 m0, s56, 2048
mqa 120,232,100
tq 230,9,28672
buffer_load_dwordx4 v10, s[76:79], s51 offen offset:0 lds
mqa 124,236,100
tq 232,12,26624
mqa 116,236,96
tq 234,12,28672
s_waitcnt lgkmcnt(6)
mqa 96,240,104
tq 236,13,26624
mqa 104,240,108
tq 238,13,28672
buffer_load_dwordx4 v11, s[76:79], s51 offen offset:1024 lds
mqa 108,244,108
tq 240,8,30720
mqa 100,244,104
tq 242,8,32768
mqa 112,248,104
tq 244,9,30720
s_add_u32 m0, s56, 43008
mqa 120,248,108
tq 246,9,32768
buffer_load_dwordx4 v10, s[76:79], s53 offen offset:0 lds
mqa 124,252,108
tq 248,12,30720
mqa 116,252,104
tq 250,12,32768
s_waitcnt lgkmcnt(6)
s_waitcnt vmcnt(24)
mqa 64,224,128
tq 252,13,30720
mqa 72,224,132
tq 254,13,32768
buffer_load_dwordx4 v11, s[76:79], s53 offen offset:1024 lds
mqa 76,228,132
tq 224,16,2048
mqa 68,228,128
tq 226,16,4096
mqa 80,232,128
tq 228,17,2048
s_add_u32 m0, s56, 18432
mqa 88,232,132
tq 230,17,4096
buffer_load_dwordx4 v30, s[76:79], s43 offen offset:0 lds
mqa 92,236,132
tq 232,28,2048
mqa 84,236,128
tq 234,28,4096
s_waitcnt lgkmcnt(6)
mqa 64,240,136
tq 236,29,2048
mqa 72,240,140
tq 238,29,4096
buffer_load_dwordx4 v31, s[76:79], s43 offen offset:1024 lds
mqa 76,244,140
tq 240,16,6144
mqa 68,244,136
tq 242,16,8192
mqa 80,248,136
tq 244,17,6144
mqa 88,248,140
tq 246,17,8192
mqa 92,252,140
tq 248,28,6144
mqa 84,252,136
tq 250,28,8192
s_waitcnt lgkmcnt(6)
mqa 96,224,128
tq 252,29,6144
tq 254,29,8192
mqa 104,224,132
s_waitcnt vmcnt(7)
v_cmp_ne_u32 vcc, v23, v22
s_cbranch_vccnz L_slow3_0_%=
L_back3_0_%=:
buffer_load_dwordx4 a[16:19], v15, s[28:31], s83 offen offset:0 sc1
s_barrier
mqa 108,228,132
tq 224,4,59392
mqa 100,228,128
tq 226,4,61440
buffer_load_dwordx4 a[20:23], v15, s[28:31], s83 offen offset:1024 sc1
mqa 112,232,128
tq 228,5,59392
mqa 120,232,132
tq 230,5,61440
buffer_load_dwordx4 a[24:27], v15, s[28:31], s83 offen offset:2048 sc1
mqa 124,236,132
tq 232,6,59392
mqa 116,236,128
tq 234,6,61440
s_waitcnt lgkmcnt(6)
mqa 96,240,136
tq 236,7,59392
mqa 104,240,140
tq 238,7,61440
buffer_load_dwordx4 a[28:31], v15, s[28:31], s83 offen offset:3072 sc1
mqa 108,244,140
tq 240,4,63488
mqa 100,244,136
tq 242,8,0
mqa 112,248,136
tq 244,5,63488
mqa 120,248,140
tq 246,9,0
buffer_load_dwordx4 a[48:51], v15, s[28:31], s85 offen offset:0 sc1
mqa 124,252,140
tq 248,6,63488
mqa 116,252,136
tq 250,12,0
s_waitcnt lgkmcnt(6)
mqa 64,224,160
tq 252,7,63488
mqa 72,224,164
tq 254,13,0
buffer_load_dwordx4 a[52:55], v15, s[28:31], s85 offen offset:1024 sc1
mqa 76,228,164
tq 224,8,51200
mqa 68,228,160
tq 226,8,53248
mqa 80,232,160
tq 228,9,51200
mqa 88,232,164
tq 230,9,53248
buffer_load_dwordx4 a[56:59], v15, s[28:31], s85 offen offset:2048 sc1
mqa 92,236,164
tq 232,12,51200
mqa 84,236,160
tq 234,12,53248
s_waitcnt lgkmcnt(6)
mqa 64,240,168
tq 236,13,51200
mqa 72,240,172
tq 238,13,53248
s_nop 0
L_hop7_0_%=:
s_cbranch_execz L_ret_0_%=
buffer_load_dwordx4 a[60:63], v15, s[28:31], s85 offen offset:3072 sc1
mqa 76,244,172
tq 240,8,55296
mqa 68,244,168
tq 242,8,57344
mqa 80,248,168
tq 244,9,55296
mqa 88,248,172
tq 246,9,57344
buffer_load_dwordx4 a[80:83], v15, s[28:31], s87 offen offset:0 sc1
mqa 92,252,172
tq 248,12,55296
mqa 84,252,168
tq 250,12,57344
s_waitcnt lgkmcnt(6)
mqa 96,224,160
tq 252,13,55296
tq 254,13,57344
mqa 104,224,164
s_barrier
mqa 108,228,164
tq 224,4,34816
mqa 100,228,160
tq 226,4,36864
buffer_load_dwordx4 a[84:87], v15, s[28:31], s87 offen offset:1024 sc1
mqa 112,232,160
tq 228,5,34816
mqa 120,232,164
tq 230,5,36864
buffer_load_dwordx4 a[88:91], v15, s[28:31], s87 offen offset:2048 sc1
mqa 124,236,164
tq 232,6,34816
mqa 116,236,160
tq 234,6,36864
s_waitcnt lgkmcnt(6)
mqa 96,240,168
tq 236,7,34816
mqa 104,240,172
tq 238,7,36864
buffer_load_dwordx4 a[92:95], v15, s[28:31], s87 offen offset:3072 sc1
mqa 108,244,172
tq 240,4,38912
mqa 100,244,168
tq 242,4,40960
mqa 112,248,168
tq 244,5,38912
mqa 120,248,172
tq 246,5,40960
buffer_load_dwordx4 a[112:115], v15, s[28:31], s89 offen offset:0 sc1
mqa 124,252,172
tq 248,6,38912
mqa 116,252,168
tq 250,6,40960
s_waitcnt lgkmcnt(6)
mqa 64,224,192
tq 252,7,38912
mqa 72,224,196
tq 254,7,40960
buffer_load_dwordx4 a[116:119], v15, s[28:31], s89 offen offset:1024 sc1
mqa 76,228,196
tq 224,8,2048
mqa 68,228,192
tq 226,8,4096
mqa 80,232,192
tq 228,9,2048
mqa 88,232,196
tq 230,9,4096
buffer_load_dwordx4 a[120:123], v15, s[28:31], s89 offen offset:2048 sc1
mqa 92,236,196
tq 232,12,2048
mqa 84,236,192
tq 234,12,4096
s_waitcnt lgkmcnt(6)
mqa 64,240,200
tq 236,13,2048
mqa 72,240,204
tq 238,13,4096
buffer_load_dwordx4 a[124:127], v15, s[28:31], s89 offen offset:3072 sc1
mqa 76,244,204
tq 240,8,6144
mqa 68,244,200
tq 242,8,8192
mqa 80,248,200
tq 244,9,6144
mqa 88,248,204
tq 246,9,8192
buffer_load_dwordx4 a[144:147], v15, s[28:31], s91 offen offset:0 sc1
mqa 92,252,204
tq 248,12,6144
mqa 84,252,200
tq 250,12,8192
s_waitcnt lgkmcnt(6)
mqa 96,224,192
tq 252,13,6144
tq 254,13,8192
mqa 104,224,196
s_barrier
mqa 108,228,196
tq 224,16,10240
mqa 100,228,192
tq 226,16,12288
buffer_load_dwordx4 a[148:151], v15, s[28:31], s91 offen offset:1024 sc1
mqa 112,232,192
tq 228,17,10240
mqa 120,232,196
tq 230,17,12288
buffer_load_dwordx4 a[152:155], v15, s[28:31], s91 offen offset:2048 sc1
mqa 124,236,196
tq 232,28,10240
mqa 116,236,192
tq 234,28,12288
s_waitcnt lgkmcnt(6)
mqa 96,240,200
tq 236,29,10240
mqa 104,240,204
tq 238,29,12288
buffer_load_dwordx4 a[156:159], v15, s[28:31], s91 offen offset:3072 sc1
mqa 108,244,204
tq 240,16,14336
mqa 100,244,200
tq 242,16,16384
mqa 112,248,200
tq 244,17,14336
s_add_u32 m0, s56, 124928
mqa 120,248,204
tq 246,17,16384
buffer_load_dwordx4 v30, s[76:79], s45 offen offset:0 lds
mqa 124,252,204
tq 248,28,14336
mqa 116,252,200
tq 250,28,16384
s_waitcnt lgkmcnt(6)
s_waitcnt vmcnt(17)
mqa 64,224,16
tq 252,29,14336
mqa 72,224,20
tq 254,29,16384
buffer_load_dwordx4 v31, s[76:79], s45 offen offset:1024 lds
mqa 76,228,20
tq 224,4,51200
mqa 68,228,16
tq 226,4,53248
mqa 80,232,16
tq 228,5,51200
s_add_u32 m0, s56, 10240
mqa 88,232,20
tq 230,5,53248
buffer_load_dwordx4 v30, s[76:79], s47 offen offset:0 lds
mqa 92,236,20
tq 232,6,51200
mqa 84,236,16
tq 234,6,53248
s_waitcnt lgkmcnt(6)
mqa 64,240,24
tq 236,7,51200
mqa 72,240,28
tq 238,7,53248
buffer_load_dwordx4 v31, s[76:79], s47 offen offset:1024 lds
mqa 76,244,28
tq 240,4,55296
mqa 68,244,24
tq 242,4,57344
mqa 80,248,24
tq 244,5,55296
mqa 88,248,28
tq 246,5,57344
buffer_load_dwordx4 a[176:179], v15, s[28:31], s93 offen offset:0 sc1
mqa 92,252,28
tq 248,6,55296
mqa 84,252,24
tq 250,6,57344
s_waitcnt lgkmcnt(6)
s_waitcnt vmcnt(17)
mqa 64,224,48
tq 252,7,55296
tq 254,7,57344
mqa 72,224,52
s_barrier
mqa 76,228,52
tq 224,8,18432
mqa 68,228,48
tq 226,8,20480
buffer_load_dwordx4 a[180:183], v15, s[28:31], s93 offen offset:1024 sc1
mqa 80,232,48
tq 228,9,18432
mqa 88,232,52
tq 230,9,20480
buffer_load_dwordx4 a[184:187], v15, s[28:31], s93 offen offset:2048 sc1
mqa 92,236,52
tq 232,12,18432
mqa 84,236,48
tq 234,12,20480
s_waitcnt lgkmcnt(6)
mqa 64,240,56
tq 236,13,18432
mqa 72,240,60
tq 238,13,20480
buffer_load_dwordx4 a[188:191], v15, s[28:31], s93 offen offset:3072 sc1
mqa 76,244,60
tq 240,8,22528
mqa 68,244,56
tq 242,8,24576
mqa 80,248,56
tq 244,9,22528
mqa 88,248,60
tq 246,9,24576
buffer_load_dwordx4 a[208:211], v15, s[28:31], s95 offen offset:0 sc1
mqa 92,252,60
tq 248,12,22528
mqa 84,252,56
tq 250,12,24576
s_waitcnt lgkmcnt(6)
s_waitcnt vmcnt(17)
mqa 64,224,80
tq 252,13,22528
mqa 72,224,84
tq 254,13,24576
buffer_load_dwordx4 a[212:215], v15, s[28:31], s95 offen offset:1024 sc1
mqa 76,228,84
tq 224,8,10240
mqa 68,228,80
tq 226,8,12288
buffer_load_dwordx4 a[216:219], v15, s[28:31], s95 offen offset:2048 sc1
mqa 80,232,80
tq 228,9,10240
mqa 88,232,84
tq 230,9,12288
buffer_load_dwordx4 a[220:223], v15, s[28:31], s95 offen offset:3072 sc1
mqa 92,236,84
tq 232,12,10240
mqa 84,236,80
tq 234,12,12288
s_waitcnt lgkmcnt(6)
mqa 64,240,88
tq 236,13,10240
mqa 72,240,92
tq 238,13,12288
mqa 76,244,92
tq 240,8,14336
mqa 68,244,88
tq 242,8,16384
mqa 80,248,88
tq 244,9,14336
s_add_u32 m0, s56, 149504
mqa 88,248,92
tq 246,9,16384
buffer_load_dwordx4 v30, s[76:79], s51 offen offset:0 lds
mqa 92,252,92
tq 248,12,14336
mqa 84,252,88
tq 250,12,16384
s_waitcnt lgkmcnt(6)
s_waitcnt vmcnt(17)
mqa 64,224,112
tq 252,13,14336
tq 254,13,16384
mqa 72,224,116
s_barrier
mqa 76,228,116
tq 224,4,2048
mqa 68,228,112
tq 226,4,4096
buffer_load_dwordx4 v31, s[76:79], s51 offen offset:1024 lds
mqa 80,232,112
tq 228,5,2048
s_add_u32 m0, s56, 92160
mqa 88,232,116
tq 230,5,4096
buffer_load_dwordx4 v30, s[76:79], s53 offen offset:0 lds
mqa 92,236,116
tq 232,6,2048
mqa 84,236,112
tq 234,6,4096
s_waitcnt lgkmcnt(6)
mqa 64,240,120
tq 236,7,2048
mqa 72,240,124
tq 238,7,4096
buffer_load_dwordx4 v31, s[76:79], s53 offen offset:1024 lds
mqa 76,244,124
tq 240,4,6144
mqa 68,244,120
tq 242,4,8192
mqa 80,248,120
tq 244,5,6144
s_add_u32 m0, s56, 108544
mqa 88,248,124
tq 246,5,8192
buffer_load_dwordx4 v30, s[76:79], s55 offen offset:0 lds
mqa 92,252,124
tq 248,6,6144
mqa 84,252,120
tq 250,6,8192
s_waitcnt lgkmcnt(6)
s_waitcnt vmcnt(17)
mqa 64,224,144
tq 252,7,6144
mqa 72,224,148
tq 254,7,8192
buffer_load_dwordx4 v31, s[76:79], s55 offen offset:1024 lds
mqa 76,228,148
tq 224,4,43008
mqa 68,228,144
tq 226,4,45056
mqa 80,232,144
tq 228,4,47104
mqa 88,232,148
tq 230,4,49152
mqa 92,236,148
tq 232,8,34816
mqa 84,236,144
tq 234,8,36864
s_waitcnt lgkmcnt(6)
mqa 64,240,152
tq 236,8,38912
mqa 72,240,156
tq 238,8,40960
mqa 76,244,156
tq 240,5,43008
mqa 68,244,152
tq 242,5,45056
mqa 80,248,152
tq 244,5,47104
mqa 88,248,156
tq 246,5,49152
mqa 92,252,156
tq 248,9,34816
mqa 84,252,152
tq 250,9,36864
s_waitcnt lgkmcnt(6)
s_waitcnt vmcnt(10)
mqa 64,224,176
tq 252,9,38912
tq 254,9,40960
mqa 72,224,180
s_barrier
mqa 72,228,188
tq 224,6,43008
mqa 64,228,184
tq 226,6,45056
s_waitcnt vmcnt(6)
mqa 64,232,208
tq 228,6,47104
mqa 72,232,212
tq 230,6,49152
mqa 72,236,220
tq 232,12,34816
mqa 64,236,216
tq 234,12,36864
s_waitcnt lgkmcnt(6)
mqa 68,240,176
tq 236,12,38912
mqa 76,240,180
tq 238,12,40960
mqa 76,244,188
tq 240,7,43008
v_fma_f32 v200, v136, v72, v200
mqa 68,244,184
tq 242,7,45056
v_fma_f32 v201, v137, v73, v201
mqa 68,248,208
tq 244,7,47104
v_fma_f32 v202, v138, v74, v202
mqa 76,248,212
tq 246,7,49152
v_fma_f32 v203, v139, v75, v203
mqa 76,252,220
tq 248,13,34816
v_fma_f32 v192, v128, v64, v192
mqa 68,252,216
tq 250,13,36864
s_waitcnt lgkmcnt(6)
mqa 80,224,176
tq 252,13,38912
v_fma_f32 v193, v129, v65, v193
mqa 88,224,180
tq 254,13,40960
v_fma_f32 v194, v130, v66, v194
mqa 88,228,188
tq 224,4,18432
v_fma_f32 v195, v131, v67, v195
mqa 80,228,184
tq 226,4,20480
v_cvt_pk_bf16_f32 v36, v200, v201
mqa 80,232,208
tq 228,4,22528
v_cvt_pk_bf16_f32 v37, v202, v203
mqa 88,232,212
tq 230,4,24576
v_cvt_pk_bf16_f32 v32, v192, v193
mqa 88,236,220
tq 232,8,59392
v_cvt_pk_bf16_f32 v33, v194, v195
mqa 80,236,216
tq 234,8,61440
s_waitcnt lgkmcnt(6)
mqa 84,240,176
tq 236,8,63488
v_fma_f32 v204, v140, v76, v204
mqa 92,240,180
tq 238,16,0
v_fma_f32 v205, v141, v77, v205
mqa 92,244,188
tq 240,4,10240
v_fma_f32 v206, v142, v78, v206
mqa 84,244,184
tq 242,4,12288
v_fma_f32 v207, v143, v79, v207
mqa 84,248,208
tq 244,4,14336
v_fma_f32 v196, v132, v68, v196
mqa 92,248,212
tq 246,4,16384
v_fma_f32 v197, v133, v69, v197
mqa 92,252,220
tq 248,4,26624
v_fma_f32 v198, v134, v70, v198
mqa 84,252,216
tq 250,4,28672
s_waitcnt lgkmcnt(6)
mqa 96,224,16
tq 252,4,30720
tq 254,4,32768
mqa 104,224,20
s_waitcnt vmcnt(0)
s_barrier
mqa 104,228,28
tq 224,16,18432
v_fma_f32 v199, v135, v71, v199
v_cvt_pk_bf16_f32 v38, v204, v205
mqa 96,228,24
tq 226,16,20480
v_cvt_pk_bf16_f32 v39, v206, v207
mqa 96,232,48
tq 228,16,22528
v_permlane16_swap_b32 v36, v38
v_permlane16_swap_b32 v37, v39
mqa 104,232,52
tq 230,16,24576
buffer_store_dwordx4 v[36:39], v18, s[32:35], s70 offen offset:0 sc1
mqa 104,236,60
tq 232,8,26624
v_cvt_pk_bf16_f32 v34, v196, v197
v_cvt_pk_bf16_f32 v35, v198, v199
mqa 96,236,56
tq 234,8,28672
s_waitcnt lgkmcnt(6)
mqa 96,240,80
tq 236,8,30720
v_permlane16_swap_b32 v32, v34
mqa 104,240,84
tq 238,8,32768
v_permlane16_swap_b32 v33, v35
mqa 104,244,92
tq 240,8,43008
mqa 96,244,88
tq 242,8,45056
buffer_store_dwordx4 v[32:35], v18, s[32:35], 0 offen offset:0 sc1
v_fma_f32 v216, v152, v88, v216
mqa 96,248,112
tq 244,8,47104
v_fma_f32 v217, v153, v89, v217
mqa 104,248,116
tq 246,8,49152
v_fma_f32 v218, v154, v90, v218
mqa 104,252,124
tq 248,5,18432
v_fma_f32 v219, v155, v91, v219
mqa 96,252,120
tq 250,5,20480
s_waitcnt lgkmcnt(6)
mqa 96,224,144
tq 252,5,22528
v_fma_f32 v208, v144, v80, v208
mqa 104,224,148
tq 254,5,24576
v_fma_f32 v209, v145, v81, v209
mqa 104,228,156
tq 224,9,59392
v_fma_f32 v210, v146, v82, v210
mqa 96,228,152
tq 226,9,61440
v_fma_f32 v211, v147, v83, v211
mqa 96,232,176
tq 228,9,63488
v_cvt_pk_bf16_f32 v44, v216, v217
mqa 104,232,180
tq 230,17,0
v_cvt_pk_bf16_f32 v45, v218, v219
mqa 104,236,188
tq 232,5,10240
v_cvt_pk_bf16_f32 v40, v208, v209
mqa 96,236,184
tq 234,5,12288
s_waitcnt lgkmcnt(6)
mqa 96,240,208
tq 236,5,14336
v_cvt_pk_bf16_f32 v41, v210, v211
mqa 104,240,212
)ASM"
R"ASM(
tq 238,5,16384
v_fma_f32 v220, v156, v92, v220
mqa 104,244,220
tq 240,5,26624
v_fma_f32 v221, v157, v93, v221
mqa 96,244,216
tq 242,5,28672
v_fma_f32 v222, v158, v94, v222
mqa 100,248,16
tq 244,5,30720
v_fma_f32 v223, v159, v95, v223
mqa 108,248,20
tq 246,5,32768
v_fma_f32 v212, v148, v84, v212
mqa 108,252,28
tq 248,17,18432
v_fma_f32 v213, v149, v85, v213
mqa 100,252,24
tq 250,17,20480
s_waitcnt lgkmcnt(6)
mqa 100,224,48
tq 252,17,22528
tq 254,17,24576
mqa 108,224,52
s_barrier
mqa 108,228,60
tq 224,9,26624
v_fma_f32 v214, v150, v86, v214
mqa 100,228,56
tq 226,9,28672
v_fma_f32 v215, v151, v87, v215
mqa 100,232,80
tq 228,9,30720
v_cvt_pk_bf16_f32 v46, v220, v221
s_nop 0
L_hop8_0_%=:
s_cbranch_execz L_ret_0_%=
mqa 108,232,84
tq 230,9,32768
v_cvt_pk_bf16_f32 v47, v222, v223
mqa 108,236,92
tq 232,9,43008
v_permlane16_swap_b32 v44, v46
mqa 100,236,88
tq 234,9,45056
s_waitcnt lgkmcnt(6)
mqa 100,240,112
tq 236,9,47104
v_permlane16_swap_b32 v45, v47
mqa 108,240,116
tq 238,9,49152
buffer_store_dwordx4 v[44:47], v18, s[32:35], s70 offen offset:64 sc1
mqa 108,244,124
tq 240,6,18432
v_cvt_pk_bf16_f32 v42, v212, v213
mqa 100,244,120
tq 242,6,20480
v_cvt_pk_bf16_f32 v43, v214, v215
mqa 100,248,144
tq 244,6,22528
v_permlane16_swap_b32 v40, v42
mqa 108,248,148
tq 246,6,24576
v_permlane16_swap_b32 v41, v43
mqa 108,252,156
tq 248,12,59392
mqa 100,252,152
tq 250,12,61440
buffer_store_dwordx4 v[40:43], v18, s[32:35], 0 offen offset:64 sc1
s_waitcnt lgkmcnt(6)
mqa 100,224,176
tq 252,12,63488
v_fma_f32 v232, v168, v104, v232
mqa 108,224,180
tq 254,28,0
v_fma_f32 v233, v169, v105, v233
mqa 108,228,188
tq 224,6,10240
v_fma_f32 v234, v170, v106, v234
mqa 100,228,184
tq 226,6,12288
v_fma_f32 v235, v171, v107, v235
mqa 100,232,208
tq 228,6,14336
v_fma_f32 v224, v160, v96, v224
mqa 108,232,212
tq 230,6,16384
v_fma_f32 v225, v161, v97, v225
mqa 108,236,220
tq 232,6,26624
v_fma_f32 v226, v162, v98, v226
mqa 100,236,216
tq 234,6,28672
s_waitcnt lgkmcnt(6)
mqa 112,240,16
tq 236,6,30720
v_fma_f32 v227, v163, v99, v227
mqa 120,240,20
tq 238,6,32768
v_cvt_pk_bf16_f32 v52, v232, v233
mqa 120,244,28
tq 240,28,18432
v_cvt_pk_bf16_f32 v53, v234, v235
mqa 112,244,24
tq 242,28,20480
v_cvt_pk_bf16_f32 v48, v224, v225
mqa 112,248,48
tq 244,28,22528
v_cvt_pk_bf16_f32 v49, v226, v227
mqa 120,248,52
tq 246,28,24576
v_fma_f32 v236, v172, v108, v236
mqa 120,252,60
tq 248,12,26624
v_fma_f32 v237, v173, v109, v237
mqa 112,252,56
tq 250,12,28672
s_waitcnt lgkmcnt(6)
mqa 112,224,80
tq 252,12,30720
tq 254,12,32768
mqa 120,224,84
s_barrier
mqa 120,228,92
tq 224,12,43008
v_fma_f32 v238, v174, v110, v238
mqa 112,228,88
tq 226,12,45056
v_fma_f32 v239, v175, v111, v239
mqa 112,232,112
tq 228,12,47104
v_fma_f32 v228, v164, v100, v228
mqa 120,232,116
tq 230,12,49152
v_fma_f32 v229, v165, v101, v229
mqa 120,236,124
tq 232,7,18432
v_fma_f32 v230, v166, v102, v230
mqa 112,236,120
tq 234,7,20480
s_waitcnt lgkmcnt(6)
mqa 112,240,144
tq 236,7,22528
v_fma_f32 v231, v167, v103, v231
mqa 120,240,148
tq 238,7,24576
v_cvt_pk_bf16_f32 v54, v236, v237
mqa 120,244,156
tq 240,13,59392
v_cvt_pk_bf16_f32 v55, v238, v239
mqa 112,244,152
tq 242,13,61440
v_permlane16_swap_b32 v52, v54
mqa 112,248,176
tq 244,13,63488
v_permlane16_swap_b32 v53, v55
mqa 120,248,180
tq 246,29,0
buffer_store_dwordx4 v[52:55], v3, s[32:35], s70 offen offset:0 sc1
mqa 120,252,188
tq 248,7,10240
v_cvt_pk_bf16_f32 v50, v228, v229
mqa 112,252,184
tq 250,7,12288
s_waitcnt lgkmcnt(6)
mqa 112,224,208
tq 252,7,14336
v_cvt_pk_bf16_f32 v51, v230, v231
mqa 120,224,212
tq 254,7,16384
v_permlane16_swap_b32 v48, v50
mqa 120,228,220
tq 224,7,26624
v_permlane16_swap_b32 v49, v51
mqa 112,228,216
tq 226,7,28672
buffer_store_dwordx4 v[48:51], v3, s[32:35], 0 offen offset:0 sc1
mqa 116,232,16
tq 228,7,30720
mqa 124,232,20
tq 230,7,32768
mqa 124,236,28
tq 232,29,18432
v_fma_f32 v248, v184, v120, v248
mqa 116,236,24
tq 234,29,20480
s_waitcnt lgkmcnt(6)
mqa 116,240,48
tq 236,29,22528
v_fma_f32 v249, v185, v121, v249
mqa 124,240,52
tq 238,29,24576
v_fma_f32 v250, v186, v122, v250
mqa 124,244,60
tq 240,13,26624
v_fma_f32 v251, v187, v123, v251
mqa 116,244,56
tq 242,13,28672
v_fma_f32 v240, v176, v112, v240
mqa 116,248,80
tq 244,13,30720
v_fma_f32 v241, v177, v113, v241
mqa 124,248,84
tq 246,13,32768
v_fma_f32 v242, v178, v114, v242
mqa 124,252,92
tq 248,13,43008
v_fma_f32 v243, v179, v115, v243
mqa 116,252,88
tq 250,13,45056
s_waitcnt lgkmcnt(6)
mqa 124,224,116
tq 252,13,47104
v_cvt_pk_bf16_f32 v60, v248, v249
mqa 124,228,124
tq 254,13,49152
v_cvt_pk_bf16_f32 v61, v250, v251
mqa 124,232,148
v_cvt_pk_bf16_f32 v56, v240, v241
v_cvt_pk_bf16_f32 v57, v242, v243
mqa 124,236,156
s_waitcnt lgkmcnt(4)
mqa 124,240,180
mqa 124,244,188
s_waitcnt lgkmcnt(0)
mqa 124,248,212
mqa 124,252,220
mqa 116,224,112
mqa 116,228,120
mqa 116,232,144
s_nop 4
v_fma_f32 v252, v188, v124, v252
v_fma_f32 v253, v189, v125, v253
mqa 116,236,152
v_fma_f32 v254, v190, v126, v254
v_fma_f32 v255, v191, v127, v255
mqa 116,240,176
v_cvt_pk_bf16_f32 v62, v252, v253
v_cvt_pk_bf16_f32 v63, v254, v255
mqa 116,244,184
v_permlane16_swap_b32 v60, v62
v_permlane16_swap_b32 v61, v63
mqa 116,248,208
buffer_store_dwordx4 v[60:63], v3, s[32:35], s70 offen offset:64 sc1
mqa 116,252,216
s_nop 7
v_fma_f32 v244, v180, v116, v244
v_fma_f32 v245, v181, v117, v245
v_fma_f32 v246, v182, v118, v246
v_fma_f32 v247, v183, v119, v247
v_cvt_pk_bf16_f32 v58, v244, v245
v_cvt_pk_bf16_f32 v59, v246, v247
s_nop 0
v_permlane16_swap_b32 v56, v58
v_permlane16_swap_b32 v57, v59
buffer_store_dwordx4 v[56:59], v3, s[32:35], 0 offen offset:64 sc1
s_endpgm
L_slow0_0_%=:
s_mov_b32 s62, 0
L_loop0_0_%=:
s_and_b32 s59, vcc_lo, 0xff00
s_cbranch_scc0 L_norep0_0_%=
v_cndmask_b32 v22, v22, v23, s[60:61]
buffer_store_dword v22, v21, s[28:31], 0 offen offset:0 sc1
L_norep0_0_%=:
buffer_load_dword v23, v20, s[28:31], 0 offen offset:0 sc1
s_waitcnt vmcnt(0)
v_cmp_ne_u32 vcc, v23, v22
s_cbranch_vccz L_back0_0_%=
s_add_u32 s62, s62, 1
s_cmp_lt_u32 s62, 100000
s_cbranch_scc1 L_loop0_0_%=
s_branch L_back0_0_%=
L_slow1_0_%=:
s_mov_b32 s62, 0
L_loop1_0_%=:
s_and_b32 s59, vcc_lo, 0xff00
s_cbranch_scc0 L_norep1_0_%=
v_cndmask_b32 v22, v22, v23, s[60:61]
buffer_store_dword v22, v21, s[28:31], 0 offen offset:0 sc1
buffer_store_dword v22, v21, s[28:31], 0 offen offset:32 sc1
L_norep1_0_%=:
buffer_load_dword v23, v20, s[28:31], 0 offen offset:32 sc1
s_waitcnt vmcnt(0)
v_cmp_ne_u32 vcc, v23, v22
s_cbranch_vccz L_back1_0_%=
s_add_u32 s62, s62, 1
s_cmp_lt_u32 s62, 100000
s_cbranch_scc1 L_loop1_0_%=
s_branch L_back1_0_%=
L_slow2_0_%=:
s_mov_b32 s62, 0
L_loop2_0_%=:
s_and_b32 s59, vcc_lo, 0xff00
s_cbranch_scc0 L_norep2_0_%=
v_cndmask_b32 v22, v22, v23, s[60:61]
buffer_store_dword v22, v21, s[28:31], 0 offen offset:0 sc1
buffer_store_dword v22, v21, s[28:31], 0 offen offset:32 sc1
buffer_store_dword v22, v21, s[28:31], 0 offen offset:64 sc1
L_norep2_0_%=:
buffer_load_dword v23, v20, s[28:31], 0 offen offset:64 sc1
s_waitcnt vmcnt(0)
v_cmp_ne_u32 vcc, v23, v22
s_cbranch_vccz L_back2_0_%=
s_add_u32 s62, s62, 1
s_cmp_lt_u32 s62, 100000
s_cbranch_scc1 L_loop2_0_%=
s_branch L_back2_0_%=
L_slow3_0_%=:
s_mov_b32 s62, 0
L_loop3_0_%=:
s_and_b32 s59, vcc_lo, 0xff00
s_cbranch_scc0 L_norep3_0_%=
v_cndmask_b32 v22, v22, v23, s[60:61]
buffer_store_dword v22, v21, s[28:31], 0 offen offset:0 sc1
buffer_store_dword v22, v21, s[28:31], 0 offen offset:32 sc1
buffer_store_dword v22, v21, s[28:31], 0 offen offset:64 sc1
buffer_store_dword v22, v21, s[28:31], 0 offen offset:96 sc1
L_norep3_0_%=:
buffer_load_dword v23, v20, s[28:31], 0 offen offset:96 sc1
s_waitcnt vmcnt(0)
v_cmp_ne_u32 vcc, v23, v22
s_cbranch_vccz L_back3_0_%=
s_add_u32 s62, s62, 1
s_cmp_lt_u32 s62, 100000
s_cbranch_scc1 L_loop3_0_%=
s_branch L_back3_0_%=
.p2align 12
L_var1_%=:
L_ret_1_%=:
s_mov_b64 exec, -1
s_bfe_u32 s46, s64, 262152
s_lshl_b32 s46, s46, 17
s_xor_b32 s47, s46, 0x20000
s_add_u32 m0, s56, 26624
s_nop 0
buffer_load_dwordx4 v10, s[20:23], s46 offen offset:0 lds
buffer_load_dwordx4 v11, s[20:23], s46 offen offset:1024 lds
buffer_load_dword v25, v19, s[20:23], 0 offen
buffer_load_dword v25, v228, s[24:27], 0 offen
buffer_load_dword v229, v224, s[28:31], 0 offen sc1
s_waitcnt vmcnt(20)
v_lshlrev_b32 v231, 16, v230
v_and_b32 v23, 0xffff0000, v230
ds_write_b32 v227, v231
ds_write_b32 v227, v23 offset:4
s_waitcnt vmcnt(16)
ds_read_b64 a[0:1], v252 offset:0
ds_read_b64 a[2:3], v253 offset:0
ds_read_b64 a[4:5], v252 offset:2048
ds_read_b64 a[6:7], v253 offset:2048
ds_read_b64 a[8:9], v254 offset:0
ds_read_b64 a[10:11], v255 offset:0
ds_read_b64 a[12:13], v254 offset:2048
ds_read_b64 a[14:15], v255 offset:2048
s_waitcnt lgkmcnt(8)
s_waitcnt vmcnt(12)
s_barrier
ds_read_b128 v[64:67], v24 offset:0
ds_read_b128 v[68:71], v24 offset:64
ds_read_b128 v[72:75], v24 offset:0
ds_read_b128 v[76:79], v24 offset:64
ds_read_b128 v[80:83], v24 offset:128
ds_read_b128 v[84:87], v24 offset:192
ds_read_b128 v[88:91], v24 offset:128
ds_read_b128 v[92:95], v24 offset:192
tq 224,4,2048
tq 226,4,4096
tq 228,5,2048
tq 230,5,4096
tq 232,4,6144
tq 234,4,8192
tq 236,5,6144
tq 238,5,8192
tq 240,6,2048
tq 242,6,4096
tq 244,7,2048
tq 246,7,4096
tq 248,6,6144
tq 250,6,8192
s_nop 1
s_waitcnt lgkmcnt(6)
mqa 64,224,0
tq 252,7,6144
s_lshr_b32 s58, s46, 10
s_add_u32 m0, s57, 116736
mqa 72,224,4
tq 254,7,8192
buffer_load_dwordx4 v246, s[24:27], s58 offen offset:0 lds
mqa 76,228,4
tq 224,4,10240
ds_read_b128 v[96:99], v24 offset:256
ds_read_b128 v[100:103], v24 offset:320
ds_read_b128 v[104:107], v24 offset:256
mqa 68,228,0
tq 226,4,12288
buffer_load_dwordx4 v247, s[24:27], s58 offen offset:1024 lds
ds_read_b128 v[108:111], v24 offset:320
mqa 64,232,8
tq 228,5,10240
ds_read_b128 v[112:115], v24 offset:384
ds_read_b128 v[116:119], v24 offset:448
ds_read_b128 v[120:123], v24 offset:384
mqa 72,232,12
tq 230,5,12288
buffer_load_dwordx4 v248, s[24:27], s58 offen offset:2048 lds
ds_read_b128 v[124:127], v24 offset:448
mqa 76,236,12
tq 232,4,14336
mqa 68,236,8
tq 234,4,16384
buffer_load_dwordx4 v249, s[24:27], s58 offen offset:3072 lds
s_waitcnt lgkmcnt(14)
mqa 80,240,0
tq 236,5,14336
s_add_u32 m0, s56, 34816
mqa 88,240,4
tq 238,5,16384
buffer_load_dwordx4 v30, s[20:23], s46 offen offset:0 lds
mqa 92,244,4
tq 240,6,10240
mqa 84,244,0
tq 242,6,12288
buffer_load_dwordx4 v31, s[20:23], s46 offen offset:1024 lds
s_waitcnt vmcnt(13)
ds_read_b64 a[32:33], v252 offset:4096
ds_read_b64 a[34:35], v253 offset:4096
mqa 80,248,8
tq 244,7,10240
s_bfe_u32 s48, s64, 262156
s_lshl_b32 s48, s48, 17
s_xor_b32 s49, s48, 0x20000
s_add_u32 m0, s56, 43008
mqa 88,248,12
tq 246,7,12288
buffer_load_dwordx4 v10, s[20:23], s48 offen offset:0 lds
ds_read_b64 a[36:37], v252 offset:6144
ds_read_b64 a[38:39], v253 offset:6144
mqa 92,252,12
tq 248,6,14336
ds_read_b64 a[40:41], v254 offset:4096
ds_read_b64 a[42:43], v255 offset:4096
mqa 84,252,8
tq 250,6,16384
buffer_load_dwordx4 v11, s[20:23], s48 offen offset:1024 lds
s_waitcnt lgkmcnt(12)
mqa 96,224,0
tq 252,7,14336
tq 254,7,16384
ds_read_b64 a[44:45], v254 offset:6144
ds_read_b64 a[46:47], v255 offset:6144
mqa 104,224,4
s_lshr_b32 s58, s48, 10
s_add_u32 m0, s57, 108544
s_nop 0
buffer_load_dwordx4 v246, s[24:27], s58 offen offset:0 lds
s_waitcnt vmcnt(12)
s_barrier
mqa 108,228,4
tq 224,4,18432
mqa 100,228,0
tq 226,4,20480
buffer_load_dwordx4 v247, s[24:27], s58 offen offset:1024 lds
mqa 96,232,8
tq 228,5,18432
mqa 104,232,12
tq 230,5,20480
buffer_load_dwordx4 v248, s[24:27], s58 offen offset:2048 lds
mqa 108,236,12
tq 232,6,18432
mqa 100,236,8
tq 234,6,20480
buffer_load_dwordx4 v249, s[24:27], s58 offen offset:3072 lds
s_waitcnt lgkmcnt(8)
mqa 112,240,0
tq 236,7,18432
mqa 120,240,4
tq 238,7,20480
mqa 124,244,4
tq 240,4,22528
mqa 116,244,0
s_waitcnt lgkmcnt(9)
s_lshr_b32 s58, s40, 10
s_add_u32 m0, s57, 112640
tq 242,4,24576
buffer_load_dwordx4 v246, s[24:27], s58 offen offset:0 lds
s_waitcnt vmcnt(9)
ds_read_b64 a[64:65], v252 offset:8192
ds_read_b64 a[66:67], v253 offset:8192
mqa 112,248,8
tq 244,5,22528
ds_read_b64 a[68:69], v252 offset:10240
ds_read_b64 a[70:71], v253 offset:10240
mqa 120,248,12
tq 246,5,24576
buffer_load_dwordx4 v247, s[24:27], s58 offen offset:1024 lds
mqa 124,252,12
tq 248,6,22528
ds_read_b64 a[72:73], v254 offset:8192
ds_read_b64 a[74:75], v255 offset:8192
mqa 116,252,8
tq 250,6,24576
buffer_load_dwordx4 v248, s[24:27], s58 offen offset:2048 lds
s_waitcnt lgkmcnt(12)
mqa 64,224,32
tq 252,7,22528
ds_read_b64 a[76:77], v254 offset:10240
ds_read_b64 a[78:79], v255 offset:10240
mqa 72,224,36
tq 254,7,24576
buffer_load_dwordx4 v249, s[24:27], s58 offen offset:3072 lds
mqa 76,228,36
tq 224,4,26624
s_add_u32 m0, s56, 51200
mqa 68,228,32
tq 226,4,28672
buffer_load_dwordx4 v10, s[20:23], s40 offen offset:0 lds
mqa 80,232,32
tq 228,5,26624
mqa 88,232,36
tq 230,5,28672
buffer_load_dwordx4 v11, s[20:23], s40 offen offset:1024 lds
mqa 92,236,36
tq 232,6,26624
s_add_u32 m0, s56, 59392
mqa 84,236,32
tq 234,6,28672
buffer_load_dwordx4 v30, s[20:23], s40 offen offset:0 lds
s_waitcnt lgkmcnt(6)
mqa 64,240,40
tq 236,7,26624
mqa 72,240,44
tq 238,7,28672
buffer_load_dwordx4 v31, s[20:23], s40 offen offset:1024 lds
mqa 76,244,44
tq 240,4,30720
mqa 68,244,40
tq 242,4,32768
mqa 80,248,40
tq 244,5,30720
mqa 88,248,44
tq 246,5,32768
mqa 92,252,44
tq 248,6,30720
mqa 84,252,40
tq 250,6,32768
s_waitcnt lgkmcnt(6)
mqa 64,224,64
tq 252,7,30720
tq 254,7,32768
mqa 72,224,68
v_readlane_b32 s63, v229, 32
L_nonce_1_%=:
s_add_u32 s63, s63, 1
v_cmp_eq_u32 vcc, s63, v229
s_cbranch_vccnz L_nonce_1_%=
v_mov_b32 v230, s63
s_mov_b32 exec_lo, 0
s_mov_b32 exec_hi, 15
buffer_store_dword v230, v224, s[28:31], 0 offen sc1
s_mov_b64 exec, -1
v_and_b32 v22, v22, v230
s_waitcnt vmcnt(13)
s_barrier
mqa 76,228,68
tq 224,4,34816
mqa 68,228,64
tq 226,4,36864
mqa 80,232,64
tq 228,5,34816
mqa 88,232,68
tq 230,5,36864
mqa 92,236,68
tq 232,6,34816
mqa 84,236,64
tq 234,6,36864
s_waitcnt lgkmcnt(6)
mqa 64,240,72
tq 236,7,34816
mqa 72,240,76
tq 238,7,36864
mqa 76,244,76
tq 240,4,38912
s_bfe_u32 s50, s64, 262160
s_lshl_b32 s50, s50, 17
s_xor_b32 s51, s50, 0x20000
mqa 68,244,72
s_lshr_b32 s58, s50, 10
s_add_u32 m0, s57, 116736
tq 242,4,40960
buffer_load_dwordx4 v246, s[24:27], s58 offen offset:0 lds
s_waitcnt vmcnt(10)
ds_read_b64 a[96:97], v252 offset:0
ds_read_b64 a[98:99], v253 offset:0
mqa 80,248,72
tq 244,5,38912
ds_read_b64 a[100:101], v252 offset:2048
ds_read_b64 a[102:103], v253 offset:2048
mqa 88,248,76
tq 246,5,40960
buffer_load_dwordx4 v247, s[24:27], s58 offen offset:1024 lds
mqa 92,252,76
tq 248,6,38912
ds_read_b64 a[104:105], v254 offset:0
ds_read_b64 a[106:107], v255 offset:0
mqa 84,252,72
tq 250,6,40960
buffer_load_dwordx4 v248, s[24:27], s58 offen offset:2048 lds
s_waitcnt lgkmcnt(12)
mqa 96,224,64
tq 252,7,38912
ds_read_b64 a[108:109], v254 offset:2048
ds_read_b64 a[110:111], v255 offset:2048
mqa 104,224,68
tq 254,7,40960
buffer_load_dwordx4 v249, s[24:27], s58 offen offset:3072 lds
mqa 108,228,68
tq 224,4,43008
s_add_u32 m0, s56, 67584
mqa 100,228,64
tq 226,4,45056
buffer_load_dwordx4 v10, s[20:23], s50 offen offset:0 lds
mqa 112,232,64
tq 228,5,43008
mqa 120,232,68
tq 230,5,45056
buffer_load_dwordx4 v11, s[20:23], s50 offen offset:1024 lds
mqa 124,236,68
tq 232,6,43008
s_bfe_u32 s52, s64, 262164
s_lshl_b32 s52, s52, 17
s_xor_b32 s53, s52, 0x20000
mqa 116,236,64
s_add_u32 m0, s56, 75776
tq 234,6,45056
buffer_load_dwordx4 v10, s[20:23], s52 offen offset:0 lds
s_waitcnt lgkmcnt(6)
mqa 96,240,72
tq 236,7,43008
mqa 104,240,76
tq 238,7,45056
buffer_load_dwordx4 v11, s[20:23], s52 offen offset:1024 lds
mqa 108,244,76
tq 240,4,47104
mqa 100,244,72
s_lshr_b32 s58, s52, 10
s_add_u32 m0, s57, 108544
tq 242,4,49152
buffer_load_dwordx4 v246, s[24:27], s58 offen offset:0 lds
s_waitcnt vmcnt(14)
ds_read_b64 v[32:33], v252 offset:4096
ds_read_b64 v[34:35], v253 offset:4096
mqa 112,248,72
tq 244,5,47104
ds_read_b64 v[36:37], v252 offset:6144
ds_read_b64 v[38:39], v253 offset:6144
mqa 120,248,76
tq 246,5,49152
buffer_load_dwordx4 v247, s[24:27], s58 offen offset:1024 lds
mqa 124,252,76
tq 248,6,47104
ds_read_b64 v[40:41], v254 offset:4096
ds_read_b64 v[42:43], v255 offset:4096
mqa 116,252,72
tq 250,6,49152
buffer_load_dwordx4 v248, s[24:27], s58 offen offset:2048 lds
s_waitcnt lgkmcnt(12)
mqa 64,224,96
tq 252,7,47104
tq 254,7,49152
ds_read_b64 v[44:45], v254 offset:6144
ds_read_b64 v[46:47], v255 offset:6144
mqa 72,224,100
buffer_load_dwordx4 v249, s[24:27], s58 offen offset:3072 lds
s_waitcnt vmcnt(13)
s_barrier
mqa 76,228,100
tq 224,4,51200
mqa 68,228,96
tq 226,4,53248
mqa 80,232,96
tq 228,5,51200
mqa 88,232,100
tq 230,5,53248
mqa 92,236,100
tq 232,6,51200
mqa 84,236,96
tq 234,6,53248
s_waitcnt lgkmcnt(8)
mqa 64,240,104
tq 236,7,51200
mqa 72,240,108
tq 238,7,53248
mqa 76,244,108
tq 240,4,55296
s_bfe_u32 s54, s64, 262168
s_lshl_b32 s54, s54, 17
s_xor_b32 s55, s54, 0x20000
mqa 68,244,104
s_waitcnt lgkmcnt(9)
s_lshr_b32 s58, s54, 10
s_add_u32 m0, s57, 112640
tq 242,4,57344
buffer_load_dwordx4 v246, s[24:27], s58 offen offset:0 lds
mqa 80,248,104
tq 244,5,55296
mqa 88,248,108
tq 246,5,57344
buffer_load_dwordx4 v247, s[24:27], s58 offen offset:1024 lds
mqa 92,252,108
tq 248,6,55296
mqa 84,252,104
tq 250,6,57344
buffer_load_dwordx4 v248, s[24:27], s58 offen offset:2048 lds
s_waitcnt lgkmcnt(6)
mqv 64,224,32
tq 252,7,55296
mqv 72,224,36
tq 254,7,57344
buffer_load_dwordx4 v249, s[24:27], s58 offen offset:3072 lds
mqv 76,228,36
tq 224,4,59392
s_add_u32 m0, s56, 83968
mqv 68,228,32
tq 226,4,61440
buffer_load_dwordx4 v10, s[20:23], s54 offen offset:0 lds
mqv 80,232,32
tq 228,5,59392
v_lshlrev_b32 v128, 16, v32
mqv 88,232,36
tq 230,5,61440
buffer_load_dwordx4 v11, s[20:23], s54 offen offset:1024 lds
mqv 92,236,36
tq 232,6,59392
s_add_u32 m0, s56, 92160
mqv 84,236,32
tq 234,6,61440
buffer_load_dwordx4 v10, s[20:23], s41 offen offset:0 lds
s_waitcnt lgkmcnt(6)
mqv 64,240,40
tq 236,7,59392
v_and_b32 v129, 0xffff0000, v32
mqv 72,240,44
tq 238,7,61440
buffer_load_dwordx4 v11, s[20:23], s41 offen offset:1024 lds
mqv 76,244,44
tq 240,4,63488
mqv 68,244,40
tq 242,8,0
s_waitcnt vmcnt(16)
ds_read_b64 a[128:129], v252 offset:8192
ds_read_b64 a[130:131], v253 offset:8192
mqv 80,248,40
tq 244,5,63488
ds_read_b64 a[132:133], v252 offset:10240
mqv 88,248,44
tq 246,9,0
ds_read_b64 a[134:135], v253 offset:10240
mqv 92,252,44
tq 248,6,63488
ds_read_b64 a[136:137], v254 offset:8192
ds_read_b64 a[138:139], v255 offset:8192
mqv 84,252,40
tq 250,12,0
s_waitcnt lgkmcnt(12)
mqv 96,224,32
tq 252,7,63488
tq 254,13,0
ds_read_b64 a[140:141], v254 offset:10240
ds_read_b64 a[142:143], v255 offset:10240
mqv 104,224,36
s_waitcnt vmcnt(12)
s_barrier
mqv 108,228,36
tq 224,8,2048
v_lshlrev_b32 v130, 16, v33
mqv 100,228,32
tq 226,8,4096
v_and_b32 v131, 0xffff0000, v33
mqv 112,232,32
tq 228,9,2048
v_lshlrev_b32 v132, 16, v34
mqv 120,232,36
tq 230,9,4096
v_and_b32 v133, 0xffff0000, v34
mqv 124,236,36
tq 232,12,2048
mqv 116,236,32
s_waitcnt lgkmcnt(5)
s_lshr_b32 s58, s41, 10
s_add_u32 m0, s57, 116736
tq 234,12,4096
buffer_load_dwordx4 v246, s[24:27], s58 offen offset:0 lds
mqv 96,240,40
tq 236,13,2048
v_lshlrev_b32 v134, 16, v35
mqv 104,240,44
tq 238,13,4096
buffer_load_dwordx4 v247, s[24:27], s58 offen offset:1024 lds
mqv 108,244,44
tq 240,8,6144
mqv 100,244,40
tq 242,8,8192
buffer_load_dwordx4 v248, s[24:27], s58 offen offset:2048 lds
s_waitcnt vmcnt(11)
ds_read_b64 a[160:161], v252 offset:0
ds_read_b64 a[162:163], v253 offset:0
mqv 112,248,40
tq 244,9,6144
ds_read_b64 a[164:165], v252 offset:2048
ds_read_b64 a[166:167], v253 offset:2048
mqv 120,248,44
tq 246,9,8192
buffer_load_dwordx4 v249, s[24:27], s58 offen offset:3072 lds
mqv 124,252,44
tq 248,12,6144
ds_read_b64 a[168:169], v254 offset:0
ds_read_b64 a[170:171], v255 offset:0
mqv 116,252,40
tq 250,12,8192
s_waitcnt lgkmcnt(12)
mqa 64,224,128
tq 252,13,6144
s_add_u32 m0, s56, 100352
ds_read_b64 a[172:173], v254 offset:2048
mqa 72,224,132
tq 254,13,8192
buffer_load_dwordx4 v30, s[20:23], s41 offen offset:0 lds
ds_read_b64 a[174:175], v255 offset:2048
mqa 76,228,132
tq 224,8,10240
v_and_b32 v135, 0xffff0000, v35
mqa 68,228,128
tq 226,8,12288
buffer_load_dwordx4 v31, s[20:23], s41 offen offset:1024 lds
mqa 80,232,128
tq 228,9,10240
s_add_u32 m0, s56, 2048
mqa 88,232,132
tq 230,9,12288
buffer_load_dwordx4 v10, s[20:23], s43 offen offset:0 lds
mqa 92,236,132
tq 232,12,10240
mqa 84,236,128
tq 234,12,12288
buffer_load_dwordx4 v11, s[20:23], s43 offen offset:1024 lds
s_waitcnt lgkmcnt(7)
mqa 64,240,136
tq 236,13,10240
v_lshlrev_b32 v136, 16, v36
mqa 72,240,140
tq 238,13,12288
v_and_b32 v137, 0xffff0000, v36
mqa 76,244,140
tq 240,8,14336
mqa 68,244,136
s_waitcnt lgkmcnt(9)
s_lshr_b32 s58, s43, 10
s_add_u32 m0, s57, 108544
tq 242,8,16384
buffer_load_dwordx4 v246, s[24:27], s58 offen offset:0 lds
s_waitcnt vmcnt(13)
ds_read_b64 a[192:193], v252 offset:4096
ds_read_b64 a[194:195], v253 offset:4096
mqa 80,248,136
tq 244,9,14336
ds_read_b64 a[196:197], v252 offset:6144
ds_read_b64 a[198:199], v253 offset:6144
mqa 88,248,140
tq 246,9,16384
buffer_load_dwordx4 v247, s[24:27], s58 offen offset:1024 lds
mqa 92,252,140
tq 248,12,14336
ds_read_b64 a[200:201], v254 offset:4096
ds_read_b64 a[202:203], v255 offset:4096
mqa 84,252,136
tq 250,12,16384
buffer_load_dwordx4 v248, s[24:27], s58 offen offset:2048 lds
s_waitcnt lgkmcnt(12)
s_nop 0
L_hop1_1_%=:
s_cbranch_execz L_hop5_1_%=
mqa 64,224,160
tq 252,13,14336
tq 254,13,16384
ds_read_b64 a[204:205], v254 offset:6144
ds_read_b64 a[206:207], v255 offset:6144
mqa 72,224,164
buffer_load_dwordx4 v249, s[24:27], s58 offen offset:3072 lds
s_waitcnt vmcnt(12)
s_barrier
mqa 76,228,164
tq 224,8,18432
v_lshlrev_b32 v138, 16, v37
mqa 68,228,160
tq 226,8,20480
v_and_b32 v139, 0xffff0000, v37
mqa 80,232,160
tq 228,9,18432
v_lshlrev_b32 v140, 16, v38
mqa 88,232,164
tq 230,9,20480
v_and_b32 v141, 0xffff0000, v38
mqa 92,236,164
tq 232,12,18432
v_lshlrev_b32 v142, 16, v39
mqa 84,236,160
tq 234,12,20480
s_waitcnt lgkmcnt(8)
mqa 64,240,168
tq 236,13,18432
v_and_b32 v143, 0xffff0000, v39
mqa 72,240,172
tq 238,13,20480
v_lshlrev_b32 v144, 16, v40
mqa 76,244,172
tq 240,8,22528
mqa 68,244,168
s_waitcnt lgkmcnt(9)
s_lshr_b32 s58, s45, 10
s_add_u32 m0, s57, 112640
tq 242,8,24576
buffer_load_dwordx4 v246, s[24:27], s58 offen offset:0 lds
s_waitcnt vmcnt(9)
ds_read_b64 v[48:49], v252 offset:8192
ds_read_b64 v[50:51], v253 offset:8192
mqa 80,248,168
tq 244,9,22528
ds_read_b64 v[52:53], v252 offset:10240
ds_read_b64 v[54:55], v253 offset:10240
mqa 88,248,172
tq 246,9,24576
buffer_load_dwordx4 v247, s[24:27], s58 offen offset:1024 lds
mqa 92,252,172
tq 248,12,22528
ds_read_b64 v[56:57], v254 offset:8192
ds_read_b64 v[58:59], v255 offset:8192
mqa 84,252,168
tq 250,12,24576
buffer_load_dwordx4 v248, s[24:27], s58 offen offset:2048 lds
s_waitcnt lgkmcnt(12)
mqa 64,224,192
tq 252,13,22528
ds_read_b64 v[60:61], v254 offset:10240
ds_read_b64 v[62:63], v255 offset:10240
mqa 72,224,196
tq 254,13,24576
buffer_load_dwordx4 v249, s[24:27], s58 offen offset:3072 lds
mqa 76,228,196
tq 224,8,26624
s_add_u32 m0, s56, 10240
mqa 68,228,192
tq 226,8,28672
buffer_load_dwordx4 v10, s[20:23], s45 offen offset:0 lds
mqa 80,232,192
tq 228,9,26624
v_and_b32 v145, 0xffff0000, v40
mqa 88,232,196
tq 230,9,28672
buffer_load_dwordx4 v11, s[20:23], s45 offen offset:1024 lds
mqa 92,236,196
tq 232,12,26624
s_add_u32 m0, s56, 18432
mqa 84,236,192
tq 234,12,28672
buffer_load_dwordx4 v10, s[20:23], s47 offen offset:0 lds
s_waitcnt lgkmcnt(6)
mqa 64,240,200
tq 236,13,26624
v_lshlrev_b32 v146, 16, v41
mqa 72,240,204
tq 238,13,28672
buffer_load_dwordx4 v11, s[20:23], s47 offen offset:1024 lds
mqa 76,244,204
tq 240,8,30720
mqa 68,244,200
s_lshr_b32 s58, s47, 10
s_add_u32 m0, s57, 116736
tq 242,8,32768
buffer_load_dwordx4 v246, s[24:27], s58 offen offset:0 lds
mqa 80,248,200
tq 244,9,30720
v_and_b32 v147, 0xffff0000, v41
mqa 88,248,204
tq 246,9,32768
buffer_load_dwordx4 v247, s[24:27], s58 offen offset:1024 lds
mqa 92,252,204
tq 248,12,30720
mqa 84,252,200
tq 250,12,32768
buffer_load_dwordx4 v248, s[24:27], s58 offen offset:2048 lds
s_waitcnt lgkmcnt(6)
mqv 64,224,48
tq 252,13,30720
tq 254,13,32768
mqv 72,224,52
buffer_load_dwordx4 v249, s[24:27], s58 offen offset:3072 lds
s_waitcnt vmcnt(16)
s_barrier
mqv 76,228,52
tq 224,8,34816
v_lshlrev_b32 v148, 16, v42
mqv 68,228,48
tq 226,8,36864
v_and_b32 v149, 0xffff0000, v42
mqv 80,232,48
tq 228,9,34816
v_lshlrev_b32 v150, 16, v43
mqv 88,232,52
tq 230,9,36864
v_and_b32 v151, 0xffff0000, v43
mqv 92,236,52
tq 232,12,34816
v_lshlrev_b32 v152, 16, v44
mqv 84,236,48
tq 234,12,36864
s_waitcnt lgkmcnt(6)
mqv 64,240,56
tq 236,13,34816
v_and_b32 v153, 0xffff0000, v44
mqv 72,240,60
tq 238,13,36864
v_lshlrev_b32 v154, 16, v45
mqv 76,244,60
tq 240,8,38912
mqv 68,244,56
tq 242,8,40960
s_waitcnt vmcnt(12)
ds_read_b64 a[16:17], v252 offset:0
ds_read_b64 a[18:19], v253 offset:0
mqv 80,248,56
tq 244,9,38912
ds_read_b64 a[20:21], v252 offset:2048
mqv 88,248,60
tq 246,9,40960
ds_read_b64 a[22:23], v253 offset:2048
mqv 92,252,60
tq 248,12,38912
ds_read_b64 a[24:25], v254 offset:0
ds_read_b64 a[26:27], v255 offset:0
mqv 84,252,56
tq 250,12,40960
s_waitcnt lgkmcnt(12)
mqv 96,224,48
tq 252,13,38912
ds_read_b64 a[28:29], v254 offset:2048
ds_read_b64 a[30:31], v255 offset:2048
mqv 104,224,52
tq 254,13,40960
buffer_load_dword v23, v20, s[28:31], 0 offen sc1
s_add_u32 m0, s56, 26624
s_nop 0
buffer_load_dwordx4 v10, s[20:23], s49 offen offset:0 lds
mqv 108,228,52
tq 224,4,2048
v_and_b32 v155, 0xffff0000, v45
mqv 100,228,48
tq 226,4,4096
buffer_load_dwordx4 v11, s[20:23], s49 offen offset:1024 lds
mqv 112,232,48
tq 228,5,2048
s_add_u32 m0, s56, 34816
mqv 120,232,52
tq 230,5,4096
buffer_load_dwordx4 v10, s[20:23], s51 offen offset:0 lds
mqv 124,236,52
tq 232,6,2048
mqv 116,236,48
tq 234,6,4096
buffer_load_dwordx4 v11, s[20:23], s51 offen offset:1024 lds
s_waitcnt lgkmcnt(6)
mqv 96,240,56
tq 236,7,2048
mqv 104,240,60
s_lshr_b32 s58, s49, 10
s_add_u32 m0, s57, 108544
tq 238,7,4096
buffer_load_dwordx4 v246, s[24:27], s58 offen offset:0 lds
mqv 108,244,60
tq 240,4,6144
mqv 100,244,56
tq 242,4,8192
buffer_load_dwordx4 v247, s[24:27], s58 offen offset:1024 lds
s_waitcnt vmcnt(15)
ds_read_b64 a[48:49], v252 offset:4096
ds_read_b64 a[50:51], v253 offset:4096
mqv 112,248,56
tq 244,5,6144
ds_read_b64 a[52:53], v252 offset:6144
ds_read_b64 a[54:55], v253 offset:6144
mqv 120,248,60
tq 246,5,8192
buffer_load_dwordx4 v248, s[24:27], s58 offen offset:2048 lds
mqv 124,252,60
tq 248,6,6144
ds_read_b64 a[56:57], v254 offset:4096
ds_read_b64 a[58:59], v255 offset:4096
mqv 116,252,56
tq 250,6,8192
buffer_load_dwordx4 v249, s[24:27], s58 offen offset:3072 lds
s_waitcnt lgkmcnt(12)
mqa 64,224,16
tq 252,7,6144
tq 254,7,8192
ds_read_b64 a[60:61], v254 offset:6144
ds_read_b64 a[62:63], v255 offset:6144
mqa 72,224,20
s_waitcnt vmcnt(13)
s_barrier
mqa 76,228,20
tq 224,4,10240
v_lshlrev_b32 v156, 16, v46
mqa 68,228,16
tq 226,4,12288
v_and_b32 v157, 0xffff0000, v46
mqa 80,232,16
tq 228,5,10240
v_lshlrev_b32 v158, 16, v47
mqa 88,232,20
tq 230,5,12288
v_and_b32 v159, 0xffff0000, v47
mqa 92,236,20
tq 232,6,10240
mqa 84,236,16
s_waitcnt lgkmcnt(5)
s_lshr_b32 s58, s51, 10
s_add_u32 m0, s57, 112640
tq 234,6,12288
buffer_load_dwordx4 v246, s[24:27], s58 offen offset:0 lds
mqa 64,240,24
tq 236,7,10240
v_lshlrev_b32 v160, 16, v48
mqa 72,240,28
tq 238,7,12288
buffer_load_dwordx4 v247, s[24:27], s58 offen offset:1024 lds
mqa 76,244,28
tq 240,4,14336
mqa 68,244,24
tq 242,4,16384
buffer_load_dwordx4 v248, s[24:27], s58 offen offset:2048 lds
s_waitcnt vmcnt(12)
ds_read_b64 a[80:81], v252 offset:8192
ds_read_b64 a[82:83], v253 offset:8192
mqa 80,248,24
tq 244,5,14336
ds_read_b64 a[84:85], v252 offset:10240
ds_read_b64 a[86:87], v253 offset:10240
mqa 88,248,28
tq 246,5,16384
buffer_load_dwordx4 v249, s[24:27], s58 offen offset:3072 lds
mqa 92,252,28
tq 248,6,14336
ds_read_b64 a[88:89], v254 offset:8192
ds_read_b64 a[90:91], v255 offset:8192
mqa 84,252,24
tq 250,6,16384
s_waitcnt lgkmcnt(12)
mqa 64,224,48
tq 252,7,14336
s_add_u32 m0, s56, 43008
ds_read_b64 a[92:93], v254 offset:10240
mqa 72,224,52
tq 254,7,16384
buffer_load_dwordx4 v10, s[20:23], s53 offen offset:0 lds
ds_read_b64 a[94:95], v255 offset:10240
mqa 76,228,52
tq 224,4,18432
v_and_b32 v161, 0xffff0000, v48
mqa 68,228,48
tq 226,4,20480
buffer_load_dwordx4 v11, s[20:23], s53 offen offset:1024 lds
mqa 80,232,48
tq 228,5,18432
s_add_u32 m0, s56, 51200
mqa 88,232,52
tq 230,5,20480
buffer_load_dwordx4 v10, s[20:23], s55 offen offset:0 lds
mqa 92,236,52
tq 232,6,18432
mqa 84,236,48
tq 234,6,20480
buffer_load_dwordx4 v11, s[20:23], s55 offen offset:1024 lds
s_waitcnt lgkmcnt(7)
mqa 64,240,56
tq 236,7,18432
mqa 72,240,60
s_waitcnt lgkmcnt(7)
s_lshr_b32 s58, s53, 10
s_add_u32 m0, s57, 116736
tq 238,7,20480
buffer_load_dwordx4 v246, s[24:27], s58 offen offset:0 lds
mqa 76,244,60
tq 240,4,22528
mqa 68,244,56
tq 242,4,24576
buffer_load_dwordx4 v247, s[24:27], s58 offen offset:1024 lds
s_waitcnt vmcnt(10)
ds_read_b64 a[112:113], v252 offset:0
ds_read_b64 a[114:115], v253 offset:0
mqa 80,248,56
tq 244,5,22528
ds_read_b64 a[116:117], v252 offset:2048
ds_read_b64 a[118:119], v253 offset:2048
mqa 88,248,60
tq 246,5,24576
buffer_load_dwordx4 v248, s[24:27], s58 offen offset:2048 lds
mqa 92,252,60
tq 248,6,22528
ds_read_b64 a[120:121], v254 offset:0
ds_read_b64 a[122:123], v255 offset:0
mqa 84,252,56
tq 250,6,24576
buffer_load_dwordx4 v249, s[24:27], s58 offen offset:3072 lds
s_waitcnt lgkmcnt(12)
mqa 64,224,80
tq 252,7,22528
tq 254,7,24576
ds_read_b64 a[124:125], v254 offset:2048
ds_read_b64 a[126:127], v255 offset:2048
mqa 72,224,84
s_barrier
mqa 76,228,84
tq 224,4,26624
v_lshlrev_b32 v162, 16, v49
mqa 68,228,80
tq 226,4,28672
v_and_b32 v163, 0xffff0000, v49
mqa 80,232,80
tq 228,5,26624
v_lshlrev_b32 v164, 16, v50
mqa 88,232,84
tq 230,5,28672
v_and_b32 v165, 0xffff0000, v50
mqa 92,236,84
tq 232,6,26624
mqa 84,236,80
s_waitcnt lgkmcnt(5)
s_lshr_b32 s58, s55, 10
s_add_u32 m0, s57, 108544
tq 234,6,28672
buffer_load_dwordx4 v246, s[24:27], s58 offen offset:0 lds
mqa 64,240,88
tq 236,7,26624
v_lshlrev_b32 v166, 16, v51
mqa 72,240,92
tq 238,7,28672
buffer_load_dwordx4 v247, s[24:27], s58 offen offset:1024 lds
mqa 76,244,92
tq 240,4,30720
mqa 68,244,88
tq 242,4,32768
buffer_load_dwordx4 v248, s[24:27], s58 offen offset:2048 lds
s_waitcnt vmcnt(11)
ds_read_b64 a[144:145], v252 offset:4096
ds_read_b64 a[146:147], v253 offset:4096
mqa 80,248,88
tq 244,5,30720
ds_read_b64 a[148:149], v252 offset:6144
ds_read_b64 a[150:151], v253 offset:6144
mqa 88,248,92
tq 246,5,32768
buffer_load_dwordx4 v249, s[24:27], s58 offen offset:3072 lds
mqa 92,252,92
tq 248,6,30720
ds_read_b64 a[152:153], v254 offset:4096
ds_read_b64 a[154:155], v255 offset:4096
mqa 84,252,88
tq 250,6,32768
buffer_load_dword v25, v19, s[72:75], 0 offen
s_waitcnt lgkmcnt(12)
mqa 64,224,112
tq 252,7,30720
s_add_u32 m0, s56, 59392
ds_read_b64 a[156:157], v254 offset:6144
mqa 72,224,116
tq 254,7,32768
buffer_load_dwordx4 v30, s[20:23], s44 offen offset:0 lds
ds_read_b64 a[158:159], v255 offset:6144
mqa 76,228,116
tq 224,4,34816
v_and_b32 v167, 0xffff0000, v51
mqa 68,228,112
tq 226,4,36864
buffer_load_dwordx4 v31, s[20:23], s44 offen offset:1024 lds
mqa 80,232,112
tq 228,5,34816
s_add_u32 m0, s56, 67584
mqa 88,232,116
tq 230,5,36864
buffer_load_dwordx4 v30, s[20:23], s48 offen offset:0 lds
mqa 92,236,116
tq 232,6,34816
mqa 84,236,112
tq 234,6,36864
buffer_load_dwordx4 v31, s[20:23], s48 offen offset:1024 lds
s_waitcnt vmcnt(9)
ds_read_b64 a[176:177], v252 offset:8192
ds_read_b64 a[178:179], v253 offset:8192
s_waitcnt lgkmcnt(9)
mqa 64,240,120
tq 236,7,34816
ds_read_b64 a[180:181], v252 offset:10240
mqa 72,240,124
tq 238,7,36864
ds_read_b64 a[182:183], v253 offset:10240
mqa 76,244,124
tq 240,4,38912
ds_read_b64 a[184:185], v254 offset:8192
mqa 68,244,120
tq 242,4,40960
ds_read_b64 a[186:187], v255 offset:8192
mqa 80,248,120
tq 244,5,38912
ds_read_b64 a[188:189], v254 offset:10240
mqa 88,248,124
tq 246,5,40960
ds_read_b64 a[190:191], v255 offset:10240
mqa 92,252,124
tq 248,6,38912
mqa 84,252,120
tq 250,6,40960
s_waitcnt vmcnt(5)
ds_read_b64 a[208:209], v252 offset:0
ds_read_b64 a[210:211], v253 offset:0
s_waitcnt lgkmcnt(13)
mqa 64,224,144
tq 252,7,38912
tq 254,7,40960
ds_read_b64 a[212:213], v252 offset:2048
ds_read_b64 a[214:215], v253 offset:2048
mqa 72,224,148
s_barrier
mqa 76,228,148
tq 224,4,43008
ds_read_b64 a[216:217], v254 offset:0
mqa 68,228,144
tq 226,4,45056
ds_read_b64 a[218:219], v255 offset:0
mqa 80,232,144
tq 228,4,47104
ds_read_b64 a[220:221], v254 offset:2048
mqa 88,232,148
tq 230,4,49152
ds_read_b64 a[222:223], v255 offset:2048
mqa 92,236,148
tq 232,4,51200
s_add_u32 m0, s56, 75776
mqa 84,236,144
tq 234,4,53248
buffer_load_dwordx4 v30, s[20:23], s43 offen offset:0 lds
s_waitcnt lgkmcnt(12)
mqa 64,240,152
tq 236,4,55296
v_lshlrev_b32 v168, 16, v52
mqa 72,240,156
tq 238,4,57344
buffer_load_dwordx4 v31, s[20:23], s43 offen offset:1024 lds
mqa 76,244,156
tq 240,5,43008
s_add_u32 m0, s56, 83968
mqa 68,244,152
tq 242,5,45056
buffer_load_dwordx4 v30, s[20:23], s50 offen offset:0 lds
mqa 80,248,152
tq 244,5,47104
v_and_b32 v169, 0xffff0000, v52
mqa 88,248,156
tq 246,5,49152
v_lshlrev_b32 v170, 16, v53
mqa 92,252,156
tq 248,5,51200
mqa 84,252,152
tq 250,5,53248
buffer_load_dwordx4 v31, s[20:23], s50 offen offset:1024 lds
s_waitcnt lgkmcnt(6)
mqa 64,224,176
tq 252,5,55296
s_add_u32 m0, s56, 92160
mqa 72,224,180
tq 254,5,57344
buffer_load_dwordx4 v30, s[20:23], s52 offen offset:0 lds
mqa 72,228,188
tq 224,6,43008
mqa 64,228,184
tq 226,6,45056
buffer_load_dwordx4 v31, s[20:23], s52 offen offset:1024 lds
mqa 64,232,208
tq 228,6,47104
v_and_b32 v171, 0xffff0000, v53
mqa 72,232,212
tq 230,6,49152
v_lshlrev_b32 v172, 16, v54
mqa 72,236,220
tq 232,6,51200
s_add_u32 m0, s56, 100352
mqa 64,236,216
tq 234,6,53248
buffer_load_dwordx4 v30, s[20:23], s51 offen offset:0 lds
s_waitcnt lgkmcnt(6)
mqa 68,240,176
tq 236,6,55296
v_and_b32 v173, 0xffff0000, v54
mqa 76,240,180
tq 238,6,57344
v_lshlrev_b32 v174, 16, v55
mqa 76,244,188
tq 240,7,43008
v_fma_f32 v200, v136, v72, v136
v_fma_f32 v201, v137, v73, v137
mqa 68,244,184
tq 242,7,45056
v_fma_f32 v202, v138, v74, v138
mqa 68,248,208
tq 244,7,47104
v_fma_f32 v203, v139, v75, v139
v_fma_f32 v192, v128, v64, v128
mqa 76,248,212
tq 246,7,49152
v_fma_f32 v193, v129, v65, v129
mqa 76,252,220
tq 248,7,51200
v_fma_f32 v194, v130, v66, v130
mqa 68,252,216
tq 250,7,53248
buffer_load_dwordx4 v31, s[20:23], s51 offen offset:1024 lds
L_hop2_1_%=:
s_cbranch_execz L_hop6_1_%=
s_waitcnt lgkmcnt(6)
mqa 80,224,176
tq 252,7,55296
tq 254,7,57344
v_fma_f32 v195, v131, v67, v131
mqa 88,224,180
s_waitcnt vmcnt(8)
s_barrier
mqa 88,228,188
tq 224,4,59392
s_add_u32 m0, s56, 2048
v_cvt_pk_bf16_f32 v36, v200, v201
mqa 80,228,184
tq 226,4,61440
buffer_load_dwordx4 v30, s[20:23], s54 offen offset:0 lds
v_cvt_pk_bf16_f32 v37, v202, v203
mqa 80,232,208
tq 228,5,59392
v_cvt_pk_bf16_f32 v32, v192, v193
v_cvt_pk_bf16_f32 v33, v194, v195
mqa 88,232,212
tq 230,5,61440
v_fma_f32 v204, v140, v76, v140
v_fma_f32 v205, v141, v77, v141
mqa 88,236,220
tq 232,6,59392
v_fma_f32 v206, v142, v78, v142
v_fma_f32 v207, v143, v79, v143
mqa 80,236,216
tq 234,6,61440
buffer_load_dwordx4 v31, s[20:23], s54 offen offset:1024 lds
s_waitcnt lgkmcnt(6)
mqa 84,240,176
tq 236,7,59392
v_fma_f32 v196, v132, v68, v132
v_fma_f32 v197, v133, v69, v133
v_fma_f32 v198, v134, v70, v134
mqa 92,240,180
tq 238,7,61440
v_fma_f32 v199, v135, v71, v135
v_cndmask_b32 v22, v22, v23, s[60:61]
mqa 92,244,188
tq 240,4,63488
s_add_u32 m0, s56, 10240
v_cvt_pk_bf16_f32 v38, v204, v205
mqa 84,244,184
tq 242,8,0
buffer_load_dwordx4 v30, s[72:75], s40 offen offset:0 lds
v_cvt_pk_bf16_f32 v39, v206, v207
mqa 84,248,208
tq 244,5,63488
mqa 92,248,212
tq 246,9,0
buffer_store_dwordx4 v[36:39], v14, s[28:31], s80 offen offset:1024 sc1
v_cvt_pk_bf16_f32 v34, v196, v197
v_cvt_pk_bf16_f32 v35, v198, v199
buffer_store_dwordx4 v[32:35], v14, s[28:31], s80 offen offset:0 sc1
mqa 92,252,220
tq 248,6,63488
v_fma_f32 v216, v152, v88, v152
v_fma_f32 v217, v153, v89, v153
mqa 84,252,216
tq 250,12,0
buffer_load_dwordx4 v31, s[72:75], s40 offen offset:1024 lds
s_waitcnt lgkmcnt(6)
mqa 96,224,32
tq 252,7,63488
v_fma_f32 v218, v154, v90, v154
v_fma_f32 v219, v155, v91, v155
mqa 104,224,36
tq 254,13,0
v_fma_f32 v208, v144, v80, v144
v_fma_f32 v209, v145, v81, v145
)ASM"
R"ASM(
mqa 108,228,36
tq 224,8,2048
s_add_u32 m0, s56, 18432
v_fma_f32 v210, v146, v82, v146
mqa 100,228,32
tq 226,8,4096
buffer_load_dwordx4 v30, s[20:23], s45 offen offset:0 lds
v_fma_f32 v211, v147, v83, v147
mqa 112,232,32
tq 228,9,2048
v_cvt_pk_bf16_f32 v44, v216, v217
v_cvt_pk_bf16_f32 v45, v218, v219
mqa 120,232,36
tq 230,9,4096
v_cvt_pk_bf16_f32 v40, v208, v209
v_cvt_pk_bf16_f32 v41, v210, v211
mqa 124,236,36
tq 232,12,2048
v_fma_f32 v220, v156, v92, v156
v_fma_f32 v221, v157, v93, v157
mqa 116,236,32
tq 234,12,4096
buffer_load_dwordx4 v31, s[20:23], s45 offen offset:1024 lds
s_waitcnt lgkmcnt(6)
mqa 96,240,40
tq 236,13,2048
v_fma_f32 v222, v158, v94, v158
v_fma_f32 v223, v159, v95, v159
mqa 104,240,44
tq 238,13,4096
v_fma_f32 v212, v148, v84, v148
v_fma_f32 v213, v149, v85, v149
mqa 108,244,44
tq 240,8,6144
s_add_u32 m0, s56, 26624
v_fma_f32 v214, v150, v86, v150
mqa 100,244,40
tq 242,8,8192
buffer_load_dwordx4 v30, s[20:23], s47 offen offset:0 lds
v_fma_f32 v215, v151, v87, v151
mqa 112,248,40
tq 244,9,6144
v_cvt_pk_bf16_f32 v46, v220, v221
v_cvt_pk_bf16_f32 v47, v222, v223
mqa 120,248,44
tq 246,9,8192
buffer_store_dwordx4 v[44:47], v14, s[28:31], s80 offen offset:3072 sc1
v_cvt_pk_bf16_f32 v42, v212, v213
mqa 124,252,44
tq 248,12,6144
v_cvt_pk_bf16_f32 v43, v214, v215
mqa 116,252,40
tq 250,12,8192
buffer_load_dwordx4 v31, s[20:23], s47 offen offset:1024 lds
buffer_store_dwordx4 v[40:43], v14, s[28:31], s80 offen offset:2048 sc1
s_waitcnt lgkmcnt(6)
mqa 96,224,96
tq 252,13,6144
tq 254,13,8192
mqa 104,224,100
s_waitcnt vmcnt(14)
s_barrier
mqa 108,228,100
tq 224,8,18432
s_add_u32 m0, s56, 34816
mqa 100,228,96
tq 226,8,20480
buffer_load_dwordx4 v10, s[72:75], s44 offen offset:0 lds
mqa 112,232,96
tq 228,9,18432
v_and_b32 v175, 0xffff0000, v55
mqa 120,232,100
tq 230,9,20480
v_lshlrev_b32 v176, 16, v56
mqa 124,236,100
tq 232,12,18432
mqa 116,236,96
tq 234,12,20480
buffer_load_dwordx4 v11, s[72:75], s44 offen offset:1024 lds
s_waitcnt lgkmcnt(6)
mqa 96,240,104
tq 236,13,18432
s_add_u32 m0, s56, 108544
mqa 104,240,108
tq 238,13,20480
buffer_load_dwordx4 v30, s[20:23], s49 offen offset:0 lds
mqa 108,244,108
tq 240,8,22528
v_and_b32 v177, 0xffff0000, v56
mqa 100,244,104
tq 242,8,24576
buffer_load_dwordx4 v31, s[20:23], s49 offen offset:1024 lds
mqa 112,248,104
tq 244,9,22528
v_lshlrev_b32 v178, 16, v57
mqa 120,248,108
tq 246,9,24576
v_and_b32 v179, 0xffff0000, v57
mqa 124,252,108
tq 248,12,22528
s_add_u32 m0, s56, 116736
mqa 116,252,104
tq 250,12,24576
buffer_load_dwordx4 v30, s[20:23], s53 offen offset:0 lds
s_waitcnt lgkmcnt(6)
mqa 96,224,128
tq 252,13,22528
v_lshlrev_b32 v180, 16, v58
mqa 104,224,132
tq 254,13,24576
v_and_b32 v181, 0xffff0000, v58
mqa 108,228,132
tq 224,8,26624
v_lshlrev_b32 v182, 16, v59
mqa 100,228,128
tq 226,8,28672
buffer_load_dwordx4 v31, s[20:23], s53 offen offset:1024 lds
mqa 112,232,128
tq 228,9,26624
v_and_b32 v183, 0xffff0000, v59
mqa 120,232,132
tq 230,9,28672
v_lshlrev_b32 v184, 16, v60
mqa 124,236,132
tq 232,12,26624
s_add_u32 m0, s56, 124928
mqa 116,236,128
tq 234,12,28672
buffer_load_dwordx4 v30, s[20:23], s55 offen offset:0 lds
s_waitcnt lgkmcnt(6)
mqa 96,240,136
tq 236,13,26624
v_and_b32 v185, 0xffff0000, v60
mqa 104,240,140
tq 238,13,28672
v_lshlrev_b32 v186, 16, v61
mqa 108,244,140
tq 240,8,30720
v_and_b32 v187, 0xffff0000, v61
mqa 100,244,136
tq 242,8,32768
buffer_load_dwordx4 v31, s[20:23], s55 offen offset:1024 lds
mqa 112,248,136
tq 244,9,30720
v_lshlrev_b32 v188, 16, v62
mqa 120,248,140
tq 246,9,32768
v_and_b32 v189, 0xffff0000, v62
mqa 124,252,140
tq 248,12,30720
s_add_u32 m0, s56, 133120
mqa 116,252,136
tq 250,12,32768
buffer_load_dwordx4 v30, s[72:75], s44 offen offset:0 lds
s_waitcnt lgkmcnt(6)
mqa 96,224,160
tq 252,13,30720
tq 254,13,32768
mqa 104,224,164
s_waitcnt vmcnt(19)
s_barrier
mqa 108,228,164
tq 224,4,2048
v_lshlrev_b32 v190, 16, v63
mqa 100,228,160
tq 226,4,4096
buffer_load_dwordx4 v31, s[72:75], s44 offen offset:1024 lds
mqa 112,232,160
tq 228,5,2048
v_and_b32 v191, 0xffff0000, v63
mqa 120,232,164
tq 230,5,4096
ds_read_b128 v[64:67], v24 offset:512
mqa 124,236,164
tq 232,6,2048
s_add_u32 m0, s56, 141312
mqa 116,236,160
tq 234,6,4096
buffer_load_dwordx4 v10, s[72:75], s40 offen offset:0 lds
s_waitcnt lgkmcnt(7)
mqa 96,240,168
tq 236,7,2048
ds_read_b128 v[72:75], v24 offset:512
mqa 104,240,172
tq 238,7,4096
ds_read_b128 v[68:71], v24 offset:576
mqa 108,244,172
tq 240,4,6144
ds_read_b128 v[76:79], v24 offset:576
mqa 100,244,168
tq 242,4,8192
buffer_load_dwordx4 v11, s[72:75], s40 offen offset:1024 lds
mqa 112,248,168
tq 244,5,6144
ds_read_b128 v[80:83], v24 offset:640
mqa 120,248,172
tq 246,5,8192
ds_read_b128 v[88:91], v24 offset:640
mqa 124,252,172
tq 248,6,6144
mqa 116,252,168
tq 250,6,8192
s_waitcnt vmcnt(12)
buffer_store_dword v22, v21, s[28:31], 0 offen offset:0 sc1
s_waitcnt lgkmcnt(10)
mqa 96,224,192
tq 252,7,6144
s_add_u32 m0, s56, 149504
mqa 104,224,196
tq 254,7,8192
buffer_load_dwordx4 v10, s[72:75], s41 offen offset:0 lds
mqa 108,228,196
tq 224,8,10240
ds_read_b128 v[84:87], v24 offset:704
mqa 100,228,192
tq 226,8,12288
buffer_load_dwordx4 v11, s[72:75], s41 offen offset:1024 lds
mqa 112,232,192
tq 228,9,10240
ds_read_b128 v[92:95], v24 offset:704
mqa 120,232,196
tq 230,9,12288
mqa 124,236,196
tq 232,12,10240
s_add_u32 m0, s56, 43008
mqa 116,236,192
tq 234,12,12288
buffer_load_dwordx4 v10, s[72:75], s54 offen offset:0 lds
s_waitcnt lgkmcnt(8)
mqa 96,240,200
tq 236,13,10240
mqa 104,240,204
tq 238,13,12288
mqa 108,244,204
tq 240,8,14336
mqa 100,244,200
tq 242,8,16384
buffer_load_dwordx4 v11, s[72:75], s54 offen offset:1024 lds
mqa 112,248,200
tq 244,9,14336
mqa 120,248,204
tq 246,9,16384
mqa 124,252,204
tq 248,12,14336
s_add_u32 m0, s56, 51200
mqa 116,252,200
tq 250,12,16384
buffer_load_dwordx4 v30, s[72:75], s41 offen offset:0 lds
s_waitcnt lgkmcnt(6)
mqa 96,224,16
tq 252,13,14336
tq 254,13,16384
mqa 104,224,20
s_barrier
mqa 108,228,20
tq 224,4,18432
mqa 100,228,16
tq 226,4,20480
buffer_load_dwordx4 v31, s[72:75], s41 offen offset:1024 lds
mqa 112,232,16
tq 228,5,18432
mqa 120,232,20
tq 230,5,20480
s_lshr_b32 s82, s42, 4
mqa 124,236,20
tq 232,6,18432
s_add_u32 m0, s56, 59392
mqa 116,236,16
tq 234,6,20480
buffer_load_dwordx4 v10, s[72:75], s42 offen offset:0 lds
s_waitcnt lgkmcnt(6)
mqa 96,240,24
tq 236,7,18432
mqa 104,240,28
tq 238,7,20480
s_lshr_b32 s83, s43, 4
mqa 108,244,28
tq 240,4,22528
mqa 100,244,24
tq 242,4,24576
buffer_load_dwordx4 v11, s[72:75], s42 offen offset:1024 lds
mqa 112,248,24
tq 244,5,22528
mqa 120,248,28
tq 246,5,24576
s_lshr_b32 s84, s44, 4
mqa 124,252,28
tq 248,6,22528
s_add_u32 m0, s56, 67584
mqa 116,252,24
tq 250,6,24576
buffer_load_dwordx4 v30, s[72:75], s42 offen offset:0 lds
s_waitcnt lgkmcnt(6)
mqa 96,224,48
tq 252,7,22528
mqa 104,224,52
tq 254,7,24576
s_lshr_b32 s85, s45, 4
mqa 108,228,52
tq 224,4,26624
mqa 100,228,48
tq 226,4,28672
buffer_load_dwordx4 v31, s[72:75], s42 offen offset:1024 lds
mqa 112,232,48
tq 228,5,26624
mqa 120,232,52
tq 230,5,28672
s_lshr_b32 s86, s46, 4
mqa 124,236,52
tq 232,6,26624
s_add_u32 m0, s56, 83968
mqa 116,236,48
tq 234,6,28672
buffer_load_dwordx4 v10, s[72:75], s49 offen offset:0 lds
s_waitcnt lgkmcnt(6)
mqa 96,240,56
tq 236,7,26624
mqa 104,240,60
tq 238,7,28672
s_lshr_b32 s87, s47, 4
mqa 108,244,60
tq 240,4,30720
mqa 100,244,56
tq 242,4,32768
buffer_load_dwordx4 v11, s[72:75], s49 offen offset:1024 lds
mqa 112,248,56
tq 244,5,30720
mqa 120,248,60
tq 246,5,32768
s_lshr_b32 s88, s48, 4
mqa 124,252,60
tq 248,6,30720
s_add_u32 m0, s56, 2048
mqa 116,252,56
tq 250,6,32768
buffer_load_dwordx4 v10, s[72:75], s46 offen offset:0 lds
s_waitcnt lgkmcnt(6)
mqa 96,224,80
tq 252,7,30720
tq 254,7,32768
mqa 104,224,84
s_waitcnt vmcnt(22)
s_barrier
mqa 108,228,84
tq 224,8,43008
mqa 100,228,80
tq 226,8,45056
buffer_load_dwordx4 v11, s[72:75], s46 offen offset:1024 lds
mqa 112,232,80
tq 228,9,43008
mqa 120,232,84
tq 230,9,45056
s_lshr_b32 s89, s49, 4
mqa 124,236,84
tq 232,12,43008
s_add_u32 m0, s56, 92160
mqa 116,236,80
tq 234,12,45056
buffer_load_dwordx4 v10, s[72:75], s48 offen offset:0 lds
s_waitcnt lgkmcnt(6)
mqa 96,240,88
tq 236,13,43008
mqa 104,240,92
tq 238,13,45056
s_lshr_b32 s90, s50, 4
mqa 108,244,92
tq 240,8,47104
mqa 100,244,88
tq 242,8,49152
buffer_load_dwordx4 v11, s[72:75], s48 offen offset:1024 lds
mqa 112,248,88
tq 244,9,47104
mqa 120,248,92
tq 246,9,49152
s_lshr_b32 s91, s51, 4
mqa 124,252,92
tq 248,12,47104
s_add_u32 m0, s56, 18432
mqa 116,252,88
tq 250,12,49152
buffer_load_dwordx4 v30, s[72:75], s48 offen offset:0 lds
s_waitcnt lgkmcnt(6)
mqa 96,224,112
tq 252,13,47104
mqa 104,224,116
tq 254,13,49152
s_lshr_b32 s92, s52, 4
mqa 108,228,116
tq 224,8,34816
mqa 100,228,112
tq 226,8,36864
buffer_load_dwordx4 v31, s[72:75], s48 offen offset:1024 lds
mqa 112,232,112
tq 228,9,34816
mqa 120,232,116
tq 230,9,36864
s_lshr_b32 s93, s53, 4
mqa 124,236,116
tq 232,12,34816
mqa 116,236,112
tq 234,12,36864
s_lshr_b32 s94, s54, 4
s_waitcnt lgkmcnt(6)
mqa 96,240,120
tq 236,13,34816
mqa 104,240,124
tq 238,13,36864
s_lshr_b32 s95, s55, 4
mqa 108,244,124
tq 240,8,38912
mqa 100,244,120
tq 242,8,40960
mqa 112,248,120
tq 244,9,38912
mqa 120,248,124
tq 246,9,40960
mqa 124,252,124
tq 248,12,38912
mqa 116,252,120
tq 250,12,40960
s_waitcnt lgkmcnt(6)
mqa 96,224,144
tq 252,13,38912
tq 254,13,40960
s_add_u32 m0, s56, 75776
mqa 104,224,148
buffer_load_dwordx4 v10, s[72:75], s50 offen offset:0 lds
s_waitcnt vmcnt(24)
s_barrier
mqa 108,228,148
tq 224,8,51200
mqa 100,228,144
tq 226,8,53248
buffer_load_dwordx4 v11, s[72:75], s50 offen offset:1024 lds
mqa 112,232,144
tq 228,8,55296
mqa 120,232,148
tq 230,8,57344
mqa 124,236,148
tq 232,8,59392
mqa 116,236,144
tq 234,8,61440
s_waitcnt lgkmcnt(6)
mqa 96,240,152
tq 236,8,63488
mqa 104,240,156
tq 238,16,0
mqa 108,244,156
tq 240,9,51200
mqa 100,244,152
tq 242,9,53248
mqa 112,248,152
tq 244,9,55296
mqa 120,248,156
tq 246,9,57344
mqa 124,252,156
tq 248,9,59392
mqa 116,252,152
tq 250,9,61440
s_waitcnt lgkmcnt(6)
mqa 96,224,176
tq 252,9,63488
mqa 104,224,180
tq 254,17,0
buffer_load_dword v23, v20, s[28:31], 0 offen offset:0 sc1
s_add_u32 m0, s56, 26624
s_nop 0
s_nop 0
L_hop3_1_%=:
s_cbranch_execz L_hop7_1_%=
buffer_load_dwordx4 v10, s[72:75], s52 offen offset:0 lds
mqa 104,228,188
tq 224,12,51200
mqa 96,228,184
tq 226,12,53248
buffer_load_dwordx4 v11, s[72:75], s52 offen offset:1024 lds
mqa 96,232,208
tq 228,12,55296
mqa 104,232,212
tq 230,12,57344
mqa 104,236,220
tq 232,12,59392
mqa 96,236,216
tq 234,12,61440
s_waitcnt lgkmcnt(6)
mqa 100,240,176
tq 236,12,63488
mqa 108,240,180
tq 238,28,0
mqa 108,244,188
tq 240,13,51200
v_fma_f32 v232, v168, v104, v168
v_fma_f32 v233, v169, v105, v169
mqa 100,244,184
tq 242,13,53248
v_fma_f32 v234, v170, v106, v170
mqa 100,248,208
tq 244,13,55296
v_fma_f32 v235, v171, v107, v171
v_fma_f32 v224, v160, v96, v160
mqa 108,248,212
tq 246,13,57344
v_fma_f32 v225, v161, v97, v161
mqa 108,252,220
tq 248,13,59392
v_fma_f32 v226, v162, v98, v162
v_fma_f32 v227, v163, v99, v163
mqa 100,252,216
tq 250,13,61440
s_waitcnt lgkmcnt(6)
mqa 112,224,176
tq 252,13,63488
tq 254,29,0
s_add_u32 m0, s56, 108544
mqa 120,224,180
buffer_load_dwordx4 v30, s[72:75], s46 offen offset:0 lds
s_waitcnt vmcnt(25)
s_barrier
mqa 120,228,188
tq 224,16,10240
v_cvt_pk_bf16_f32 v52, v232, v233
v_cvt_pk_bf16_f32 v53, v234, v235
mqa 112,228,184
tq 226,16,12288
v_cvt_pk_bf16_f32 v48, v224, v225
mqa 112,232,208
tq 228,17,10240
v_cvt_pk_bf16_f32 v49, v226, v227
v_fma_f32 v236, v172, v108, v172
mqa 120,232,212
tq 230,17,12288
buffer_load_dwordx4 v31, s[72:75], s46 offen offset:1024 lds
mqa 120,236,220
tq 232,16,14336
v_fma_f32 v237, v173, v109, v173
v_fma_f32 v238, v174, v110, v174
mqa 112,236,216
tq 234,16,16384
s_waitcnt lgkmcnt(6)
mqa 116,240,176
tq 236,17,14336
v_fma_f32 v239, v175, v111, v175
v_fma_f32 v228, v164, v100, v164
mqa 124,240,180
tq 238,17,16384
v_fma_f32 v229, v165, v101, v165
mqa 124,244,188
tq 240,28,10240
v_fma_f32 v230, v166, v102, v166
v_fma_f32 v231, v167, v103, v167
mqa 116,244,184
tq 242,28,12288
v_cvt_pk_bf16_f32 v54, v236, v237
v_cvt_pk_bf16_f32 v55, v238, v239
mqa 116,248,208
tq 244,29,10240
mqa 124,248,212
tq 246,29,12288
buffer_store_dwordx4 v[52:55], v14, s[28:31], s81 offen offset:1024 sc1
v_cvt_pk_bf16_f32 v50, v228, v229
v_cvt_pk_bf16_f32 v51, v230, v231
buffer_store_dwordx4 v[48:51], v14, s[28:31], s81 offen offset:0 sc1
mqa 124,252,220
tq 248,28,14336
ds_read_b128 v[96:99], v24 offset:768
v_fma_f32 v248, v184, v120, v184
v_fma_f32 v249, v185, v121, v185
mqa 116,252,216
tq 250,28,16384
v_fma_f32 v250, v186, v122, v186
s_waitcnt lgkmcnt(7)
mqv 64,224,32
tq 252,29,14336
v_fma_f32 v251, v187, v123, v187
mqv 72,224,36
tq 254,29,16384
s_waitcnt vmcnt(6)
v_cmp_ne_u32 vcc, v23, v22
s_cbranch_vccnz L_slow0_1_%=
L_back0_1_%=:
buffer_load_dwordx4 a[0:3], v14, s[28:31], s82 offen offset:0 sc1
mqv 76,228,36
tq 224,4,10240
ds_read_b128 v[104:107], v24 offset:768
v_fma_f32 v240, v176, v112, v176
mqv 68,228,32
tq 226,4,12288
v_fma_f32 v241, v177, v113, v177
v_fma_f32 v242, v178, v114, v178
mqv 64,232,40
tq 228,5,10240
v_fma_f32 v243, v179, v115, v179
v_cvt_pk_bf16_f32 v60, v248, v249
v_cvt_pk_bf16_f32 v61, v250, v251
mqv 72,232,44
tq 230,5,12288
buffer_load_dwordx4 a[4:7], v14, s[28:31], s82 offen offset:1024 sc1
ds_read_b128 v[100:103], v24 offset:832
mqv 76,236,44
tq 232,4,14336
v_cvt_pk_bf16_f32 v56, v240, v241
v_cvt_pk_bf16_f32 v57, v242, v243
ds_read_b128 v[108:111], v24 offset:832
mqv 68,236,40
tq 234,4,16384
v_fma_f32 v252, v188, v124, v188
s_waitcnt lgkmcnt(9)
mqv 80,240,32
tq 236,5,14336
v_fma_f32 v253, v189, v125, v189
v_fma_f32 v254, v190, v126, v190
v_fma_f32 v255, v191, v127, v191
mqv 88,240,36
tq 238,5,16384
buffer_load_dwordx4 a[8:11], v14, s[28:31], s82 offen offset:2048 sc1
ds_read_b128 v[112:115], v24 offset:896
mqv 92,244,36
tq 240,6,10240
v_fma_f32 v244, v180, v116, v180
v_fma_f32 v245, v181, v117, v181
mqv 84,244,32
tq 242,6,12288
v_fma_f32 v246, v182, v118, v182
v_fma_f32 v247, v183, v119, v183
mqv 80,248,40
tq 244,7,10240
v_cvt_pk_bf16_f32 v62, v252, v253
v_cvt_pk_bf16_f32 v63, v254, v255
mqv 88,248,44
tq 246,7,12288
buffer_load_dwordx4 a[12:15], v14, s[28:31], s82 offen offset:3072 sc1
buffer_store_dwordx4 v[60:63], v14, s[28:31], s81 offen offset:3072 sc1
ds_read_b128 v[120:123], v24 offset:896
mqv 92,252,44
tq 248,6,14336
v_cvt_pk_bf16_f32 v58, v244, v245
v_cvt_pk_bf16_f32 v59, v246, v247
mqv 84,252,40
tq 250,6,16384
buffer_store_dwordx4 v[56:59], v14, s[28:31], s81 offen offset:2048 sc1
ds_read_b128 v[116:119], v24 offset:960
s_waitcnt lgkmcnt(9)
mqv 96,224,32
tq 252,7,14336
tq 254,7,16384
ds_read_b128 v[124:127], v24 offset:960
mqv 104,224,36
buffer_load_dwordx4 a[32:35], v14, s[28:31], s84 offen offset:0 sc1
s_barrier
mqv 108,228,36
tq 224,16,18432
mqv 100,228,32
tq 226,16,20480
buffer_load_dwordx4 a[36:39], v14, s[28:31], s84 offen offset:1024 sc1
mqv 96,232,40
tq 228,17,18432
mqv 104,232,44
tq 230,17,20480
buffer_load_dwordx4 a[40:43], v14, s[28:31], s84 offen offset:2048 sc1
mqv 108,236,44
tq 232,28,18432
mqv 100,236,40
tq 234,28,20480
s_waitcnt lgkmcnt(6)
mqv 112,240,32
tq 236,29,18432
mqv 120,240,36
tq 238,29,20480
buffer_load_dwordx4 a[44:47], v14, s[28:31], s84 offen offset:3072 sc1
mqv 124,244,36
tq 240,16,22528
mqv 116,244,32
tq 242,16,24576
mqv 112,248,40
tq 244,17,22528
s_add_u32 m0, s56, 100352
mqv 120,248,44
tq 246,17,24576
buffer_load_dwordx4 v30, s[72:75], s43 offen offset:0 lds
mqv 124,252,44
tq 248,28,22528
mqv 116,252,40
tq 250,28,24576
s_waitcnt lgkmcnt(6)
mqv 64,224,48
tq 252,29,22528
mqv 72,224,52
tq 254,29,24576
buffer_load_dwordx4 v31, s[72:75], s43 offen offset:1024 lds
mqv 76,228,52
tq 224,4,51200
mqv 68,228,48
tq 226,4,53248
mqv 80,232,48
tq 228,5,51200
mqv 88,232,52
tq 230,5,53248
buffer_load_dwordx4 a[64:67], v14, s[28:31], s86 offen offset:0 sc1
mqv 92,236,52
tq 232,6,51200
mqv 84,236,48
tq 234,6,53248
s_waitcnt lgkmcnt(6)
mqv 64,240,56
tq 236,7,51200
mqv 72,240,60
tq 238,7,53248
buffer_load_dwordx4 a[68:71], v14, s[28:31], s86 offen offset:1024 sc1
mqv 76,244,60
tq 240,4,55296
mqv 68,244,56
tq 242,4,57344
mqv 80,248,56
tq 244,5,55296
mqv 88,248,60
tq 246,5,57344
buffer_load_dwordx4 a[72:75], v14, s[28:31], s86 offen offset:2048 sc1
mqv 92,252,60
tq 248,6,55296
mqv 84,252,56
tq 250,6,57344
s_waitcnt lgkmcnt(6)
mqv 96,224,48
tq 252,7,55296
tq 254,7,57344
mqv 104,224,52
s_barrier
mqv 108,228,52
tq 224,4,59392
mqv 100,228,48
tq 226,4,61440
buffer_load_dwordx4 a[76:79], v14, s[28:31], s86 offen offset:3072 sc1
mqv 112,232,48
tq 228,5,59392
s_add_u32 m0, s56, 116736
mqv 120,232,52
tq 230,5,61440
buffer_load_dwordx4 v30, s[72:75], s51 offen offset:0 lds
mqv 124,236,52
tq 232,6,59392
mqv 116,236,48
tq 234,6,61440
s_waitcnt lgkmcnt(6)
mqv 96,240,56
tq 236,7,59392
mqv 104,240,60
tq 238,7,61440
buffer_load_dwordx4 v31, s[72:75], s51 offen offset:1024 lds
mqv 108,244,60
tq 240,4,63488
mqv 100,244,56
tq 242,8,0
mqv 112,248,56
tq 244,5,63488
mqv 120,248,60
tq 246,9,0
buffer_load_dwordx4 a[96:99], v14, s[28:31], s88 offen offset:0 sc1
mqv 124,252,60
tq 248,6,63488
mqv 116,252,56
tq 250,12,0
s_waitcnt vmcnt(13)
buffer_store_dword v22, v21, s[28:31], 0 offen offset:32 sc1
s_waitcnt lgkmcnt(6)
mqa 64,224,0
tq 252,7,63488
mqa 72,224,4
tq 254,13,0
buffer_load_dwordx4 a[100:103], v14, s[28:31], s88 offen offset:1024 sc1
mqa 76,228,4
tq 224,4,34816
mqa 68,228,0
tq 226,4,36864
mqa 80,232,0
tq 228,5,34816
mqa 88,232,4
tq 230,5,36864
buffer_load_dwordx4 a[104:107], v14, s[28:31], s88 offen offset:2048 sc1
mqa 92,236,4
tq 232,6,34816
mqa 84,236,0
tq 234,6,36864
s_waitcnt lgkmcnt(6)
mqa 64,240,8
tq 236,7,34816
mqa 72,240,12
tq 238,7,36864
buffer_load_dwordx4 a[108:111], v14, s[28:31], s88 offen offset:3072 sc1
mqa 76,244,12
tq 240,4,38912
mqa 68,244,8
tq 242,4,40960
mqa 80,248,8
tq 244,5,38912
mqa 88,248,12
tq 246,5,40960
buffer_load_dwordx4 a[128:131], v14, s[28:31], s90 offen offset:0 sc1
mqa 92,252,12
tq 248,6,38912
mqa 84,252,8
tq 250,6,40960
s_waitcnt lgkmcnt(6)
s_waitcnt vmcnt(14)
mqa 64,224,32
tq 252,7,38912
tq 254,7,40960
mqa 72,224,36
s_barrier
mqa 76,228,36
tq 224,8,2048
mqa 68,228,32
tq 226,8,4096
buffer_load_dwordx4 a[132:135], v14, s[28:31], s90 offen offset:1024 sc1
mqa 80,232,32
tq 228,9,2048
mqa 88,232,36
tq 230,9,4096
buffer_load_dwordx4 a[136:139], v14, s[28:31], s90 offen offset:2048 sc1
mqa 92,236,36
tq 232,12,2048
mqa 84,236,32
tq 234,12,4096
s_waitcnt lgkmcnt(6)
mqa 64,240,40
tq 236,13,2048
mqa 72,240,44
tq 238,13,4096
buffer_load_dwordx4 a[140:143], v14, s[28:31], s90 offen offset:3072 sc1
mqa 76,244,44
tq 240,8,6144
mqa 68,244,40
tq 242,8,8192
mqa 80,248,40
tq 244,9,6144
mqa 88,248,44
tq 246,9,8192
buffer_load_dwordx4 a[160:163], v14, s[28:31], s92 offen offset:0 sc1
mqa 92,252,44
tq 248,12,6144
mqa 84,252,40
tq 250,12,8192
s_waitcnt lgkmcnt(6)
mqa 96,224,0
tq 252,13,6144
mqa 104,224,4
tq 254,13,8192
buffer_load_dwordx4 a[164:167], v14, s[28:31], s92 offen offset:1024 sc1
mqa 108,228,4
tq 224,4,2048
mqa 100,228,0
tq 226,4,4096
mqa 112,232,0
tq 228,5,2048
mqa 120,232,4
tq 230,5,4096
buffer_load_dwordx4 a[168:171], v14, s[28:31], s92 offen offset:2048 sc1
mqa 124,236,4
tq 232,6,2048
mqa 116,236,0
tq 234,6,4096
s_waitcnt lgkmcnt(6)
mqa 96,240,8
tq 236,7,2048
mqa 104,240,12
tq 238,7,4096
buffer_load_dwordx4 a[172:175], v14, s[28:31], s92 offen offset:3072 sc1
mqa 108,244,12
tq 240,4,6144
mqa 100,244,8
tq 242,4,8192
mqa 112,248,8
tq 244,5,6144
s_add_u32 m0, s56, 124928
mqa 120,248,12
tq 246,5,8192
buffer_load_dwordx4 v10, s[72:75], s43 offen offset:0 lds
mqa 124,252,12
tq 248,6,6144
mqa 116,252,8
tq 250,6,8192
s_waitcnt lgkmcnt(6)
s_waitcnt vmcnt(16)
mqa 64,224,64
tq 252,7,6144
tq 254,7,8192
mqa 72,224,68
s_barrier
mqa 76,228,68
tq 224,8,26624
mqa 68,228,64
tq 226,8,28672
buffer_load_dwordx4 v11, s[72:75], s43 offen offset:1024 lds
mqa 80,232,64
tq 228,9,26624
s_add_u32 m0, s56, 141312
mqa 88,232,68
tq 230,9,28672
buffer_load_dwordx4 v30, s[76:79], s40 offen offset:0 lds
mqa 92,236,68
tq 232,12,26624
mqa 84,236,64
tq 234,12,28672
s_waitcnt lgkmcnt(6)
mqa 64,240,72
tq 236,13,26624
mqa 72,240,76
tq 238,13,28672
buffer_load_dwordx4 v31, s[76:79], s40 offen offset:1024 lds
mqa 76,244,76
tq 240,8,30720
mqa 68,244,72
tq 242,8,32768
mqa 80,248,72
tq 244,9,30720
s_add_u32 m0, s56, 10240
mqa 88,248,76
tq 246,9,32768
buffer_load_dwordx4 v10, s[72:75], s45 offen offset:0 lds
mqa 92,252,76
tq 248,12,30720
mqa 84,252,72
tq 250,12,32768
s_waitcnt lgkmcnt(6)
s_waitcnt vmcnt(13)
mqa 64,224,96
tq 252,13,30720
mqa 72,224,100
tq 254,13,32768
buffer_load_dwordx4 v11, s[72:75], s45 offen offset:1024 lds
mqa 76,228,100
tq 224,16,2048
mqa 68,228,96
tq 226,16,4096
mqa 80,232,96
tq 228,17,2048
mqa 88,232,100
tq 230,17,4096
buffer_load_dwordx4 a[192:195], v14, s[28:31], s94 offen offset:0 sc1
mqa 92,236,100
tq 232,28,2048
mqa 84,236,96
tq 234,28,4096
s_waitcnt lgkmcnt(6)
mqa 64,240,104
tq 236,29,2048
mqa 72,240,108
tq 238,29,4096
buffer_load_dwordx4 a[196:199], v14, s[28:31], s94 offen offset:1024 sc1
mqa 76,244,108
tq 240,16,6144
mqa 68,244,104
tq 242,16,8192
mqa 80,248,104
tq 244,17,6144
mqa 88,248,108
tq 246,17,8192
buffer_load_dwordx4 a[200:203], v14, s[28:31], s94 offen offset:2048 sc1
mqa 92,252,108
tq 248,28,6144
mqa 84,252,104
tq 250,28,8192
s_waitcnt lgkmcnt(6)
mqa 96,224,32
tq 252,29,6144
tq 254,29,8192
mqa 104,224,36
buffer_load_dword v23, v20, s[28:31], 0 offen offset:32 sc1
s_barrier
mqa 108,228,36
tq 224,8,10240
mqa 100,228,32
tq 226,8,12288
buffer_load_dwordx4 a[204:207], v14, s[28:31], s94 offen offset:3072 sc1
mqa 112,232,32
tq 228,9,10240
s_add_u32 m0, s56, 149504
mqa 120,232,36
tq 230,9,12288
buffer_load_dwordx4 v10, s[72:75], s47 offen offset:0 lds
mqa 124,236,36
tq 232,12,10240
mqa 116,236,32
tq 234,12,12288
s_waitcnt lgkmcnt(6)
mqa 96,240,40
tq 236,13,10240
mqa 104,240,44
tq 238,13,12288
buffer_load_dwordx4 v11, s[72:75], s47 offen offset:1024 lds
mqa 108,244,44
tq 240,8,14336
mqa 100,244,40
tq 242,8,16384
mqa 112,248,40
tq 244,9,14336
s_add_u32 m0, s56, 51200
mqa 120,248,44
tq 246,9,16384
buffer_load_dwordx4 v10, s[72:75], s51 offen offset:0 lds
mqa 124,252,44
tq 248,12,14336
L_hop4_1_%=:
s_cbranch_execz L_hop8_1_%=
mqa 116,252,40
tq 250,12,16384
s_waitcnt lgkmcnt(6)
s_waitcnt vmcnt(18)
mqa 64,224,128
tq 252,13,14336
mqa 72,224,132
tq 254,13,16384
buffer_load_dwordx4 v11, s[72:75], s51 offen offset:1024 lds
mqa 76,228,132
tq 224,4,26624
mqa 68,228,128
tq 226,4,28672
mqa 80,232,128
tq 228,5,26624
s_add_u32 m0, s56, 59392
mqa 88,232,132
tq 230,5,28672
buffer_load_dwordx4 v10, s[72:75], s53 offen offset:0 lds
mqa 92,236,132
tq 232,6,26624
mqa 84,236,128
tq 234,6,28672
s_waitcnt lgkmcnt(6)
mqa 64,240,136
tq 236,7,26624
mqa 72,240,140
tq 238,7,28672
buffer_load_dwordx4 v11, s[72:75], s53 offen offset:1024 lds
mqa 76,244,140
tq 240,4,30720
mqa 68,244,136
tq 242,4,32768
mqa 80,248,136
tq 244,5,30720
s_add_u32 m0, s56, 34816
mqa 88,248,140
tq 246,5,32768
buffer_load_dwordx4 v10, s[72:75], s55 offen offset:0 lds
mqa 92,252,140
tq 248,6,30720
mqa 84,252,136
tq 250,6,32768
s_waitcnt lgkmcnt(6)
s_waitcnt vmcnt(18)
mqa 64,224,160
tq 252,7,30720
tq 254,7,32768
mqa 72,224,164
s_waitcnt vmcnt(8)
v_cmp_ne_u32 vcc, v23, v22
s_cbranch_vccnz L_slow1_1_%=
L_back1_1_%=:
s_barrier
mqa 76,228,164
tq 224,8,43008
mqa 68,228,160
tq 226,8,45056
buffer_load_dwordx4 v11, s[72:75], s55 offen offset:1024 lds
mqa 80,232,160
tq 228,9,43008
mqa 88,232,164
tq 230,9,45056
buffer_load_dwordx4 a[16:19], v14, s[28:31], s83 offen offset:0 sc1
mqa 92,236,164
tq 232,12,43008
mqa 84,236,160
tq 234,12,45056
buffer_load_dwordx4 a[20:23], v14, s[28:31], s83 offen offset:1024 sc1
s_waitcnt lgkmcnt(6)
mqa 64,240,168
tq 236,13,43008
mqa 72,240,172
tq 238,13,45056
buffer_load_dwordx4 a[24:27], v14, s[28:31], s83 offen offset:2048 sc1
mqa 76,244,172
tq 240,8,47104
mqa 68,244,168
tq 242,8,49152
buffer_load_dwordx4 a[28:31], v14, s[28:31], s83 offen offset:3072 sc1
mqa 80,248,168
tq 244,9,47104
mqa 88,248,172
tq 246,9,49152
mqa 92,252,172
tq 248,12,47104
mqa 84,252,168
tq 250,12,49152
buffer_load_dwordx4 a[48:51], v14, s[28:31], s85 offen offset:0 sc1
s_waitcnt lgkmcnt(6)
mqa 96,224,64
tq 252,13,47104
mqa 104,224,68
tq 254,13,49152
buffer_load_dwordx4 a[52:55], v14, s[28:31], s85 offen offset:1024 sc1
mqa 108,228,68
tq 224,4,43008
mqa 100,228,64
tq 226,4,45056
buffer_load_dwordx4 a[56:59], v14, s[28:31], s85 offen offset:2048 sc1
mqa 112,232,64
tq 228,5,43008
mqa 120,232,68
tq 230,5,45056
buffer_load_dwordx4 a[60:63], v14, s[28:31], s85 offen offset:3072 sc1
mqa 124,236,68
tq 232,6,43008
mqa 116,236,64
tq 234,6,45056
s_waitcnt lgkmcnt(6)
mqa 96,240,72
tq 236,7,43008
mqa 104,240,76
tq 238,7,45056
mqa 108,244,76
tq 240,4,47104
mqa 100,244,72
tq 242,4,49152
mqa 112,248,72
tq 244,5,47104
mqa 120,248,76
tq 246,5,49152
mqa 124,252,76
tq 248,6,47104
mqa 116,252,72
tq 250,6,49152
buffer_load_dwordx4 a[80:83], v14, s[28:31], s87 offen offset:0 sc1
s_waitcnt lgkmcnt(6)
s_waitcnt vmcnt(17)
mqa 64,224,192
tq 252,7,47104
tq 254,7,49152
mqa 72,224,196
buffer_load_dwordx4 a[84:87], v14, s[28:31], s87 offen offset:1024 sc1
s_barrier
mqa 76,228,196
tq 224,8,59392
mqa 68,228,192
tq 226,8,61440
buffer_load_dwordx4 a[88:91], v14, s[28:31], s87 offen offset:2048 sc1
mqa 80,232,192
tq 228,9,59392
mqa 88,232,196
tq 230,9,61440
buffer_load_dwordx4 a[92:95], v14, s[28:31], s87 offen offset:3072 sc1
mqa 92,236,196
tq 232,12,59392
mqa 84,236,192
tq 234,12,61440
s_waitcnt lgkmcnt(6)
mqa 64,240,200
tq 236,13,59392
mqa 72,240,204
tq 238,13,61440
mqa 76,244,204
tq 240,8,63488
mqa 68,244,200
tq 242,16,0
mqa 80,248,200
tq 244,9,63488
mqa 88,248,204
tq 246,17,0
mqa 92,252,204
tq 248,12,63488
mqa 84,252,200
tq 250,28,0
buffer_load_dwordx4 a[112:115], v14, s[28:31], s89 offen offset:0 sc1
s_waitcnt lgkmcnt(6)
s_waitcnt vmcnt(9)
mqa 64,224,16
tq 252,13,63488
mqa 72,224,20
tq 254,29,0
buffer_load_dwordx4 a[116:119], v14, s[28:31], s89 offen offset:1024 sc1
mqa 76,228,20
tq 224,4,10240
mqa 68,228,16
tq 226,4,12288
buffer_load_dwordx4 a[120:123], v14, s[28:31], s89 offen offset:2048 sc1
mqa 80,232,16
tq 228,5,10240
mqa 88,232,20
tq 230,5,12288
buffer_load_dwordx4 a[124:127], v14, s[28:31], s89 offen offset:3072 sc1
mqa 92,236,20
tq 232,6,10240
mqa 84,236,16
tq 234,6,12288
s_waitcnt lgkmcnt(6)
mqa 64,240,24
tq 236,7,10240
mqa 72,240,28
tq 238,7,12288
mqa 76,244,28
tq 240,4,14336
mqa 68,244,24
tq 242,4,16384
mqa 80,248,24
tq 244,5,14336
mqa 88,248,28
tq 246,5,16384
mqa 92,252,28
tq 248,6,14336
mqa 84,252,24
tq 250,6,16384
buffer_load_dwordx4 a[144:147], v14, s[28:31], s91 offen offset:0 sc1
s_waitcnt lgkmcnt(6)
s_waitcnt vmcnt(9)
mqa 64,224,48
tq 252,7,14336
tq 254,7,16384
mqa 72,224,52
buffer_load_dwordx4 a[148:151], v14, s[28:31], s91 offen offset:1024 sc1
s_barrier
mqa 76,228,52
tq 224,16,18432
mqa 68,228,48
tq 226,16,20480
buffer_load_dwordx4 a[152:155], v14, s[28:31], s91 offen offset:2048 sc1
mqa 80,232,48
tq 228,17,18432
mqa 88,232,52
tq 230,17,20480
buffer_load_dwordx4 a[156:159], v14, s[28:31], s91 offen offset:3072 sc1
mqa 92,236,52
tq 232,28,18432
mqa 84,236,48
tq 234,28,20480
s_waitcnt lgkmcnt(6)
mqa 64,240,56
tq 236,29,18432
mqa 72,240,60
tq 238,29,20480
buffer_load_dword v25, v19, s[76:79], 0 offen
mqa 76,244,60
tq 240,16,22528
mqa 68,244,56
tq 242,16,24576
mqa 80,248,56
tq 244,17,22528
mqa 88,248,60
tq 246,17,24576
mqa 92,252,60
tq 248,28,22528
mqa 84,252,56
tq 250,28,24576
buffer_load_dwordx4 a[176:179], v14, s[28:31], s93 offen offset:0 sc1
s_waitcnt lgkmcnt(6)
s_waitcnt vmcnt(10)
mqa 64,224,80
tq 252,29,22528
mqa 72,224,84
tq 254,29,24576
buffer_load_dwordx4 a[180:183], v14, s[28:31], s93 offen offset:1024 sc1
mqa 76,228,84
tq 224,8,18432
mqa 68,228,80
tq 226,8,20480
buffer_load_dwordx4 a[184:187], v14, s[28:31], s93 offen offset:2048 sc1
mqa 80,232,80
tq 228,9,18432
mqa 88,232,84
tq 230,9,20480
buffer_load_dwordx4 a[188:191], v14, s[28:31], s93 offen offset:3072 sc1
mqa 92,236,84
tq 232,12,18432
mqa 84,236,80
tq 234,12,20480
buffer_load_dwordx4 a[208:211], v14, s[28:31], s95 offen offset:0 sc1
s_waitcnt lgkmcnt(6)
mqa 64,240,88
tq 236,13,18432
mqa 72,240,92
tq 238,13,20480
buffer_load_dwordx4 a[212:215], v14, s[28:31], s95 offen offset:1024 sc1
mqa 76,244,92
tq 240,8,22528
mqa 68,244,88
tq 242,8,24576
buffer_load_dwordx4 a[216:219], v14, s[28:31], s95 offen offset:2048 sc1
mqa 80,248,88
tq 244,9,22528
mqa 88,248,92
tq 246,9,24576
buffer_load_dwordx4 a[220:223], v14, s[28:31], s95 offen offset:3072 sc1
mqa 92,252,92
tq 248,12,22528
mqa 84,252,88
tq 250,12,24576
s_waitcnt lgkmcnt(6)
s_waitcnt vmcnt(13)
mqa 64,224,112
tq 252,13,22528
tq 254,13,24576
mqa 72,224,116
s_barrier
mqa 76,228,116
tq 224,4,51200
mqa 68,228,112
tq 226,4,53248
mqa 80,232,112
tq 228,5,51200
mqa 88,232,116
tq 230,5,53248
mqa 92,236,116
tq 232,6,51200
mqa 84,236,112
tq 234,6,53248
s_waitcnt lgkmcnt(6)
mqa 64,240,120
tq 236,7,51200
mqa 72,240,124
tq 238,7,53248
mqa 76,244,124
tq 240,4,55296
mqa 68,244,120
tq 242,4,57344
mqa 80,248,120
tq 244,5,55296
mqa 88,248,124
tq 246,5,57344
mqa 92,252,124
tq 248,6,55296
mqa 84,252,120
tq 250,6,57344
s_waitcnt lgkmcnt(6)
s_waitcnt vmcnt(9)
mqa 64,224,144
tq 252,7,55296
s_add_u32 m0, s56, 67584
mqa 72,224,148
tq 254,7,57344
buffer_load_dwordx4 v30, s[72:75], s50 offen offset:0 lds
mqa 76,228,148
tq 224,4,59392
mqa 68,228,144
tq 226,4,61440
buffer_load_dwordx4 v31, s[72:75], s50 offen offset:1024 lds
mqa 80,232,144
tq 228,4,63488
s_add_u32 m0, s56, 2048
mqa 88,232,148
tq 230,8,0
buffer_load_dwordx4 v30, s[72:75], s52 offen offset:0 lds
mqa 92,236,148
tq 232,4,34816
mqa 84,236,144
tq 234,4,36864
buffer_load_dwordx4 v31, s[72:75], s52 offen offset:1024 lds
s_waitcnt lgkmcnt(6)
mqa 64,240,152
tq 236,4,38912
mqa 72,240,156
tq 238,4,40960
mqa 76,244,156
tq 240,5,59392
mqa 68,244,152
tq 242,5,61440
mqa 80,248,152
tq 244,5,63488
mqa 88,248,156
tq 246,9,0
mqa 92,252,156
tq 248,5,34816
mqa 84,252,152
tq 250,5,36864
s_waitcnt lgkmcnt(6)
s_waitcnt vmcnt(8)
mqa 64,224,176
tq 252,5,38912
tq 254,5,40960
mqa 72,224,180
s_barrier
mqa 72,228,188
tq 224,6,59392
mqa 64,228,184
tq 226,6,61440
s_waitcnt vmcnt(4)
mqa 64,232,208
tq 228,6,63488
s_add_u32 m0, s56, 92160
mqa 72,232,212
tq 230,12,0
buffer_load_dwordx4 v30, s[76:79], s42 offen offset:0 lds
mqa 72,236,220
tq 232,6,34816
mqa 64,236,216
tq 234,6,36864
s_waitcnt lgkmcnt(6)
mqa 68,240,176
tq 236,6,38912
mqa 76,240,180
tq 238,6,40960
buffer_load_dwordx4 v31, s[76:79], s42 offen offset:1024 lds
mqa 76,244,188
tq 240,7,59392
v_fma_f32 v200, v136, v72, v200
v_fma_f32 v201, v137, v73, v201
mqa 68,244,184
tq 242,7,61440
v_fma_f32 v202, v138, v74, v202
mqa 68,248,208
tq 244,7,63488
v_fma_f32 v203, v139, v75, v203
v_fma_f32 v192, v128, v64, v192
mqa 76,248,212
tq 246,13,0
v_fma_f32 v193, v129, v65, v193
mqa 76,252,220
tq 248,7,34816
v_fma_f32 v194, v130, v66, v194
v_fma_f32 v195, v131, v67, v195
mqa 68,252,216
tq 250,7,36864
s_waitcnt lgkmcnt(6)
mqa 80,224,176
tq 252,7,38912
s_add_u32 m0, s56, 75776
mqa 88,224,180
tq 254,7,40960
buffer_load_dwordx4 v30, s[72:75], s54 offen offset:0 lds
mqa 88,228,188
tq 224,4,18432
v_cvt_pk_bf16_f32 v36, v200, v201
v_cvt_pk_bf16_f32 v37, v202, v203
mqa 80,228,184
tq 226,4,20480
buffer_load_dwordx4 v31, s[72:75], s54 offen offset:1024 lds
v_cvt_pk_bf16_f32 v32, v192, v193
mqa 80,232,208
tq 228,5,18432
v_cvt_pk_bf16_f32 v33, v194, v195
v_fma_f32 v204, v140, v76, v204
mqa 88,232,212
tq 230,5,20480
v_fma_f32 v205, v141, v77, v205
v_fma_f32 v206, v142, v78, v206
mqa 88,236,220
tq 232,6,18432
v_fma_f32 v207, v143, v79, v207
v_fma_f32 v196, v132, v68, v196
mqa 80,236,216
tq 234,6,20480
v_fma_f32 v197, v133, v69, v197
s_waitcnt lgkmcnt(6)
mqa 84,240,176
tq 236,7,18432
s_add_u32 m0, s56, 133120
v_fma_f32 v198, v134, v70, v198
mqa 92,240,180
tq 238,7,20480
buffer_load_dwordx4 v30, s[76:79], s46 offen offset:0 lds
mqa 92,244,188
tq 240,4,22528
v_fma_f32 v199, v135, v71, v199
v_cvt_pk_bf16_f32 v38, v204, v205
mqa 84,244,184
tq 242,4,24576
v_cvt_pk_bf16_f32 v39, v206, v207
buffer_store_dwordx4 v[36:39], v15, s[28:31], s80 offen offset:1024 sc1
mqa 84,248,208
tq 244,5,22528
v_cvt_pk_bf16_f32 v34, v196, v197
v_cvt_pk_bf16_f32 v35, v198, v199
mqa 92,248,212
tq 246,5,24576
buffer_load_dwordx4 v31, s[76:79], s46 offen offset:1024 lds
buffer_store_dwordx4 v[32:35], v15, s[28:31], s80 offen offset:0 sc1
mqa 92,252,220
tq 248,6,22528
v_fma_f32 v216, v152, v88, v216
v_fma_f32 v217, v153, v89, v217
mqa 84,252,216
tq 250,6,24576
v_fma_f32 v218, v154, v90, v218
s_waitcnt lgkmcnt(6)
mqa 96,224,96
tq 252,7,22528
tq 254,7,24576
s_add_u32 m0, s56, 26624
mqa 104,224,100
buffer_load_dwordx4 v30, s[76:79], s50 offen offset:0 lds
s_waitcnt vmcnt(9)
s_barrier
mqa 108,228,100
tq 224,8,2048
v_fma_f32 v219, v155, v91, v219
v_fma_f32 v208, v144, v80, v208
mqa 100,228,96
tq 226,8,4096
v_fma_f32 v209, v145, v81, v209
v_fma_f32 v210, v146, v82, v210
mqa 112,232,96
tq 228,9,2048
v_fma_f32 v211, v147, v83, v211
v_cvt_pk_bf16_f32 v44, v216, v217
v_cvt_pk_bf16_f32 v45, v218, v219
)ASM"
R"ASM(
mqa 120,232,100
tq 230,9,4096
buffer_load_dwordx4 v31, s[76:79], s50 offen offset:1024 lds
v_cvt_pk_bf16_f32 v40, v208, v209
mqa 124,236,100
tq 232,12,2048
v_cvt_pk_bf16_f32 v41, v210, v211
v_fma_f32 v220, v156, v92, v220
mqa 116,236,96
tq 234,12,4096
v_fma_f32 v221, v157, v93, v221
s_waitcnt lgkmcnt(6)
mqa 96,240,104
tq 236,13,2048
s_add_u32 m0, s56, 108544
v_fma_f32 v222, v158, v94, v222
mqa 104,240,108
tq 238,13,4096
buffer_load_dwordx4 v30, s[72:75], s45 offen offset:0 lds
v_fma_f32 v223, v159, v95, v223
mqa 108,244,108
tq 240,8,6144
v_fma_f32 v212, v148, v84, v212
v_fma_f32 v213, v149, v85, v213
mqa 100,244,104
tq 242,8,8192
v_fma_f32 v214, v150, v86, v214
v_fma_f32 v215, v151, v87, v215
mqa 112,248,104
tq 244,9,6144
v_cvt_pk_bf16_f32 v46, v220, v221
v_cvt_pk_bf16_f32 v47, v222, v223
mqa 120,248,108
tq 246,9,8192
buffer_load_dwordx4 v31, s[72:75], s45 offen offset:1024 lds
buffer_store_dwordx4 v[44:47], v15, s[28:31], s80 offen offset:3072 sc1
mqa 124,252,108
s_nop 0
L_hop5_1_%=:
s_cbranch_execz L_ret_1_%=
tq 248,12,6144
v_cvt_pk_bf16_f32 v42, v212, v213
v_cvt_pk_bf16_f32 v43, v214, v215
mqa 116,252,104
tq 250,12,8192
buffer_store_dwordx4 v[40:43], v15, s[28:31], s80 offen offset:2048 sc1
s_waitcnt lgkmcnt(6)
mqa 96,224,128
tq 252,13,6144
s_add_u32 m0, s56, 43008
mqa 104,224,132
tq 254,13,8192
buffer_load_dwordx4 v30, s[72:75], s47 offen offset:0 lds
mqa 108,228,132
tq 224,4,2048
ds_read_b128 v[64:67], v24 offset:1024
mqa 100,228,128
tq 226,4,4096
buffer_load_dwordx4 v31, s[72:75], s47 offen offset:1024 lds
mqa 112,232,128
tq 228,5,2048
ds_read_b128 v[72:75], v24 offset:1024
mqa 120,232,132
tq 230,5,4096
ds_read_b128 v[68:71], v24 offset:1088
mqa 124,236,132
tq 232,6,2048
s_add_u32 m0, s56, 124928
ds_read_b128 v[76:79], v24 offset:1088
mqa 116,236,128
tq 234,6,4096
buffer_load_dwordx4 v30, s[72:75], s49 offen offset:0 lds
s_waitcnt lgkmcnt(10)
mqa 96,240,136
tq 236,7,2048
mqa 104,240,140
tq 238,7,4096
buffer_load_dwordx4 v31, s[72:75], s49 offen offset:1024 lds
mqa 108,244,140
tq 240,4,6144
ds_read_b128 v[80:83], v24 offset:1152
mqa 100,244,136
tq 242,4,8192
mqa 112,248,136
tq 244,5,6144
s_add_u32 m0, s56, 10240
mqa 120,248,140
tq 246,5,8192
buffer_load_dwordx4 v30, s[72:75], s53 offen offset:0 lds
mqa 124,252,140
tq 248,6,6144
ds_read_b128 v[88:91], v24 offset:1152
mqa 116,252,136
tq 250,6,8192
s_waitcnt lgkmcnt(8)
mqa 96,224,160
tq 252,7,6144
tq 254,7,8192
mqa 104,224,164
s_waitcnt vmcnt(15)
s_barrier
mqa 108,228,164
tq 224,8,10240
ds_read_b128 v[84:87], v24 offset:1216
mqa 100,228,160
tq 226,8,12288
buffer_load_dwordx4 v31, s[72:75], s53 offen offset:1024 lds
mqa 112,232,160
tq 228,9,10240
s_add_u32 m0, s56, 149504
mqa 120,232,164
tq 230,9,12288
buffer_load_dwordx4 v30, s[76:79], s54 offen offset:0 lds
mqa 124,236,164
tq 232,12,10240
ds_read_b128 v[92:95], v24 offset:1216
mqa 116,236,160
tq 234,12,12288
s_waitcnt lgkmcnt(8)
mqa 96,240,168
tq 236,13,10240
mqa 104,240,172
tq 238,13,12288
buffer_load_dwordx4 v31, s[76:79], s54 offen offset:1024 lds
mqa 108,244,172
tq 240,8,14336
mqa 100,244,168
tq 242,8,16384
mqa 112,248,168
tq 244,9,14336
s_add_u32 m0, s56, 51200
mqa 120,248,172
tq 246,9,16384
buffer_load_dwordx4 v30, s[72:75], s55 offen offset:0 lds
mqa 124,252,172
tq 248,12,14336
mqa 116,252,168
tq 250,12,16384
s_waitcnt lgkmcnt(6)
mqa 96,224,192
tq 252,13,14336
mqa 104,224,196
tq 254,13,16384
buffer_load_dwordx4 v31, s[72:75], s55 offen offset:1024 lds
mqa 108,228,196
tq 224,8,34816
mqa 100,228,192
tq 226,8,36864
mqa 112,232,192
tq 228,9,34816
s_add_u32 m0, s56, 83968
mqa 120,232,196
tq 230,9,36864
buffer_load_dwordx4 v10, s[76:79], s40 offen offset:0 lds
mqa 124,236,196
tq 232,12,34816
mqa 116,236,192
tq 234,12,36864
s_waitcnt lgkmcnt(6)
mqa 96,240,200
tq 236,13,34816
mqa 104,240,204
tq 238,13,36864
buffer_load_dwordx4 v11, s[76:79], s40 offen offset:1024 lds
mqa 108,244,204
tq 240,8,38912
mqa 100,244,200
tq 242,8,40960
mqa 112,248,200
tq 244,9,38912
s_add_u32 m0, s56, 34816
mqa 120,248,204
tq 246,9,40960
buffer_load_dwordx4 v10, s[76:79], s41 offen offset:0 lds
mqa 124,252,204
tq 248,12,38912
mqa 116,252,200
tq 250,12,40960
s_waitcnt vmcnt(13)
buffer_store_dword v22, v21, s[28:31], 0 offen offset:64 sc1
s_waitcnt lgkmcnt(6)
mqa 96,224,16
tq 252,13,38912
tq 254,13,40960
mqa 104,224,20
s_waitcnt vmcnt(12)
s_barrier
mqa 108,228,20
tq 224,8,43008
mqa 100,228,16
tq 226,8,45056
buffer_load_dwordx4 v11, s[76:79], s41 offen offset:1024 lds
mqa 112,232,16
tq 228,9,43008
s_add_u32 m0, s56, 59392
mqa 120,232,20
tq 230,9,45056
buffer_load_dwordx4 v30, s[76:79], s41 offen offset:0 lds
mqa 124,236,20
tq 232,12,43008
mqa 116,236,16
tq 234,12,45056
s_waitcnt lgkmcnt(6)
mqa 96,240,24
tq 236,13,43008
mqa 104,240,28
tq 238,13,45056
buffer_load_dwordx4 v31, s[76:79], s41 offen offset:1024 lds
mqa 108,244,28
tq 240,8,47104
mqa 100,244,24
tq 242,8,49152
mqa 112,248,24
tq 244,9,47104
s_add_u32 m0, s56, 18432
mqa 120,248,28
tq 246,9,49152
buffer_load_dwordx4 v10, s[76:79], s42 offen offset:0 lds
mqa 124,252,28
tq 248,12,47104
mqa 116,252,24
tq 250,12,49152
s_waitcnt lgkmcnt(6)
mqa 96,224,48
tq 252,13,47104
mqa 104,224,52
tq 254,13,49152
buffer_load_dwordx4 v11, s[76:79], s42 offen offset:1024 lds
mqa 108,228,52
tq 224,4,43008
mqa 100,228,48
tq 226,4,45056
mqa 112,232,48
tq 228,5,43008
s_add_u32 m0, s56, 67584
mqa 120,232,52
tq 230,5,45056
buffer_load_dwordx4 v10, s[76:79], s44 offen offset:0 lds
mqa 124,236,52
tq 232,6,43008
mqa 116,236,48
tq 234,6,45056
s_waitcnt lgkmcnt(6)
mqa 96,240,56
tq 236,7,43008
mqa 104,240,60
tq 238,7,45056
buffer_load_dwordx4 v11, s[76:79], s44 offen offset:1024 lds
mqa 108,244,60
tq 240,4,47104
mqa 100,244,56
tq 242,4,49152
mqa 112,248,56
tq 244,5,47104
s_add_u32 m0, s56, 2048
mqa 120,248,60
tq 246,5,49152
buffer_load_dwordx4 v30, s[76:79], s44 offen offset:0 lds
mqa 124,252,60
tq 248,6,47104
mqa 116,252,56
tq 250,6,49152
s_waitcnt lgkmcnt(6)
mqa 96,224,80
tq 252,7,47104
tq 254,7,49152
mqa 104,224,84
s_waitcnt vmcnt(18)
s_barrier
mqa 108,228,84
tq 224,8,59392
mqa 100,228,80
tq 226,8,61440
buffer_load_dwordx4 v31, s[76:79], s44 offen offset:1024 lds
mqa 112,232,80
tq 228,9,59392
s_add_u32 m0, s56, 75776
mqa 120,232,84
tq 230,9,61440
buffer_load_dwordx4 v10, s[76:79], s49 offen offset:0 lds
mqa 124,236,84
tq 232,12,59392
mqa 116,236,80
tq 234,12,61440
s_waitcnt lgkmcnt(6)
mqa 96,240,88
tq 236,13,59392
mqa 104,240,92
tq 238,13,61440
buffer_load_dwordx4 v11, s[76:79], s49 offen offset:1024 lds
mqa 108,244,92
tq 240,8,63488
mqa 100,244,88
tq 242,16,0
mqa 112,248,88
tq 244,9,63488
s_add_u32 m0, s56, 100352
mqa 120,248,92
tq 246,17,0
buffer_load_dwordx4 v10, s[76:79], s46 offen offset:0 lds
mqa 124,252,92
tq 248,12,63488
mqa 116,252,88
tq 250,28,0
s_waitcnt lgkmcnt(6)
mqa 96,224,112
tq 252,13,63488
mqa 104,224,116
tq 254,29,0
buffer_load_dwordx4 v11, s[76:79], s46 offen offset:1024 lds
mqa 108,228,116
tq 224,8,51200
mqa 100,228,112
tq 226,8,53248
mqa 112,232,112
tq 228,9,51200
mqa 120,232,116
tq 230,9,53248
mqa 124,236,116
tq 232,12,51200
mqa 116,236,112
tq 234,12,53248
s_waitcnt lgkmcnt(6)
mqa 96,240,120
tq 236,13,51200
mqa 104,240,124
tq 238,13,53248
mqa 108,244,124
tq 240,8,55296
mqa 100,244,120
tq 242,8,57344
mqa 112,248,120
tq 244,9,55296
mqa 120,248,124
tq 246,9,57344
mqa 124,252,124
tq 248,12,55296
mqa 116,252,120
tq 250,12,57344
s_waitcnt lgkmcnt(6)
mqa 96,224,144
tq 252,13,55296
tq 254,13,57344
s_add_u32 m0, s56, 108544
mqa 104,224,148
buffer_load_dwordx4 v10, s[76:79], s48 offen offset:0 lds
s_waitcnt vmcnt(18)
s_barrier
mqa 108,228,148
tq 224,4,10240
mqa 100,228,144
tq 226,4,12288
buffer_load_dwordx4 v11, s[76:79], s48 offen offset:1024 lds
mqa 112,232,144
tq 228,4,14336
mqa 120,232,148
tq 230,4,16384
mqa 124,236,148
tq 232,4,51200
mqa 116,236,144
tq 234,4,53248
s_waitcnt lgkmcnt(6)
mqa 96,240,152
tq 236,4,55296
mqa 104,240,156
tq 238,4,57344
mqa 108,244,156
tq 240,5,10240
mqa 100,244,152
tq 242,5,12288
mqa 112,248,152
tq 244,5,14336
mqa 120,248,156
tq 246,5,16384
mqa 124,252,156
tq 248,5,51200
mqa 116,252,152
tq 250,5,53248
s_waitcnt lgkmcnt(6)
mqa 96,224,176
tq 252,5,55296
s_add_u32 m0, s56, 43008
mqa 104,224,180
tq 254,5,57344
buffer_load_dwordx4 v30, s[76:79], s48 offen offset:0 lds
mqa 104,228,188
tq 224,6,10240
mqa 96,228,184
tq 226,6,12288
buffer_load_dwordx4 v31, s[76:79], s48 offen offset:1024 lds
mqa 96,232,208
tq 228,6,14336
mqa 104,232,212
tq 230,6,16384
mqa 104,236,220
tq 232,6,51200
mqa 96,236,216
tq 234,6,53248
s_waitcnt lgkmcnt(6)
mqa 100,240,176
tq 236,6,55296
mqa 108,240,180
tq 238,6,57344
mqa 108,244,188
tq 240,7,10240
v_fma_f32 v232, v168, v104, v232
v_fma_f32 v233, v169, v105, v233
mqa 100,244,184
tq 242,7,12288
v_fma_f32 v234, v170, v106, v234
mqa 100,248,208
tq 244,7,14336
v_fma_f32 v235, v171, v107, v235
v_fma_f32 v224, v160, v96, v224
mqa 108,248,212
tq 246,7,16384
v_fma_f32 v225, v161, v97, v225
mqa 108,252,220
tq 248,7,51200
v_fma_f32 v226, v162, v98, v226
v_fma_f32 v227, v163, v99, v227
mqa 100,252,216
tq 250,7,53248
s_waitcnt lgkmcnt(6)
mqa 112,224,176
tq 252,7,55296
tq 254,7,57344
mqa 120,224,180
buffer_load_dword v23, v20, s[28:31], 0 offen offset:64 sc1
s_add_u32 m0, s56, 124928
s_nop 0
buffer_load_dwordx4 v10, s[76:79], s50 offen offset:0 lds
s_waitcnt vmcnt(21)
s_barrier
mqa 120,228,188
tq 224,8,18432
v_cvt_pk_bf16_f32 v52, v232, v233
v_cvt_pk_bf16_f32 v53, v234, v235
mqa 112,228,184
tq 226,8,20480
v_cvt_pk_bf16_f32 v48, v224, v225
mqa 112,232,208
tq 228,9,18432
v_cvt_pk_bf16_f32 v49, v226, v227
v_fma_f32 v236, v172, v108, v236
mqa 120,232,212
tq 230,9,20480
buffer_load_dwordx4 v11, s[76:79], s50 offen offset:1024 lds
mqa 120,236,220
tq 232,8,22528
v_fma_f32 v237, v173, v109, v237
v_fma_f32 v238, v174, v110, v238
mqa 112,236,216
tq 234,8,24576
s_waitcnt lgkmcnt(6)
mqa 116,240,176
tq 236,9,22528
v_fma_f32 v239, v175, v111, v239
v_fma_f32 v228, v164, v100, v228
mqa 124,240,180
tq 238,9,24576
v_fma_f32 v229, v165, v101, v229
mqa 124,244,188
tq 240,12,18432
v_fma_f32 v230, v166, v102, v230
v_fma_f32 v231, v167, v103, v231
mqa 116,244,184
tq 242,12,20480
v_cvt_pk_bf16_f32 v54, v236, v237
v_cvt_pk_bf16_f32 v55, v238, v239
mqa 116,248,208
tq 244,13,18432
mqa 124,248,212
tq 246,13,20480
buffer_store_dwordx4 v[52:55], v15, s[28:31], s81 offen offset:1024 sc1
v_cvt_pk_bf16_f32 v50, v228, v229
v_cvt_pk_bf16_f32 v51, v230, v231
buffer_store_dwordx4 v[48:51], v15, s[28:31], s81 offen offset:0 sc1
mqa 124,252,220
tq 248,12,22528
ds_read_b128 v[96:99], v24 offset:1280
v_fma_f32 v248, v184, v120, v248
v_fma_f32 v249, v185, v121, v249
mqa 116,252,216
tq 250,12,24576
s_waitcnt lgkmcnt(7)
mqv 64,224,32
tq 252,13,22528
v_fma_f32 v250, v186, v122, v250
v_fma_f32 v251, v187, v123, v251
mqv 72,224,36
tq 254,13,24576
ds_read_b128 v[104:107], v24 offset:1280
v_fma_f32 v240, v176, v112, v240
mqv 76,228,36
tq 224,16,10240
v_fma_f32 v241, v177, v113, v241
v_fma_f32 v242, v178, v114, v242
mqv 68,228,32
tq 226,16,12288
v_fma_f32 v243, v179, v115, v243
v_cvt_pk_bf16_f32 v60, v248, v249
mqv 64,232,40
tq 228,17,10240
v_cvt_pk_bf16_f32 v61, v250, v251
ds_read_b128 v[100:103], v24 offset:1344
mqv 72,232,44
tq 230,17,12288
v_cvt_pk_bf16_f32 v56, v240, v241
v_cvt_pk_bf16_f32 v57, v242, v243
mqv 76,236,44
tq 232,16,14336
ds_read_b128 v[108:111], v24 offset:1344
v_fma_f32 v252, v188, v124, v252
v_fma_f32 v253, v189, v125, v253
mqv 68,236,40
tq 234,16,16384
v_fma_f32 v254, v190, v126, v254
s_waitcnt lgkmcnt(9)
mqv 80,240,32
tq 236,17,14336
v_fma_f32 v255, v191, v127, v255
ds_read_b128 v[112:115], v24 offset:1408
mqv 88,240,36
tq 238,17,16384
v_fma_f32 v244, v180, v116, v244
v_fma_f32 v245, v181, v117, v245
mqv 92,244,36
tq 240,28,10240
v_fma_f32 v246, v182, v118, v246
v_fma_f32 v247, v183, v119, v247
mqv 84,244,32
tq 242,28,12288
v_cvt_pk_bf16_f32 v62, v252, v253
v_cvt_pk_bf16_f32 v63, v254, v255
mqv 80,248,40
tq 244,29,10240
mqv 88,248,44
tq 246,29,12288
buffer_store_dwordx4 v[60:63], v15, s[28:31], s81 offen offset:3072 sc1
ds_read_b128 v[120:123], v24 offset:1408
v_cvt_pk_bf16_f32 v58, v244, v245
v_cvt_pk_bf16_f32 v59, v246, v247
mqv 92,252,44
tq 248,28,14336
mqv 84,252,40
tq 250,28,16384
buffer_store_dwordx4 v[56:59], v15, s[28:31], s81 offen offset:2048 sc1
ds_read_b128 v[116:119], v24 offset:1472
ds_read_b128 v[124:127], v24 offset:1472
s_waitcnt lgkmcnt(9)
mqv 96,224,32
tq 252,29,14336
tq 254,29,16384
mqv 104,224,36
s_waitcnt vmcnt(6)
v_cmp_ne_u32 vcc, v23, v22
s_cbranch_vccnz L_slow2_1_%=
L_back2_1_%=:
buffer_load_dwordx4 a[0:3], v15, s[28:31], s82 offen offset:0 sc1
s_barrier
mqv 108,228,36
tq 224,4,34816
mqv 100,228,32
tq 226,4,36864
buffer_load_dwordx4 a[4:7], v15, s[28:31], s82 offen offset:1024 sc1
mqv 96,232,40
tq 228,5,34816
mqv 104,232,44
tq 230,5,36864
buffer_load_dwordx4 a[8:11], v15, s[28:31], s82 offen offset:2048 sc1
mqv 108,236,44
L_hop6_1_%=:
s_cbranch_execz L_ret_1_%=
tq 232,6,34816
mqv 100,236,40
tq 234,6,36864
buffer_load_dwordx4 a[12:15], v15, s[28:31], s82 offen offset:3072 sc1
s_waitcnt lgkmcnt(6)
mqv 112,240,32
tq 236,7,34816
mqv 120,240,36
tq 238,7,36864
mqv 124,244,36
tq 240,4,38912
mqv 116,244,32
tq 242,4,40960
mqv 112,248,40
tq 244,5,38912
s_add_u32 m0, s56, 116736
mqv 120,248,44
tq 246,5,40960
buffer_load_dwordx4 v30, s[76:79], s43 offen offset:0 lds
mqv 124,252,44
tq 248,6,38912
mqv 116,252,40
tq 250,6,40960
s_waitcnt lgkmcnt(6)
mqv 64,224,48
tq 252,7,38912
mqv 72,224,52
tq 254,7,40960
buffer_load_dwordx4 v31, s[76:79], s43 offen offset:1024 lds
mqv 76,228,52
tq 224,4,59392
mqv 68,228,48
tq 226,4,61440
mqv 80,232,48
tq 228,5,59392
mqv 88,232,52
tq 230,5,61440
buffer_load_dwordx4 a[32:35], v15, s[28:31], s84 offen offset:0 sc1
mqv 92,236,52
tq 232,6,59392
mqv 84,236,48
tq 234,6,61440
s_waitcnt lgkmcnt(6)
mqv 64,240,56
tq 236,7,59392
mqv 72,240,60
tq 238,7,61440
buffer_load_dwordx4 a[36:39], v15, s[28:31], s84 offen offset:1024 sc1
mqv 76,244,60
tq 240,4,63488
mqv 68,244,56
tq 242,8,0
mqv 80,248,56
tq 244,5,63488
mqv 88,248,60
tq 246,9,0
buffer_load_dwordx4 a[40:43], v15, s[28:31], s84 offen offset:2048 sc1
mqv 92,252,60
tq 248,6,63488
mqv 84,252,56
tq 250,12,0
buffer_load_dwordx4 a[44:47], v15, s[28:31], s84 offen offset:3072 sc1
s_waitcnt lgkmcnt(6)
mqv 96,224,48
tq 252,7,63488
tq 254,13,0
mqv 104,224,52
s_barrier
mqv 108,228,52
tq 224,4,18432
mqv 100,228,48
tq 226,4,20480
mqv 112,232,48
tq 228,5,18432
s_add_u32 m0, s56, 10240
mqv 120,232,52
tq 230,5,20480
buffer_load_dwordx4 v30, s[76:79], s51 offen offset:0 lds
mqv 124,236,52
tq 232,6,18432
mqv 116,236,48
tq 234,6,20480
s_waitcnt lgkmcnt(6)
mqv 96,240,56
tq 236,7,18432
mqv 104,240,60
tq 238,7,20480
buffer_load_dwordx4 v31, s[76:79], s51 offen offset:1024 lds
mqv 108,244,60
tq 240,4,22528
mqv 100,244,56
tq 242,4,24576
mqv 112,248,56
tq 244,5,22528
mqv 120,248,60
tq 246,5,24576
buffer_load_dwordx4 a[64:67], v15, s[28:31], s86 offen offset:0 sc1
mqv 124,252,60
tq 248,6,22528
mqv 116,252,56
tq 250,6,24576
s_waitcnt vmcnt(13)
buffer_store_dword v22, v21, s[28:31], 0 offen offset:96 sc1
s_waitcnt lgkmcnt(6)
s_waitcnt vmcnt(10)
mqa 64,224,0
tq 252,7,22528
mqa 72,224,4
tq 254,7,24576
buffer_load_dwordx4 a[68:71], v15, s[28:31], s86 offen offset:1024 sc1
mqa 76,228,4
tq 224,8,26624
mqa 68,228,0
tq 226,8,28672
mqa 80,232,0
tq 228,9,26624
mqa 88,232,4
tq 230,9,28672
buffer_load_dwordx4 a[72:75], v15, s[28:31], s86 offen offset:2048 sc1
mqa 92,236,4
tq 232,12,26624
mqa 84,236,0
tq 234,12,28672
s_waitcnt lgkmcnt(6)
mqa 64,240,8
tq 236,13,26624
mqa 72,240,12
tq 238,13,28672
buffer_load_dwordx4 a[76:79], v15, s[28:31], s86 offen offset:3072 sc1
mqa 76,244,12
tq 240,8,30720
mqa 68,244,8
tq 242,8,32768
mqa 80,248,8
tq 244,9,30720
mqa 88,248,12
tq 246,9,32768
buffer_load_dwordx4 a[96:99], v15, s[28:31], s88 offen offset:0 sc1
mqa 92,252,12
tq 248,12,30720
mqa 84,252,8
tq 250,12,32768
s_waitcnt lgkmcnt(6)
mqa 96,224,0
tq 252,13,30720
tq 254,13,32768
mqa 104,224,4
s_barrier
mqa 108,228,4
tq 224,8,2048
mqa 100,228,0
tq 226,8,4096
buffer_load_dwordx4 a[100:103], v15, s[28:31], s88 offen offset:1024 sc1
mqa 112,232,0
tq 228,9,2048
mqa 120,232,4
tq 230,9,4096
buffer_load_dwordx4 a[104:107], v15, s[28:31], s88 offen offset:2048 sc1
mqa 124,236,4
tq 232,12,2048
mqa 116,236,0
tq 234,12,4096
s_waitcnt lgkmcnt(6)
mqa 96,240,8
tq 236,13,2048
mqa 104,240,12
tq 238,13,4096
buffer_load_dwordx4 a[108:111], v15, s[28:31], s88 offen offset:3072 sc1
mqa 108,244,12
tq 240,8,6144
mqa 100,244,8
tq 242,8,8192
mqa 112,248,8
tq 244,9,6144
mqa 120,248,12
tq 246,9,8192
buffer_load_dwordx4 a[128:131], v15, s[28:31], s90 offen offset:0 sc1
mqa 124,252,12
tq 248,12,6144
mqa 116,252,8
tq 250,12,8192
s_waitcnt lgkmcnt(6)
s_waitcnt vmcnt(12)
mqa 64,224,32
tq 252,13,6144
mqa 72,224,36
tq 254,13,8192
buffer_load_dwordx4 a[132:135], v15, s[28:31], s90 offen offset:1024 sc1
mqa 76,228,36
tq 224,4,2048
mqa 68,228,32
tq 226,4,4096
mqa 80,232,32
tq 228,5,2048
mqa 88,232,36
tq 230,5,4096
buffer_load_dwordx4 a[136:139], v15, s[28:31], s90 offen offset:2048 sc1
mqa 92,236,36
tq 232,6,2048
mqa 84,236,32
tq 234,6,4096
s_waitcnt lgkmcnt(6)
mqa 64,240,40
tq 236,7,2048
mqa 72,240,44
tq 238,7,4096
buffer_load_dwordx4 a[140:143], v15, s[28:31], s90 offen offset:3072 sc1
mqa 76,244,44
tq 240,4,6144
mqa 68,244,40
tq 242,4,8192
mqa 80,248,40
tq 244,5,6144
s_add_u32 m0, s56, 51200
mqa 88,248,44
tq 246,5,8192
buffer_load_dwordx4 v10, s[76:79], s52 offen offset:0 lds
mqa 92,252,44
tq 248,6,6144
mqa 84,252,40
tq 250,6,8192
s_waitcnt lgkmcnt(6)
mqa 96,224,32
tq 252,7,6144
tq 254,7,8192
mqa 104,224,36
s_barrier
mqa 108,228,36
tq 224,8,34816
mqa 100,228,32
tq 226,8,36864
buffer_load_dwordx4 v11, s[76:79], s52 offen offset:1024 lds
mqa 112,232,32
tq 228,9,34816
s_add_u32 m0, s56, 83968
mqa 120,232,36
tq 230,9,36864
buffer_load_dwordx4 v30, s[76:79], s52 offen offset:0 lds
mqa 124,236,36
tq 232,12,34816
mqa 116,236,32
tq 234,12,36864
s_waitcnt lgkmcnt(6)
mqa 96,240,40
tq 236,13,34816
mqa 104,240,44
tq 238,13,36864
buffer_load_dwordx4 v31, s[76:79], s52 offen offset:1024 lds
mqa 108,244,44
tq 240,8,38912
mqa 100,244,40
tq 242,8,40960
mqa 112,248,40
tq 244,9,38912
mqa 120,248,44
tq 246,9,40960
buffer_load_dwordx4 a[160:163], v15, s[28:31], s92 offen offset:0 sc1
mqa 124,252,44
tq 248,12,38912
mqa 116,252,40
tq 250,12,40960
s_waitcnt lgkmcnt(6)
s_waitcnt vmcnt(13)
mqa 64,224,64
tq 252,13,38912
mqa 72,224,68
tq 254,13,40960
buffer_load_dwordx4 a[164:167], v15, s[28:31], s92 offen offset:1024 sc1
mqa 76,228,68
tq 224,16,2048
mqa 68,228,64
tq 226,16,4096
mqa 80,232,64
tq 228,17,2048
mqa 88,232,68
tq 230,17,4096
buffer_load_dwordx4 a[168:171], v15, s[28:31], s92 offen offset:2048 sc1
mqa 92,236,68
tq 232,28,2048
mqa 84,236,64
tq 234,28,4096
s_waitcnt lgkmcnt(6)
mqa 64,240,72
tq 236,29,2048
mqa 72,240,76
tq 238,29,4096
buffer_load_dwordx4 a[172:175], v15, s[28:31], s92 offen offset:3072 sc1
mqa 76,244,76
tq 240,16,6144
mqa 68,244,72
tq 242,16,8192
mqa 80,248,72
tq 244,17,6144
s_add_u32 m0, s56, 34816
mqa 88,248,76
tq 246,17,8192
buffer_load_dwordx4 v10, s[76:79], s54 offen offset:0 lds
mqa 92,252,76
tq 248,28,6144
mqa 84,252,72
tq 250,28,8192
s_waitcnt lgkmcnt(6)
mqa 96,224,64
tq 252,29,6144
tq 254,29,8192
mqa 104,224,68
s_barrier
mqa 108,228,68
tq 224,8,43008
mqa 100,228,64
tq 226,8,45056
buffer_load_dwordx4 v11, s[76:79], s54 offen offset:1024 lds
mqa 112,232,64
tq 228,9,43008
mqa 120,232,68
tq 230,9,45056
buffer_load_dwordx4 a[192:195], v15, s[28:31], s94 offen offset:0 sc1
mqa 124,236,68
tq 232,12,43008
mqa 116,236,64
tq 234,12,45056
s_waitcnt lgkmcnt(6)
mqa 96,240,72
tq 236,13,43008
mqa 104,240,76
tq 238,13,45056
buffer_load_dwordx4 a[196:199], v15, s[28:31], s94 offen offset:1024 sc1
mqa 108,244,76
tq 240,8,47104
mqa 100,244,72
tq 242,8,49152
mqa 112,248,72
tq 244,9,47104
mqa 120,248,76
tq 246,9,49152
buffer_load_dwordx4 a[200:203], v15, s[28:31], s94 offen offset:2048 sc1
mqa 124,252,76
tq 248,12,47104
mqa 116,252,72
tq 250,12,49152
s_waitcnt lgkmcnt(6)
s_waitcnt vmcnt(17)
mqa 64,224,96
tq 252,13,47104
mqa 72,224,100
tq 254,13,49152
buffer_load_dwordx4 a[204:207], v15, s[28:31], s94 offen offset:3072 sc1
mqa 76,228,100
tq 224,4,43008
mqa 68,228,96
tq 226,4,45056
mqa 80,232,96
tq 228,5,43008
s_add_u32 m0, s56, 141312
mqa 88,232,100
tq 230,5,45056
buffer_load_dwordx4 v10, s[76:79], s43 offen offset:0 lds
mqa 92,236,100
tq 232,6,43008
mqa 84,236,96
tq 234,6,45056
s_waitcnt lgkmcnt(6)
mqa 64,240,104
tq 236,7,43008
mqa 72,240,108
tq 238,7,45056
buffer_load_dwordx4 v11, s[76:79], s43 offen offset:1024 lds
mqa 76,244,108
tq 240,4,47104
mqa 68,244,104
tq 242,4,49152
mqa 80,248,104
tq 244,5,47104
s_add_u32 m0, s56, 18432
mqa 88,248,108
tq 246,5,49152
buffer_load_dwordx4 v10, s[76:79], s45 offen offset:0 lds
mqa 92,252,108
tq 248,6,47104
mqa 84,252,104
tq 250,6,49152
s_waitcnt lgkmcnt(6)
mqa 96,224,96
tq 252,7,47104
tq 254,7,49152
mqa 104,224,100
buffer_load_dword v23, v20, s[28:31], 0 offen offset:96 sc1
s_barrier
mqa 108,228,100
tq 224,8,59392
mqa 100,228,96
tq 226,8,61440
buffer_load_dwordx4 v11, s[76:79], s45 offen offset:1024 lds
mqa 112,232,96
tq 228,9,59392
s_add_u32 m0, s56, 59392
mqa 120,232,100
tq 230,9,61440
buffer_load_dwordx4 v10, s[76:79], s47 offen offset:0 lds
mqa 124,236,100
tq 232,12,59392
mqa 116,236,96
tq 234,12,61440
s_waitcnt lgkmcnt(6)
mqa 96,240,104
tq 236,13,59392
mqa 104,240,108
tq 238,13,61440
buffer_load_dwordx4 v11, s[76:79], s47 offen offset:1024 lds
mqa 108,244,108
tq 240,8,63488
mqa 100,244,104
tq 242,16,0
mqa 112,248,104
tq 244,9,63488
s_add_u32 m0, s56, 67584
mqa 120,248,108
tq 246,17,0
buffer_load_dwordx4 v10, s[76:79], s51 offen offset:0 lds
mqa 124,252,108
tq 248,12,63488
mqa 116,252,104
tq 250,28,0
s_waitcnt lgkmcnt(6)
s_waitcnt vmcnt(22)
mqa 64,224,128
tq 252,13,63488
mqa 72,224,132
tq 254,29,0
buffer_load_dwordx4 v11, s[76:79], s51 offen offset:1024 lds
mqa 76,228,132
tq 224,4,26624
mqa 68,228,128
tq 226,4,28672
mqa 80,232,128
tq 228,5,26624
s_add_u32 m0, s56, 92160
mqa 88,232,132
tq 230,5,28672
buffer_load_dwordx4 v10, s[76:79], s53 offen offset:0 lds
mqa 92,236,132
tq 232,6,26624
mqa 84,236,128
tq 234,6,28672
s_waitcnt lgkmcnt(6)
mqa 64,240,136
tq 236,7,26624
mqa 72,240,140
tq 238,7,28672
buffer_load_dwordx4 v11, s[76:79], s53 offen offset:1024 lds
mqa 76,244,140
tq 240,4,30720
mqa 68,244,136
tq 242,4,32768
mqa 80,248,136
tq 244,5,30720
s_add_u32 m0, s56, 2048
mqa 88,248,140
tq 246,5,32768
buffer_load_dwordx4 v10, s[76:79], s55 offen offset:0 lds
mqa 92,252,140
tq 248,6,30720
mqa 84,252,136
tq 250,6,32768
s_waitcnt lgkmcnt(6)
mqa 96,224,128
tq 252,7,30720
tq 254,7,32768
mqa 104,224,132
s_waitcnt vmcnt(8)
v_cmp_ne_u32 vcc, v23, v22
s_cbranch_vccnz L_slow3_1_%=
L_back3_1_%=:
s_barrier
mqa 108,228,132
tq 224,4,51200
mqa 100,228,128
tq 226,4,53248
buffer_load_dwordx4 v11, s[76:79], s55 offen offset:1024 lds
mqa 112,232,128
tq 228,5,51200
mqa 120,232,132
tq 230,5,53248
buffer_load_dwordx4 a[16:19], v15, s[28:31], s83 offen offset:0 sc1
mqa 124,236,132
tq 232,6,51200
mqa 116,236,128
tq 234,6,53248
s_waitcnt lgkmcnt(6)
mqa 96,240,136
tq 236,7,51200
mqa 104,240,140
tq 238,7,53248
buffer_load_dwordx4 a[20:23], v15, s[28:31], s83 offen offset:1024 sc1
mqa 108,244,140
tq 240,4,55296
mqa 100,244,136
tq 242,4,57344
mqa 112,248,136
tq 244,5,55296
mqa 120,248,140
tq 246,5,57344
buffer_load_dwordx4 a[24:27], v15, s[28:31], s83 offen offset:2048 sc1
mqa 124,252,140
tq 248,6,55296
mqa 116,252,136
tq 250,6,57344
s_waitcnt lgkmcnt(6)
mqa 64,224,160
tq 252,7,55296
mqa 72,224,164
tq 254,7,57344
buffer_load_dwordx4 a[28:31], v15, s[28:31], s83 offen offset:3072 sc1
mqa 76,228,164
tq 224,8,18432
mqa 68,228,160
tq 226,8,20480
mqa 80,232,160
tq 228,9,18432
mqa 88,232,164
tq 230,9,20480
buffer_load_dwordx4 a[48:51], v15, s[28:31], s85 offen offset:0 sc1
mqa 92,236,164
tq 232,12,18432
mqa 84,236,160
tq 234,12,20480
s_waitcnt lgkmcnt(6)
mqa 64,240,168
tq 236,13,18432
mqa 72,240,172
tq 238,13,20480
buffer_load_dwordx4 a[52:55], v15, s[28:31], s85 offen offset:1024 sc1
mqa 76,244,172
s_nop 0
L_hop7_1_%=:
s_cbranch_execz L_ret_1_%=
tq 240,8,22528
mqa 68,244,168
tq 242,8,24576
mqa 80,248,168
tq 244,9,22528
mqa 88,248,172
tq 246,9,24576
buffer_load_dwordx4 a[56:59], v15, s[28:31], s85 offen offset:2048 sc1
mqa 92,252,172
tq 248,12,22528
mqa 84,252,168
tq 250,12,24576
s_waitcnt lgkmcnt(6)
mqa 96,224,160
tq 252,13,22528
tq 254,13,24576
mqa 104,224,164
s_barrier
mqa 108,228,164
tq 224,4,34816
mqa 100,228,160
tq 226,4,36864
buffer_load_dwordx4 a[60:63], v15, s[28:31], s85 offen offset:3072 sc1
mqa 112,232,160
tq 228,5,34816
mqa 120,232,164
tq 230,5,36864
buffer_load_dwordx4 a[80:83], v15, s[28:31], s87 offen offset:0 sc1
mqa 124,236,164
tq 232,6,34816
mqa 116,236,160
tq 234,6,36864
s_waitcnt lgkmcnt(6)
mqa 96,240,168
tq 236,7,34816
mqa 104,240,172
tq 238,7,36864
buffer_load_dwordx4 a[84:87], v15, s[28:31], s87 offen offset:1024 sc1
mqa 108,244,172
tq 240,4,38912
mqa 100,244,168
tq 242,4,40960
mqa 112,248,168
tq 244,5,38912
mqa 120,248,172
tq 246,5,40960
buffer_load_dwordx4 a[88:91], v15, s[28:31], s87 offen offset:2048 sc1
mqa 124,252,172
tq 248,6,38912
mqa 116,252,168
tq 250,6,40960
s_waitcnt lgkmcnt(6)
mqa 64,224,192
tq 252,7,38912
mqa 72,224,196
tq 254,7,40960
buffer_load_dwordx4 a[92:95], v15, s[28:31], s87 offen offset:3072 sc1
mqa 76,228,196
tq 224,16,18432
mqa 68,228,192
tq 226,16,20480
mqa 80,232,192
tq 228,17,18432
mqa 88,232,196
tq 230,17,20480
buffer_load_dwordx4 a[112:115], v15, s[28:31], s89 offen offset:0 sc1
mqa 92,236,196
tq 232,28,18432
mqa 84,236,192
tq 234,28,20480
s_waitcnt lgkmcnt(6)
mqa 64,240,200
tq 236,29,18432
mqa 72,240,204
tq 238,29,20480
buffer_load_dwordx4 a[116:119], v15, s[28:31], s89 offen offset:1024 sc1
mqa 76,244,204
tq 240,16,22528
mqa 68,244,200
tq 242,16,24576
mqa 80,248,200
tq 244,17,22528
mqa 88,248,204
tq 246,17,24576
buffer_load_dwordx4 a[120:123], v15, s[28:31], s89 offen offset:2048 sc1
mqa 92,252,204
tq 248,28,22528
mqa 84,252,200
tq 250,28,24576
s_waitcnt lgkmcnt(6)
mqa 96,224,192
tq 252,29,22528
tq 254,29,24576
mqa 104,224,196
s_waitcnt vmcnt(23)
s_barrier
mqa 108,228,196
tq 224,16,10240
mqa 100,228,192
tq 226,16,12288
buffer_load_dwordx4 a[124:127], v15, s[28:31], s89 offen offset:3072 sc1
mqa 112,232,192
tq 228,17,10240
mqa 120,232,196
tq 230,17,12288
buffer_load_dwordx4 a[144:147], v15, s[28:31], s91 offen offset:0 sc1
mqa 124,236,196
tq 232,28,10240
mqa 116,236,192
tq 234,28,12288
s_waitcnt lgkmcnt(6)
mqa 96,240,200
tq 236,29,10240
mqa 104,240,204
tq 238,29,12288
buffer_load_dwordx4 a[148:151], v15, s[28:31], s91 offen offset:1024 sc1
mqa 108,244,204
tq 240,16,14336
mqa 100,244,200
tq 242,16,16384
mqa 112,248,200
tq 244,17,14336
mqa 120,248,204
tq 246,17,16384
buffer_load_dwordx4 a[152:155], v15, s[28:31], s91 offen offset:2048 sc1
mqa 124,252,204
tq 248,28,14336
mqa 116,252,200
tq 250,28,16384
s_waitcnt lgkmcnt(6)
s_waitcnt vmcnt(15)
mqa 64,224,16
tq 252,29,14336
mqa 72,224,20
tq 254,29,16384
buffer_load_dwordx4 a[156:159], v15, s[28:31], s91 offen offset:3072 sc1
mqa 76,228,20
tq 224,4,18432
mqa 68,228,16
tq 226,4,20480
mqa 80,232,16
tq 228,5,18432
s_add_u32 m0, s56, 100352
mqa 88,232,20
tq 230,5,20480
buffer_load_dwordx4 v30, s[76:79], s45 offen offset:0 lds
mqa 92,236,20
tq 232,6,18432
mqa 84,236,16
tq 234,6,20480
s_waitcnt lgkmcnt(6)
mqa 64,240,24
tq 236,7,18432
mqa 72,240,28
tq 238,7,20480
buffer_load_dwordx4 v31, s[76:79], s45 offen offset:1024 lds
mqa 76,244,28
tq 240,4,22528
mqa 68,244,24
tq 242,4,24576
mqa 80,248,24
tq 244,5,22528
s_add_u32 m0, s56, 108544
mqa 88,248,28
tq 246,5,24576
buffer_load_dwordx4 v30, s[76:79], s47 offen offset:0 lds
mqa 92,252,28
tq 248,6,22528
mqa 84,252,24
tq 250,6,24576
s_waitcnt lgkmcnt(6)
s_waitcnt vmcnt(15)
mqa 64,224,48
tq 252,7,22528
tq 254,7,24576
mqa 72,224,52
s_barrier
mqa 76,228,52
tq 224,4,59392
mqa 68,228,48
tq 226,4,61440
buffer_load_dwordx4 v31, s[76:79], s47 offen offset:1024 lds
mqa 80,232,48
tq 228,5,59392
s_add_u32 m0, s56, 133120
mqa 88,232,52
tq 230,5,61440
buffer_load_dwordx4 v30, s[76:79], s49 offen offset:0 lds
mqa 92,236,52
tq 232,6,59392
mqa 84,236,48
tq 234,6,61440
s_waitcnt lgkmcnt(6)
mqa 64,240,56
tq 236,7,59392
mqa 72,240,60
tq 238,7,61440
buffer_load_dwordx4 v31, s[76:79], s49 offen offset:1024 lds
mqa 76,244,60
tq 240,4,63488
mqa 68,244,56
tq 242,8,0
mqa 80,248,56
tq 244,5,63488
mqa 88,248,60
tq 246,9,0
buffer_load_dwordx4 a[176:179], v15, s[28:31], s93 offen offset:0 sc1
mqa 92,252,60
tq 248,6,63488
mqa 84,252,56
tq 250,12,0
buffer_load_dwordx4 a[180:183], v15, s[28:31], s93 offen offset:1024 sc1
s_waitcnt lgkmcnt(6)
s_waitcnt vmcnt(16)
mqa 64,224,80
tq 252,7,63488
mqa 72,224,84
tq 254,13,0
buffer_load_dwordx4 a[184:187], v15, s[28:31], s93 offen offset:2048 sc1
mqa 76,228,84
tq 224,8,10240
mqa 68,228,80
tq 226,8,12288
buffer_load_dwordx4 a[188:191], v15, s[28:31], s93 offen offset:3072 sc1
mqa 80,232,80
tq 228,9,10240
mqa 88,232,84
tq 230,9,12288
buffer_load_dwordx4 a[208:211], v15, s[28:31], s95 offen offset:0 sc1
mqa 92,236,84
tq 232,12,10240
mqa 84,236,80
tq 234,12,12288
buffer_load_dwordx4 a[212:215], v15, s[28:31], s95 offen offset:1024 sc1
s_waitcnt lgkmcnt(6)
mqa 64,240,88
tq 236,13,10240
mqa 72,240,92
tq 238,13,12288
buffer_load_dwordx4 a[216:219], v15, s[28:31], s95 offen offset:2048 sc1
mqa 76,244,92
tq 240,8,14336
mqa 68,244,88
tq 242,8,16384
buffer_load_dwordx4 a[220:223], v15, s[28:31], s95 offen offset:3072 sc1
mqa 80,248,88
tq 244,9,14336
mqa 88,248,92
tq 246,9,16384
mqa 92,252,92
tq 248,12,14336
mqa 84,252,88
tq 250,12,16384
s_waitcnt lgkmcnt(6)
s_waitcnt vmcnt(18)
mqa 64,224,112
tq 252,13,14336
tq 254,13,16384
mqa 72,224,116
s_barrier
mqa 76,228,116
tq 224,8,2048
mqa 68,228,112
tq 226,8,4096
mqa 80,232,112
tq 228,9,2048
mqa 88,232,116
tq 230,9,4096
mqa 92,236,116
tq 232,12,2048
mqa 84,236,112
tq 234,12,4096
s_waitcnt lgkmcnt(6)
mqa 64,240,120
tq 236,13,2048
mqa 72,240,124
tq 238,13,4096
mqa 76,244,124
tq 240,8,6144
mqa 68,244,120
tq 242,8,8192
mqa 80,248,120
tq 244,9,6144
s_add_u32 m0, s56, 43008
mqa 88,248,124
tq 246,9,8192
buffer_load_dwordx4 v30, s[76:79], s53 offen offset:0 lds
mqa 92,252,124
tq 248,12,6144
mqa 84,252,120
tq 250,12,8192
s_waitcnt lgkmcnt(6)
s_waitcnt vmcnt(15)
mqa 64,224,144
tq 252,13,6144
mqa 72,224,148
tq 254,13,8192
buffer_load_dwordx4 v31, s[76:79], s53 offen offset:1024 lds
mqa 76,228,148
tq 224,8,26624
s_add_u32 m0, s56, 124928
mqa 68,228,144
tq 226,8,28672
buffer_load_dwordx4 v30, s[76:79], s55 offen offset:0 lds
mqa 80,232,144
tq 228,8,30720
mqa 88,232,148
tq 230,8,32768
buffer_load_dwordx4 v31, s[76:79], s55 offen offset:1024 lds
mqa 92,236,148
tq 232,4,2048
mqa 84,236,144
tq 234,4,4096
s_waitcnt lgkmcnt(6)
mqa 64,240,152
tq 236,4,6144
mqa 72,240,156
tq 238,4,8192
mqa 76,244,156
tq 240,9,26624
mqa 68,244,152
tq 242,9,28672
mqa 80,248,152
tq 244,9,30720
mqa 88,248,156
tq 246,9,32768
mqa 92,252,156
tq 248,5,2048
mqa 84,252,152
tq 250,5,4096
s_waitcnt lgkmcnt(6)
s_waitcnt vmcnt(8)
mqa 64,224,176
tq 252,5,6144
tq 254,5,8192
mqa 72,224,180
s_barrier
mqa 72,228,188
tq 224,12,26624
mqa 64,228,184
tq 226,12,28672
s_waitcnt vmcnt(4)
mqa 64,232,208
tq 228,12,30720
mqa 72,232,212
tq 230,12,32768
mqa 72,236,220
tq 232,6,2048
mqa 64,236,216
tq 234,6,4096
s_waitcnt lgkmcnt(6)
mqa 68,240,176
tq 236,6,6144
mqa 76,240,180
tq 238,6,8192
mqa 76,244,188
tq 240,13,26624
v_fma_f32 v200, v136, v72, v200
mqa 68,244,184
tq 242,13,28672
v_fma_f32 v201, v137, v73, v201
mqa 68,248,208
tq 244,13,30720
v_fma_f32 v202, v138, v74, v202
mqa 76,248,212
tq 246,13,32768
v_fma_f32 v203, v139, v75, v203
mqa 76,252,220
tq 248,7,2048
v_fma_f32 v192, v128, v64, v192
mqa 68,252,216
tq 250,7,4096
s_waitcnt lgkmcnt(6)
mqa 80,224,176
tq 252,7,6144
v_fma_f32 v193, v129, v65, v193
mqa 88,224,180
tq 254,7,8192
v_fma_f32 v194, v130, v66, v194
mqa 88,228,188
tq 224,8,51200
v_fma_f32 v195, v131, v67, v195
mqa 80,228,184
tq 226,8,53248
v_cvt_pk_bf16_f32 v36, v200, v201
mqa 80,232,208
tq 228,8,55296
v_cvt_pk_bf16_f32 v37, v202, v203
mqa 88,232,212
tq 230,8,57344
)ASM"
R"ASM(
v_cvt_pk_bf16_f32 v32, v192, v193
mqa 88,236,220
tq 232,8,34816
v_cvt_pk_bf16_f32 v33, v194, v195
mqa 80,236,216
tq 234,8,36864
s_waitcnt lgkmcnt(6)
mqa 84,240,176
tq 236,8,38912
v_fma_f32 v204, v140, v76, v204
mqa 92,240,180
tq 238,8,40960
v_fma_f32 v205, v141, v77, v205
mqa 92,244,188
tq 240,8,43008
v_fma_f32 v206, v142, v78, v206
mqa 84,244,184
tq 242,8,45056
v_fma_f32 v207, v143, v79, v207
mqa 84,248,208
tq 244,8,47104
v_fma_f32 v196, v132, v68, v196
mqa 92,248,212
tq 246,8,49152
v_fma_f32 v197, v133, v69, v197
mqa 92,252,220
tq 248,16,2048
v_fma_f32 v198, v134, v70, v198
mqa 84,252,216
tq 250,16,4096
s_waitcnt lgkmcnt(6)
mqa 96,224,16
tq 252,16,6144
tq 254,16,8192
mqa 104,224,20
s_waitcnt vmcnt(0)
s_barrier
mqa 104,228,28
tq 224,4,10240
v_fma_f32 v199, v135, v71, v199
v_cvt_pk_bf16_f32 v38, v204, v205
mqa 96,228,24
tq 226,4,12288
v_cvt_pk_bf16_f32 v39, v206, v207
mqa 96,232,48
tq 228,4,14336
v_permlane16_swap_b32 v36, v38
v_permlane16_swap_b32 v37, v39
mqa 104,232,52
tq 230,4,16384
buffer_store_dwordx4 v[36:39], v18, s[32:35], s70 offen offset:0 sc1
mqa 104,236,60
tq 232,4,43008
v_cvt_pk_bf16_f32 v34, v196, v197
v_cvt_pk_bf16_f32 v35, v198, v199
mqa 96,236,56
tq 234,4,45056
s_waitcnt lgkmcnt(6)
mqa 96,240,80
tq 236,4,47104
v_permlane16_swap_b32 v32, v34
mqa 104,240,84
tq 238,4,49152
v_permlane16_swap_b32 v33, v35
mqa 104,244,92
tq 240,8,59392
mqa 96,244,88
tq 242,8,61440
buffer_store_dwordx4 v[32:35], v18, s[32:35], 0 offen offset:0 sc1
v_fma_f32 v216, v152, v88, v216
mqa 96,248,112
tq 244,8,63488
v_fma_f32 v217, v153, v89, v217
mqa 104,248,116
tq 246,16,0
v_fma_f32 v218, v154, v90, v218
mqa 104,252,124
tq 248,9,51200
v_fma_f32 v219, v155, v91, v219
mqa 96,252,120
tq 250,9,53248
s_waitcnt lgkmcnt(6)
mqa 96,224,144
tq 252,9,55296
v_fma_f32 v208, v144, v80, v208
mqa 104,224,148
tq 254,9,57344
v_fma_f32 v209, v145, v81, v209
mqa 104,228,156
tq 224,9,34816
v_fma_f32 v210, v146, v82, v210
mqa 96,228,152
tq 226,9,36864
v_fma_f32 v211, v147, v83, v211
mqa 96,232,176
tq 228,9,38912
v_cvt_pk_bf16_f32 v44, v216, v217
mqa 104,232,180
tq 230,9,40960
v_cvt_pk_bf16_f32 v45, v218, v219
mqa 104,236,188
tq 232,9,43008
v_cvt_pk_bf16_f32 v40, v208, v209
mqa 96,236,184
tq 234,9,45056
s_waitcnt lgkmcnt(6)
mqa 96,240,208
tq 236,9,47104
v_cvt_pk_bf16_f32 v41, v210, v211
mqa 104,240,212
tq 238,9,49152
v_fma_f32 v220, v156, v92, v220
mqa 104,244,220
tq 240,17,2048
v_fma_f32 v221, v157, v93, v221
mqa 96,244,216
tq 242,17,4096
v_fma_f32 v222, v158, v94, v222
mqa 100,248,16
tq 244,17,6144
v_fma_f32 v223, v159, v95, v223
mqa 108,248,20
tq 246,17,8192
v_fma_f32 v212, v148, v84, v212
mqa 108,252,28
tq 248,5,10240
v_fma_f32 v213, v149, v85, v213
mqa 100,252,24
tq 250,5,12288
s_waitcnt lgkmcnt(6)
mqa 100,224,48
tq 252,5,14336
tq 254,5,16384
mqa 108,224,52
s_barrier
mqa 108,228,60
tq 224,5,43008
v_fma_f32 v214, v150, v86, v214
mqa 100,228,56
tq 226,5,45056
v_fma_f32 v215, v151, v87, v215
mqa 100,232,80
tq 228,5,47104
v_cvt_pk_bf16_f32 v46, v220, v221
L_hop8_1_%=:
s_cbranch_execz L_ret_1_%=
mqa 108,232,84
tq 230,5,49152
v_cvt_pk_bf16_f32 v47, v222, v223
mqa 108,236,92
tq 232,9,59392
v_permlane16_swap_b32 v44, v46
mqa 100,236,88
tq 234,9,61440
s_waitcnt lgkmcnt(6)
mqa 100,240,112
tq 236,9,63488
v_permlane16_swap_b32 v45, v47
mqa 108,240,116
tq 238,17,0
buffer_store_dwordx4 v[44:47], v18, s[32:35], s70 offen offset:64 sc1
mqa 108,244,124
tq 240,12,51200
v_cvt_pk_bf16_f32 v42, v212, v213
mqa 100,244,120
tq 242,12,53248
v_cvt_pk_bf16_f32 v43, v214, v215
mqa 100,248,144
tq 244,12,55296
v_permlane16_swap_b32 v40, v42
mqa 108,248,148
tq 246,12,57344
v_permlane16_swap_b32 v41, v43
mqa 108,252,156
tq 248,12,34816
mqa 100,252,152
tq 250,12,36864
buffer_store_dwordx4 v[40:43], v18, s[32:35], 0 offen offset:64 sc1
s_waitcnt lgkmcnt(6)
mqa 100,224,176
tq 252,12,38912
v_fma_f32 v232, v168, v104, v232
mqa 108,224,180
tq 254,12,40960
v_fma_f32 v233, v169, v105, v233
mqa 108,228,188
tq 224,12,43008
v_fma_f32 v234, v170, v106, v234
mqa 100,228,184
tq 226,12,45056
v_fma_f32 v235, v171, v107, v235
mqa 100,232,208
tq 228,12,47104
v_fma_f32 v224, v160, v96, v224
mqa 108,232,212
tq 230,12,49152
v_fma_f32 v225, v161, v97, v225
mqa 108,236,220
tq 232,28,2048
v_fma_f32 v226, v162, v98, v226
mqa 100,236,216
tq 234,28,4096
s_waitcnt lgkmcnt(6)
mqa 112,240,16
tq 236,28,6144
v_fma_f32 v227, v163, v99, v227
mqa 120,240,20
tq 238,28,8192
v_cvt_pk_bf16_f32 v52, v232, v233
mqa 120,244,28
tq 240,6,10240
v_cvt_pk_bf16_f32 v53, v234, v235
mqa 112,244,24
tq 242,6,12288
v_cvt_pk_bf16_f32 v48, v224, v225
mqa 112,248,48
tq 244,6,14336
v_cvt_pk_bf16_f32 v49, v226, v227
mqa 120,248,52
tq 246,6,16384
v_fma_f32 v236, v172, v108, v236
mqa 120,252,60
tq 248,6,43008
v_fma_f32 v237, v173, v109, v237
mqa 112,252,56
tq 250,6,45056
s_waitcnt lgkmcnt(6)
mqa 112,224,80
tq 252,6,47104
tq 254,6,49152
mqa 120,224,84
s_barrier
mqa 120,228,92
tq 224,12,59392
v_fma_f32 v238, v174, v110, v238
mqa 112,228,88
tq 226,12,61440
v_fma_f32 v239, v175, v111, v239
mqa 112,232,112
tq 228,12,63488
v_fma_f32 v228, v164, v100, v228
mqa 120,232,116
tq 230,28,0
v_fma_f32 v229, v165, v101, v229
mqa 120,236,124
tq 232,13,51200
v_fma_f32 v230, v166, v102, v230
mqa 112,236,120
tq 234,13,53248
s_waitcnt lgkmcnt(6)
mqa 112,240,144
tq 236,13,55296
v_fma_f32 v231, v167, v103, v231
mqa 120,240,148
tq 238,13,57344
v_cvt_pk_bf16_f32 v54, v236, v237
mqa 120,244,156
tq 240,13,34816
v_cvt_pk_bf16_f32 v55, v238, v239
mqa 112,244,152
tq 242,13,36864
v_permlane16_swap_b32 v52, v54
mqa 112,248,176
tq 244,13,38912
v_permlane16_swap_b32 v53, v55
mqa 120,248,180
tq 246,13,40960
buffer_store_dwordx4 v[52:55], v3, s[32:35], s70 offen offset:0 sc1
mqa 120,252,188
tq 248,13,43008
v_cvt_pk_bf16_f32 v50, v228, v229
mqa 112,252,184
tq 250,13,45056
s_waitcnt lgkmcnt(6)
mqa 112,224,208
tq 252,13,47104
v_cvt_pk_bf16_f32 v51, v230, v231
mqa 120,224,212
tq 254,13,49152
v_permlane16_swap_b32 v48, v50
mqa 120,228,220
tq 224,29,2048
v_permlane16_swap_b32 v49, v51
mqa 112,228,216
tq 226,29,4096
buffer_store_dwordx4 v[48:51], v3, s[32:35], 0 offen offset:0 sc1
mqa 116,232,16
tq 228,29,6144
mqa 124,232,20
tq 230,29,8192
mqa 124,236,28
tq 232,7,10240
v_fma_f32 v248, v184, v120, v248
mqa 116,236,24
tq 234,7,12288
s_waitcnt lgkmcnt(6)
mqa 116,240,48
tq 236,7,14336
v_fma_f32 v249, v185, v121, v249
mqa 124,240,52
tq 238,7,16384
v_fma_f32 v250, v186, v122, v250
mqa 124,244,60
tq 240,7,43008
v_fma_f32 v251, v187, v123, v251
mqa 116,244,56
tq 242,7,45056
v_fma_f32 v240, v176, v112, v240
mqa 116,248,80
tq 244,7,47104
v_fma_f32 v241, v177, v113, v241
mqa 124,248,84
tq 246,7,49152
v_fma_f32 v242, v178, v114, v242
mqa 124,252,92
tq 248,13,59392
v_fma_f32 v243, v179, v115, v243
mqa 116,252,88
tq 250,13,61440
s_waitcnt lgkmcnt(6)
mqa 124,224,116
tq 252,13,63488
v_cvt_pk_bf16_f32 v60, v248, v249
mqa 124,228,124
tq 254,29,0
v_cvt_pk_bf16_f32 v61, v250, v251
mqa 124,232,148
v_cvt_pk_bf16_f32 v56, v240, v241
v_cvt_pk_bf16_f32 v57, v242, v243
mqa 124,236,156
s_waitcnt lgkmcnt(4)
mqa 124,240,180
mqa 124,244,188
s_waitcnt lgkmcnt(0)
mqa 124,248,212
mqa 124,252,220
mqa 116,224,112
mqa 116,228,120
mqa 116,232,144
s_nop 4
v_fma_f32 v252, v188, v124, v252
v_fma_f32 v253, v189, v125, v253
mqa 116,236,152
v_fma_f32 v254, v190, v126, v254
v_fma_f32 v255, v191, v127, v255
mqa 116,240,176
v_cvt_pk_bf16_f32 v62, v252, v253
v_cvt_pk_bf16_f32 v63, v254, v255
mqa 116,244,184
v_permlane16_swap_b32 v60, v62
v_permlane16_swap_b32 v61, v63
mqa 116,248,208
buffer_store_dwordx4 v[60:63], v3, s[32:35], s70 offen offset:64 sc1
mqa 116,252,216
s_nop 7
v_fma_f32 v244, v180, v116, v244
v_fma_f32 v245, v181, v117, v245
v_fma_f32 v246, v182, v118, v246
v_fma_f32 v247, v183, v119, v247
v_cvt_pk_bf16_f32 v58, v244, v245
v_cvt_pk_bf16_f32 v59, v246, v247
s_nop 0
v_permlane16_swap_b32 v56, v58
v_permlane16_swap_b32 v57, v59
buffer_store_dwordx4 v[56:59], v3, s[32:35], 0 offen offset:64 sc1
s_endpgm
L_slow0_1_%=:
s_mov_b32 s62, 0
L_loop0_1_%=:
s_and_b32 s59, vcc_lo, 0xff00
s_cbranch_scc0 L_norep0_1_%=
v_cndmask_b32 v22, v22, v23, s[60:61]
buffer_store_dword v22, v21, s[28:31], 0 offen offset:0 sc1
L_norep0_1_%=:
buffer_load_dword v23, v20, s[28:31], 0 offen offset:0 sc1
s_waitcnt vmcnt(0)
v_cmp_ne_u32 vcc, v23, v22
s_cbranch_vccz L_back0_1_%=
s_add_u32 s62, s62, 1
s_cmp_lt_u32 s62, 100000
s_cbranch_scc1 L_loop0_1_%=
s_branch L_back0_1_%=
L_slow1_1_%=:
s_mov_b32 s62, 0
L_loop1_1_%=:
s_and_b32 s59, vcc_lo, 0xff00
s_cbranch_scc0 L_norep1_1_%=
v_cndmask_b32 v22, v22, v23, s[60:61]
buffer_store_dword v22, v21, s[28:31], 0 offen offset:0 sc1
buffer_store_dword v22, v21, s[28:31], 0 offen offset:32 sc1
L_norep1_1_%=:
buffer_load_dword v23, v20, s[28:31], 0 offen offset:32 sc1
s_waitcnt vmcnt(0)
v_cmp_ne_u32 vcc, v23, v22
s_cbranch_vccz L_back1_1_%=
s_add_u32 s62, s62, 1
s_cmp_lt_u32 s62, 100000
s_cbranch_scc1 L_loop1_1_%=
s_branch L_back1_1_%=
L_slow2_1_%=:
s_mov_b32 s62, 0
L_loop2_1_%=:
s_and_b32 s59, vcc_lo, 0xff00
s_cbranch_scc0 L_norep2_1_%=
v_cndmask_b32 v22, v22, v23, s[60:61]
buffer_store_dword v22, v21, s[28:31], 0 offen offset:0 sc1
buffer_store_dword v22, v21, s[28:31], 0 offen offset:32 sc1
buffer_store_dword v22, v21, s[28:31], 0 offen offset:64 sc1
L_norep2_1_%=:
buffer_load_dword v23, v20, s[28:31], 0 offen offset:64 sc1
s_waitcnt vmcnt(0)
v_cmp_ne_u32 vcc, v23, v22
s_cbranch_vccz L_back2_1_%=
s_add_u32 s62, s62, 1
s_cmp_lt_u32 s62, 100000
s_cbranch_scc1 L_loop2_1_%=
s_branch L_back2_1_%=
L_slow3_1_%=:
s_mov_b32 s62, 0
L_loop3_1_%=:
s_and_b32 s59, vcc_lo, 0xff00
s_cbranch_scc0 L_norep3_1_%=
v_cndmask_b32 v22, v22, v23, s[60:61]
buffer_store_dword v22, v21, s[28:31], 0 offen offset:0 sc1
buffer_store_dword v22, v21, s[28:31], 0 offen offset:32 sc1
buffer_store_dword v22, v21, s[28:31], 0 offen offset:64 sc1
buffer_store_dword v22, v21, s[28:31], 0 offen offset:96 sc1
L_norep3_1_%=:
buffer_load_dword v23, v20, s[28:31], 0 offen offset:96 sc1
s_waitcnt vmcnt(0)
v_cmp_ne_u32 vcc, v23, v22
s_cbranch_vccz L_back3_1_%=
s_add_u32 s62, s62, 1
s_cmp_lt_u32 s62, 100000
s_cbranch_scc1 L_loop3_1_%=
s_branch L_back3_1_%=
.p2align 12
L_var2_%=:
L_ret_2_%=:
s_mov_b64 exec, -1
s_bfe_u32 s46, s64, 262152
s_lshl_b32 s46, s46, 17
s_xor_b32 s47, s46, 0x20000
s_add_u32 m0, s56, 26624
s_nop 0
buffer_load_dwordx4 v10, s[20:23], s46 offen offset:0 lds
buffer_load_dwordx4 v11, s[20:23], s46 offen offset:1024 lds
buffer_load_dword v25, v19, s[20:23], 0 offen
buffer_load_dword v25, v228, s[24:27], 0 offen
buffer_load_dword v229, v224, s[28:31], 0 offen sc1
s_waitcnt vmcnt(20)
v_lshlrev_b32 v231, 16, v230
v_and_b32 v23, 0xffff0000, v230
ds_write_b32 v227, v231
ds_write_b32 v227, v23 offset:4
s_waitcnt vmcnt(16)
ds_read_b64 a[0:1], v252 offset:0
ds_read_b64 a[2:3], v253 offset:0
ds_read_b64 a[4:5], v252 offset:2048
ds_read_b64 a[6:7], v253 offset:2048
ds_read_b64 a[8:9], v254 offset:0
ds_read_b64 a[10:11], v255 offset:0
ds_read_b64 a[12:13], v254 offset:2048
ds_read_b64 a[14:15], v255 offset:2048
s_waitcnt lgkmcnt(8)
s_waitcnt vmcnt(12)
s_barrier
ds_read_b128 v[64:67], v24 offset:0
ds_read_b128 v[68:71], v24 offset:64
ds_read_b128 v[72:75], v24 offset:0
ds_read_b128 v[76:79], v24 offset:64
ds_read_b128 v[80:83], v24 offset:128
ds_read_b128 v[84:87], v24 offset:192
ds_read_b128 v[88:91], v24 offset:128
ds_read_b128 v[92:95], v24 offset:192
tq 224,4,2048
tq 226,4,4096
tq 228,5,2048
tq 230,5,4096
tq 232,4,6144
tq 234,4,8192
tq 236,5,6144
tq 238,5,8192
tq 240,6,2048
tq 242,6,4096
tq 244,7,2048
tq 246,7,4096
tq 248,6,6144
tq 250,6,8192
s_nop 1
s_waitcnt lgkmcnt(6)
mqa 64,224,0
tq 252,7,6144
s_lshr_b32 s58, s46, 10
s_add_u32 m0, s57, 116736
mqa 72,224,4
tq 254,7,8192
buffer_load_dwordx4 v246, s[24:27], s58 offen offset:0 lds
mqa 76,228,4
tq 224,4,10240
ds_read_b128 v[96:99], v24 offset:256
ds_read_b128 v[100:103], v24 offset:320
ds_read_b128 v[104:107], v24 offset:256
mqa 68,228,0
tq 226,4,12288
buffer_load_dwordx4 v247, s[24:27], s58 offen offset:1024 lds
ds_read_b128 v[108:111], v24 offset:320
mqa 64,232,8
tq 228,5,10240
ds_read_b128 v[112:115], v24 offset:384
ds_read_b128 v[116:119], v24 offset:448
ds_read_b128 v[120:123], v24 offset:384
mqa 72,232,12
tq 230,5,12288
buffer_load_dwordx4 v248, s[24:27], s58 offen offset:2048 lds
ds_read_b128 v[124:127], v24 offset:448
mqa 76,236,12
tq 232,4,14336
mqa 68,236,8
tq 234,4,16384
buffer_load_dwordx4 v249, s[24:27], s58 offen offset:3072 lds
s_waitcnt lgkmcnt(14)
mqa 80,240,0
tq 236,5,14336
s_add_u32 m0, s56, 34816
mqa 88,240,4
tq 238,5,16384
buffer_load_dwordx4 v30, s[20:23], s46 offen offset:0 lds
mqa 92,244,4
tq 240,6,10240
mqa 84,244,0
tq 242,6,12288
buffer_load_dwordx4 v31, s[20:23], s46 offen offset:1024 lds
s_waitcnt vmcnt(13)
ds_read_b64 a[32:33], v252 offset:4096
ds_read_b64 a[34:35], v253 offset:4096
mqa 80,248,8
tq 244,7,10240
s_bfe_u32 s48, s64, 262156
s_lshl_b32 s48, s48, 17
s_xor_b32 s49, s48, 0x20000
s_add_u32 m0, s56, 43008
mqa 88,248,12
tq 246,7,12288
buffer_load_dwordx4 v10, s[20:23], s48 offen offset:0 lds
ds_read_b64 a[36:37], v252 offset:6144
ds_read_b64 a[38:39], v253 offset:6144
mqa 92,252,12
tq 248,6,14336
ds_read_b64 a[40:41], v254 offset:4096
ds_read_b64 a[42:43], v255 offset:4096
mqa 84,252,8
tq 250,6,16384
buffer_load_dwordx4 v11, s[20:23], s48 offen offset:1024 lds
s_waitcnt lgkmcnt(12)
mqa 96,224,0
tq 252,7,14336
tq 254,7,16384
ds_read_b64 a[44:45], v254 offset:6144
ds_read_b64 a[46:47], v255 offset:6144
mqa 104,224,4
s_lshr_b32 s58, s48, 10
s_add_u32 m0, s57, 108544
s_nop 0
buffer_load_dwordx4 v246, s[24:27], s58 offen offset:0 lds
s_waitcnt vmcnt(12)
s_barrier
mqa 108,228,4
tq 224,4,18432
mqa 100,228,0
tq 226,4,20480
buffer_load_dwordx4 v247, s[24:27], s58 offen offset:1024 lds
mqa 96,232,8
tq 228,5,18432
mqa 104,232,12
tq 230,5,20480
buffer_load_dwordx4 v248, s[24:27], s58 offen offset:2048 lds
mqa 108,236,12
tq 232,6,18432
mqa 100,236,8
tq 234,6,20480
buffer_load_dwordx4 v249, s[24:27], s58 offen offset:3072 lds
s_waitcnt lgkmcnt(8)
mqa 112,240,0
tq 236,7,18432
mqa 120,240,4
tq 238,7,20480
mqa 124,244,4
tq 240,4,22528
mqa 116,244,0
s_waitcnt lgkmcnt(9)
s_lshr_b32 s58, s40, 10
s_add_u32 m0, s57, 112640
tq 242,4,24576
buffer_load_dwordx4 v246, s[24:27], s58 offen offset:0 lds
s_waitcnt vmcnt(9)
ds_read_b64 a[64:65], v252 offset:8192
ds_read_b64 a[66:67], v253 offset:8192
mqa 112,248,8
tq 244,5,22528
ds_read_b64 a[68:69], v252 offset:10240
ds_read_b64 a[70:71], v253 offset:10240
mqa 120,248,12
tq 246,5,24576
buffer_load_dwordx4 v247, s[24:27], s58 offen offset:1024 lds
mqa 124,252,12
tq 248,6,22528
ds_read_b64 a[72:73], v254 offset:8192
ds_read_b64 a[74:75], v255 offset:8192
mqa 116,252,8
tq 250,6,24576
buffer_load_dwordx4 v248, s[24:27], s58 offen offset:2048 lds
s_waitcnt lgkmcnt(12)
mqa 64,224,32
tq 252,7,22528
ds_read_b64 a[76:77], v254 offset:10240
ds_read_b64 a[78:79], v255 offset:10240
mqa 72,224,36
tq 254,7,24576
buffer_load_dwordx4 v249, s[24:27], s58 offen offset:3072 lds
mqa 76,228,36
tq 224,4,26624
s_add_u32 m0, s56, 51200
mqa 68,228,32
tq 226,4,28672
buffer_load_dwordx4 v30, s[20:23], s40 offen offset:0 lds
mqa 80,232,32
tq 228,5,26624
mqa 88,232,36
tq 230,5,28672
buffer_load_dwordx4 v31, s[20:23], s40 offen offset:1024 lds
mqa 92,236,36
tq 232,6,26624
s_add_u32 m0, s56, 59392
mqa 84,236,32
tq 234,6,28672
buffer_load_dwordx4 v10, s[20:23], s40 offen offset:0 lds
s_waitcnt lgkmcnt(6)
mqa 64,240,40
tq 236,7,26624
mqa 72,240,44
tq 238,7,28672
buffer_load_dwordx4 v11, s[20:23], s40 offen offset:1024 lds
mqa 76,244,44
tq 240,4,30720
mqa 68,244,40
tq 242,4,32768
mqa 80,248,40
tq 244,5,30720
mqa 88,248,44
tq 246,5,32768
mqa 92,252,44
tq 248,6,30720
mqa 84,252,40
tq 250,6,32768
s_waitcnt lgkmcnt(6)
mqa 64,224,64
tq 252,7,30720
tq 254,7,32768
mqa 72,224,68
v_readlane_b32 s63, v229, 32
L_nonce_2_%=:
s_add_u32 s63, s63, 1
v_cmp_eq_u32 vcc, s63, v229
s_cbranch_vccnz L_nonce_2_%=
v_mov_b32 v230, s63
s_mov_b32 exec_lo, 0
s_mov_b32 exec_hi, 15
buffer_store_dword v230, v224, s[28:31], 0 offen sc1
s_mov_b64 exec, -1
v_and_b32 v22, v22, v230
s_waitcnt vmcnt(13)
s_barrier
mqa 76,228,68
tq 224,4,34816
mqa 68,228,64
tq 226,4,36864
mqa 80,232,64
tq 228,5,34816
mqa 88,232,68
tq 230,5,36864
mqa 92,236,68
tq 232,6,34816
mqa 84,236,64
tq 234,6,36864
s_waitcnt lgkmcnt(6)
mqa 64,240,72
tq 236,7,34816
mqa 72,240,76
tq 238,7,36864
mqa 76,244,76
tq 240,4,38912
s_bfe_u32 s50, s64, 262160
s_lshl_b32 s50, s50, 17
s_xor_b32 s51, s50, 0x20000
mqa 68,244,72
s_lshr_b32 s58, s50, 10
s_add_u32 m0, s57, 116736
tq 242,4,40960
buffer_load_dwordx4 v246, s[24:27], s58 offen offset:0 lds
s_waitcnt vmcnt(10)
ds_read_b64 a[96:97], v252 offset:0
ds_read_b64 a[98:99], v253 offset:0
mqa 80,248,72
tq 244,5,38912
ds_read_b64 a[100:101], v252 offset:2048
ds_read_b64 a[102:103], v253 offset:2048
mqa 88,248,76
tq 246,5,40960
buffer_load_dwordx4 v247, s[24:27], s58 offen offset:1024 lds
mqa 92,252,76
tq 248,6,38912
ds_read_b64 a[104:105], v254 offset:0
ds_read_b64 a[106:107], v255 offset:0
mqa 84,252,72
tq 250,6,40960
buffer_load_dwordx4 v248, s[24:27], s58 offen offset:2048 lds
s_waitcnt lgkmcnt(12)
mqa 96,224,64
tq 252,7,38912
ds_read_b64 a[108:109], v254 offset:2048
ds_read_b64 a[110:111], v255 offset:2048
mqa 104,224,68
tq 254,7,40960
buffer_load_dwordx4 v249, s[24:27], s58 offen offset:3072 lds
mqa 108,228,68
tq 224,4,43008
s_add_u32 m0, s56, 67584
mqa 100,228,64
tq 226,4,45056
buffer_load_dwordx4 v10, s[20:23], s50 offen offset:0 lds
mqa 112,232,64
tq 228,5,43008
mqa 120,232,68
tq 230,5,45056
buffer_load_dwordx4 v11, s[20:23], s50 offen offset:1024 lds
mqa 124,236,68
tq 232,6,43008
s_bfe_u32 s52, s64, 262164
s_lshl_b32 s52, s52, 17
s_xor_b32 s53, s52, 0x20000
mqa 116,236,64
s_add_u32 m0, s56, 75776
tq 234,6,45056
buffer_load_dwordx4 v10, s[20:23], s52 offen offset:0 lds
s_waitcnt lgkmcnt(6)
mqa 96,240,72
tq 236,7,43008
mqa 104,240,76
tq 238,7,45056
buffer_load_dwordx4 v11, s[20:23], s52 offen offset:1024 lds
mqa 108,244,76
tq 240,4,47104
mqa 100,244,72
s_lshr_b32 s58, s52, 10
s_add_u32 m0, s57, 108544
tq 242,4,49152
buffer_load_dwordx4 v246, s[24:27], s58 offen offset:0 lds
s_waitcnt vmcnt(14)
ds_read_b64 v[32:33], v252 offset:4096
ds_read_b64 v[34:35], v253 offset:4096
mqa 112,248,72
tq 244,5,47104
ds_read_b64 v[36:37], v252 offset:6144
ds_read_b64 v[38:39], v253 offset:6144
mqa 120,248,76
tq 246,5,49152
buffer_load_dwordx4 v247, s[24:27], s58 offen offset:1024 lds
mqa 124,252,76
tq 248,6,47104
ds_read_b64 v[40:41], v254 offset:4096
ds_read_b64 v[42:43], v255 offset:4096
mqa 116,252,72
tq 250,6,49152
buffer_load_dwordx4 v248, s[24:27], s58 offen offset:2048 lds
s_waitcnt lgkmcnt(12)
mqa 64,224,96
tq 252,7,47104
tq 254,7,49152
ds_read_b64 v[44:45], v254 offset:6144
ds_read_b64 v[46:47], v255 offset:6144
mqa 72,224,100
buffer_load_dwordx4 v249, s[24:27], s58 offen offset:3072 lds
s_waitcnt vmcnt(13)
s_barrier
mqa 76,228,100
tq 224,4,59392
mqa 68,228,96
tq 226,4,61440
mqa 80,232,96
tq 228,5,59392
mqa 88,232,100
tq 230,5,61440
mqa 92,236,100
tq 232,6,59392
mqa 84,236,96
tq 234,6,61440
s_waitcnt lgkmcnt(8)
mqa 64,240,104
tq 236,7,59392
mqa 72,240,108
tq 238,7,61440
mqa 76,244,108
tq 240,4,63488
s_bfe_u32 s54, s64, 262168
s_lshl_b32 s54, s54, 17
s_xor_b32 s55, s54, 0x20000
mqa 68,244,104
s_waitcnt lgkmcnt(9)
s_lshr_b32 s58, s54, 10
s_add_u32 m0, s57, 112640
tq 242,8,0
buffer_load_dwordx4 v246, s[24:27], s58 offen offset:0 lds
mqa 80,248,104
tq 244,5,63488
mqa 88,248,108
tq 246,9,0
buffer_load_dwordx4 v247, s[24:27], s58 offen offset:1024 lds
mqa 92,252,108
tq 248,6,63488
mqa 84,252,104
tq 250,12,0
buffer_load_dwordx4 v248, s[24:27], s58 offen offset:2048 lds
s_waitcnt lgkmcnt(6)
mqv 64,224,32
tq 252,7,63488
mqv 72,224,36
tq 254,13,0
buffer_load_dwordx4 v249, s[24:27], s58 offen offset:3072 lds
mqv 76,228,36
tq 224,4,51200
s_add_u32 m0, s56, 83968
mqv 68,228,32
tq 226,4,53248
buffer_load_dwordx4 v10, s[20:23], s41 offen offset:0 lds
mqv 80,232,32
tq 228,5,51200
v_lshlrev_b32 v128, 16, v32
mqv 88,232,36
tq 230,5,53248
buffer_load_dwordx4 v11, s[20:23], s41 offen offset:1024 lds
mqv 92,236,36
tq 232,6,51200
s_add_u32 m0, s56, 92160
mqv 84,236,32
tq 234,6,53248
buffer_load_dwordx4 v10, s[20:23], s54 offen offset:0 lds
s_waitcnt lgkmcnt(6)
mqv 64,240,40
tq 236,7,51200
v_and_b32 v129, 0xffff0000, v32
mqv 72,240,44
tq 238,7,53248
buffer_load_dwordx4 v11, s[20:23], s54 offen offset:1024 lds
mqv 76,244,44
tq 240,4,55296
mqv 68,244,40
tq 242,4,57344
s_waitcnt vmcnt(16)
ds_read_b64 a[128:129], v252 offset:8192
ds_read_b64 a[130:131], v253 offset:8192
mqv 80,248,40
tq 244,5,55296
ds_read_b64 a[132:133], v252 offset:10240
mqv 88,248,44
tq 246,5,57344
ds_read_b64 a[134:135], v253 offset:10240
mqv 92,252,44
tq 248,6,55296
ds_read_b64 a[136:137], v254 offset:8192
ds_read_b64 a[138:139], v255 offset:8192
mqv 84,252,40
tq 250,6,57344
s_waitcnt lgkmcnt(12)
mqv 96,224,32
tq 252,7,55296
tq 254,7,57344
ds_read_b64 a[140:141], v254 offset:10240
ds_read_b64 a[142:143], v255 offset:10240
mqv 104,224,36
s_waitcnt vmcnt(12)
s_barrier
mqv 108,228,36
tq 224,8,2048
v_lshlrev_b32 v130, 16, v33
mqv 100,228,32
tq 226,8,4096
v_and_b32 v131, 0xffff0000, v33
mqv 112,232,32
tq 228,9,2048
v_lshlrev_b32 v132, 16, v34
mqv 120,232,36
tq 230,9,4096
v_and_b32 v133, 0xffff0000, v34
mqv 124,236,36
tq 232,12,2048
mqv 116,236,32
s_waitcnt lgkmcnt(5)
s_lshr_b32 s58, s41, 10
s_add_u32 m0, s57, 116736
tq 234,12,4096
buffer_load_dwordx4 v246, s[24:27], s58 offen offset:0 lds
mqv 96,240,40
tq 236,13,2048
v_lshlrev_b32 v134, 16, v35
mqv 104,240,44
tq 238,13,4096
buffer_load_dwordx4 v247, s[24:27], s58 offen offset:1024 lds
mqv 108,244,44
tq 240,8,6144
mqv 100,244,40
tq 242,8,8192
buffer_load_dwordx4 v248, s[24:27], s58 offen offset:2048 lds
s_waitcnt vmcnt(11)
ds_read_b64 a[160:161], v252 offset:0
ds_read_b64 a[162:163], v253 offset:0
mqv 112,248,40
tq 244,9,6144
ds_read_b64 a[164:165], v252 offset:2048
ds_read_b64 a[166:167], v253 offset:2048
mqv 120,248,44
tq 246,9,8192
buffer_load_dwordx4 v249, s[24:27], s58 offen offset:3072 lds
mqv 124,252,44
tq 248,12,6144
ds_read_b64 a[168:169], v254 offset:0
ds_read_b64 a[170:171], v255 offset:0
mqv 116,252,40
tq 250,12,8192
s_waitcnt lgkmcnt(12)
mqa 64,224,128
tq 252,13,6144
s_add_u32 m0, s56, 100352
ds_read_b64 a[172:173], v254 offset:2048
mqa 72,224,132
tq 254,13,8192
buffer_load_dwordx4 v30, s[20:23], s41 offen offset:0 lds
ds_read_b64 a[174:175], v255 offset:2048
mqa 76,228,132
tq 224,8,10240
v_and_b32 v135, 0xffff0000, v35
mqa 68,228,128
tq 226,8,12288
buffer_load_dwordx4 v31, s[20:23], s41 offen offset:1024 lds
mqa 80,232,128
tq 228,9,10240
s_add_u32 m0, s56, 2048
mqa 88,232,132
tq 230,9,12288
buffer_load_dwordx4 v10, s[20:23], s43 offen offset:0 lds
mqa 92,236,132
tq 232,12,10240
mqa 84,236,128
tq 234,12,12288
buffer_load_dwordx4 v11, s[20:23], s43 offen offset:1024 lds
s_waitcnt lgkmcnt(7)
mqa 64,240,136
tq 236,13,10240
v_lshlrev_b32 v136, 16, v36
mqa 72,240,140
tq 238,13,12288
v_and_b32 v137, 0xffff0000, v36
mqa 76,244,140
tq 240,8,14336
mqa 68,244,136
s_waitcnt lgkmcnt(9)
s_lshr_b32 s58, s43, 10
s_add_u32 m0, s57, 108544
tq 242,8,16384
buffer_load_dwordx4 v246, s[24:27], s58 offen offset:0 lds
s_waitcnt vmcnt(13)
ds_read_b64 a[192:193], v252 offset:4096
ds_read_b64 a[194:195], v253 offset:4096
mqa 80,248,136
tq 244,9,14336
ds_read_b64 a[196:197], v252 offset:6144
ds_read_b64 a[198:199], v253 offset:6144
mqa 88,248,140
tq 246,9,16384
buffer_load_dwordx4 v247, s[24:27], s58 offen offset:1024 lds
mqa 92,252,140
tq 248,12,14336
ds_read_b64 a[200:201], v254 offset:4096
ds_read_b64 a[202:203], v255 offset:4096
mqa 84,252,136
tq 250,12,16384
buffer_load_dwordx4 v248, s[24:27], s58 offen offset:2048 lds
s_waitcnt lgkmcnt(12)
s_nop 0
L_hop1_2_%=:
s_cbranch_execz L_hop5_2_%=
mqa 64,224,160
tq 252,13,14336
tq 254,13,16384
ds_read_b64 a[204:205], v254 offset:6144
ds_read_b64 a[206:207], v255 offset:6144
mqa 72,224,164
buffer_load_dwordx4 v249, s[24:27], s58 offen offset:3072 lds
s_waitcnt vmcnt(12)
s_barrier
mqa 76,228,164
tq 224,8,26624
v_lshlrev_b32 v138, 16, v37
mqa 68,228,160
tq 226,8,28672
v_and_b32 v139, 0xffff0000, v37
mqa 80,232,160
tq 228,9,26624
v_lshlrev_b32 v140, 16, v38
mqa 88,232,164
tq 230,9,28672
v_and_b32 v141, 0xffff0000, v38
mqa 92,236,164
tq 232,12,26624
v_lshlrev_b32 v142, 16, v39
mqa 84,236,160
tq 234,12,28672
s_waitcnt lgkmcnt(8)
mqa 64,240,168
tq 236,13,26624
v_and_b32 v143, 0xffff0000, v39
mqa 72,240,172
tq 238,13,28672
v_lshlrev_b32 v144, 16, v40
mqa 76,244,172
tq 240,8,30720
mqa 68,244,168
s_waitcnt lgkmcnt(9)
s_lshr_b32 s58, s45, 10
s_add_u32 m0, s57, 112640
tq 242,8,32768
buffer_load_dwordx4 v246, s[24:27], s58 offen offset:0 lds
s_waitcnt vmcnt(9)
ds_read_b64 v[48:49], v252 offset:8192
ds_read_b64 v[50:51], v253 offset:8192
mqa 80,248,168
tq 244,9,30720
ds_read_b64 v[52:53], v252 offset:10240
ds_read_b64 v[54:55], v253 offset:10240
mqa 88,248,172
tq 246,9,32768
buffer_load_dwordx4 v247, s[24:27], s58 offen offset:1024 lds
mqa 92,252,172
tq 248,12,30720
ds_read_b64 v[56:57], v254 offset:8192
ds_read_b64 v[58:59], v255 offset:8192
mqa 84,252,168
tq 250,12,32768
buffer_load_dwordx4 v248, s[24:27], s58 offen offset:2048 lds
s_waitcnt lgkmcnt(12)
mqa 64,224,192
tq 252,13,30720
ds_read_b64 v[60:61], v254 offset:10240
ds_read_b64 v[62:63], v255 offset:10240
mqa 72,224,196
tq 254,13,32768
buffer_load_dwordx4 v249, s[24:27], s58 offen offset:3072 lds
mqa 76,228,196
tq 224,8,18432
s_add_u32 m0, s56, 10240
mqa 68,228,192
tq 226,8,20480
buffer_load_dwordx4 v10, s[20:23], s47 offen offset:0 lds
mqa 80,232,192
tq 228,9,18432
v_and_b32 v145, 0xffff0000, v40
mqa 88,232,196
tq 230,9,20480
buffer_load_dwordx4 v11, s[20:23], s47 offen offset:1024 lds
mqa 92,236,196
tq 232,12,18432
s_add_u32 m0, s56, 18432
mqa 84,236,192
tq 234,12,20480
buffer_load_dwordx4 v10, s[20:23], s45 offen offset:0 lds
s_waitcnt lgkmcnt(6)
mqa 64,240,200
tq 236,13,18432
v_lshlrev_b32 v146, 16, v41
mqa 72,240,204
tq 238,13,20480
buffer_load_dwordx4 v11, s[20:23], s45 offen offset:1024 lds
mqa 76,244,204
tq 240,8,22528
mqa 68,244,200
s_lshr_b32 s58, s47, 10
s_add_u32 m0, s57, 116736
tq 242,8,24576
buffer_load_dwordx4 v246, s[24:27], s58 offen offset:0 lds
mqa 80,248,200
tq 244,9,22528
v_and_b32 v147, 0xffff0000, v41
mqa 88,248,204
tq 246,9,24576
buffer_load_dwordx4 v247, s[24:27], s58 offen offset:1024 lds
mqa 92,252,204
tq 248,12,22528
mqa 84,252,200
tq 250,12,24576
buffer_load_dwordx4 v248, s[24:27], s58 offen offset:2048 lds
s_waitcnt lgkmcnt(6)
mqv 64,224,48
tq 252,13,22528
tq 254,13,24576
mqv 72,224,52
buffer_load_dwordx4 v249, s[24:27], s58 offen offset:3072 lds
s_waitcnt vmcnt(16)
s_barrier
mqv 76,228,52
tq 224,8,34816
v_lshlrev_b32 v148, 16, v42
mqv 68,228,48
tq 226,8,36864
v_and_b32 v149, 0xffff0000, v42
mqv 80,232,48
tq 228,9,34816
v_lshlrev_b32 v150, 16, v43
mqv 88,232,52
tq 230,9,36864
v_and_b32 v151, 0xffff0000, v43
mqv 92,236,52
tq 232,12,34816
v_lshlrev_b32 v152, 16, v44
mqv 84,236,48
tq 234,12,36864
s_waitcnt lgkmcnt(6)
mqv 64,240,56
tq 236,13,34816
v_and_b32 v153, 0xffff0000, v44
mqv 72,240,60
tq 238,13,36864
v_lshlrev_b32 v154, 16, v45
mqv 76,244,60
tq 240,8,38912
mqv 68,244,56
tq 242,8,40960
s_waitcnt vmcnt(12)
ds_read_b64 a[16:17], v252 offset:0
ds_read_b64 a[18:19], v253 offset:0
mqv 80,248,56
tq 244,9,38912
ds_read_b64 a[20:21], v252 offset:2048
mqv 88,248,60
tq 246,9,40960
ds_read_b64 a[22:23], v253 offset:2048
mqv 92,252,60
tq 248,12,38912
ds_read_b64 a[24:25], v254 offset:0
ds_read_b64 a[26:27], v255 offset:0
mqv 84,252,56
tq 250,12,40960
s_waitcnt lgkmcnt(12)
mqv 96,224,48
tq 252,13,38912
ds_read_b64 a[28:29], v254 offset:2048
ds_read_b64 a[30:31], v255 offset:2048
mqv 104,224,52
tq 254,13,40960
buffer_load_dword v23, v20, s[28:31], 0 offen sc1
s_add_u32 m0, s56, 26624
s_nop 0
buffer_load_dwordx4 v10, s[20:23], s49 offen offset:0 lds
mqv 108,228,52
tq 224,4,2048
v_and_b32 v155, 0xffff0000, v45
mqv 100,228,48
tq 226,4,4096
buffer_load_dwordx4 v11, s[20:23], s49 offen offset:1024 lds
mqv 112,232,48
tq 228,5,2048
s_add_u32 m0, s56, 34816
mqv 120,232,52
tq 230,5,4096
buffer_load_dwordx4 v10, s[20:23], s51 offen offset:0 lds
mqv 124,236,52
tq 232,6,2048
mqv 116,236,48
tq 234,6,4096
buffer_load_dwordx4 v11, s[20:23], s51 offen offset:1024 lds
s_waitcnt lgkmcnt(6)
mqv 96,240,56
tq 236,7,2048
mqv 104,240,60
s_lshr_b32 s58, s49, 10
s_add_u32 m0, s57, 108544
tq 238,7,4096
buffer_load_dwordx4 v246, s[24:27], s58 offen offset:0 lds
mqv 108,244,60
tq 240,4,6144
mqv 100,244,56
tq 242,4,8192
buffer_load_dwordx4 v247, s[24:27], s58 offen offset:1024 lds
s_waitcnt vmcnt(15)
ds_read_b64 a[48:49], v252 offset:4096
ds_read_b64 a[50:51], v253 offset:4096
mqv 112,248,56
tq 244,5,6144
ds_read_b64 a[52:53], v252 offset:6144
ds_read_b64 a[54:55], v253 offset:6144
mqv 120,248,60
tq 246,5,8192
buffer_load_dwordx4 v248, s[24:27], s58 offen offset:2048 lds
mqv 124,252,60
tq 248,6,6144
ds_read_b64 a[56:57], v254 offset:4096
ds_read_b64 a[58:59], v255 offset:4096
mqv 116,252,56
tq 250,6,8192
buffer_load_dwordx4 v249, s[24:27], s58 offen offset:3072 lds
s_waitcnt lgkmcnt(12)
mqa 64,224,16
tq 252,7,6144
tq 254,7,8192
ds_read_b64 a[60:61], v254 offset:6144
ds_read_b64 a[62:63], v255 offset:6144
mqa 72,224,20
s_waitcnt vmcnt(13)
s_barrier
mqa 76,228,20
tq 224,4,18432
v_lshlrev_b32 v156, 16, v46
mqa 68,228,16
tq 226,4,20480
v_and_b32 v157, 0xffff0000, v46
mqa 80,232,16
tq 228,5,18432
v_lshlrev_b32 v158, 16, v47
mqa 88,232,20
tq 230,5,20480
v_and_b32 v159, 0xffff0000, v47
mqa 92,236,20
tq 232,6,18432
mqa 84,236,16
s_waitcnt lgkmcnt(5)
s_lshr_b32 s58, s51, 10
s_add_u32 m0, s57, 112640
tq 234,6,20480
buffer_load_dwordx4 v246, s[24:27], s58 offen offset:0 lds
mqa 64,240,24
tq 236,7,18432
v_lshlrev_b32 v160, 16, v48
mqa 72,240,28
tq 238,7,20480
buffer_load_dwordx4 v247, s[24:27], s58 offen offset:1024 lds
mqa 76,244,28
tq 240,4,22528
mqa 68,244,24
tq 242,4,24576
buffer_load_dwordx4 v248, s[24:27], s58 offen offset:2048 lds
s_waitcnt vmcnt(12)
ds_read_b64 a[80:81], v252 offset:8192
ds_read_b64 a[82:83], v253 offset:8192
mqa 80,248,24
tq 244,5,22528
ds_read_b64 a[84:85], v252 offset:10240
ds_read_b64 a[86:87], v253 offset:10240
mqa 88,248,28
tq 246,5,24576
buffer_load_dwordx4 v249, s[24:27], s58 offen offset:3072 lds
mqa 92,252,28
tq 248,6,22528
ds_read_b64 a[88:89], v254 offset:8192
ds_read_b64 a[90:91], v255 offset:8192
mqa 84,252,24
tq 250,6,24576
s_waitcnt lgkmcnt(12)
mqa 64,224,48
tq 252,7,22528
s_add_u32 m0, s56, 43008
ds_read_b64 a[92:93], v254 offset:10240
mqa 72,224,52
tq 254,7,24576
buffer_load_dwordx4 v10, s[20:23], s55 offen offset:0 lds
ds_read_b64 a[94:95], v255 offset:10240
mqa 76,228,52
tq 224,4,10240
v_and_b32 v161, 0xffff0000, v48
mqa 68,228,48
tq 226,4,12288
buffer_load_dwordx4 v11, s[20:23], s55 offen offset:1024 lds
mqa 80,232,48
tq 228,5,10240
s_add_u32 m0, s56, 59392
mqa 88,232,52
tq 230,5,12288
buffer_load_dwordx4 v10, s[20:23], s53 offen offset:0 lds
mqa 92,236,52
tq 232,6,10240
mqa 84,236,48
tq 234,6,12288
buffer_load_dwordx4 v11, s[20:23], s53 offen offset:1024 lds
s_waitcnt lgkmcnt(7)
mqa 64,240,56
tq 236,7,10240
mqa 72,240,60
s_waitcnt lgkmcnt(7)
s_lshr_b32 s58, s53, 10
s_add_u32 m0, s57, 116736
tq 238,7,12288
buffer_load_dwordx4 v246, s[24:27], s58 offen offset:0 lds
mqa 76,244,60
tq 240,4,14336
mqa 68,244,56
tq 242,4,16384
buffer_load_dwordx4 v247, s[24:27], s58 offen offset:1024 lds
s_waitcnt vmcnt(10)
ds_read_b64 a[112:113], v252 offset:0
ds_read_b64 a[114:115], v253 offset:0
mqa 80,248,56
tq 244,5,14336
ds_read_b64 a[116:117], v252 offset:2048
ds_read_b64 a[118:119], v253 offset:2048
mqa 88,248,60
tq 246,5,16384
buffer_load_dwordx4 v248, s[24:27], s58 offen offset:2048 lds
mqa 92,252,60
tq 248,6,14336
ds_read_b64 a[120:121], v254 offset:0
ds_read_b64 a[122:123], v255 offset:0
mqa 84,252,56
tq 250,6,16384
buffer_load_dwordx4 v249, s[24:27], s58 offen offset:3072 lds
s_waitcnt lgkmcnt(12)
mqa 64,224,80
tq 252,7,14336
tq 254,7,16384
ds_read_b64 a[124:125], v254 offset:2048
ds_read_b64 a[126:127], v255 offset:2048
mqa 72,224,84
s_barrier
mqa 76,228,84
tq 224,4,26624
v_lshlrev_b32 v162, 16, v49
mqa 68,228,80
tq 226,4,28672
v_and_b32 v163, 0xffff0000, v49
mqa 80,232,80
tq 228,5,26624
v_lshlrev_b32 v164, 16, v50
mqa 88,232,84
tq 230,5,28672
v_and_b32 v165, 0xffff0000, v50
mqa 92,236,84
tq 232,6,26624
mqa 84,236,80
s_waitcnt lgkmcnt(5)
s_lshr_b32 s58, s55, 10
s_add_u32 m0, s57, 108544
tq 234,6,28672
buffer_load_dwordx4 v246, s[24:27], s58 offen offset:0 lds
mqa 64,240,88
tq 236,7,26624
v_lshlrev_b32 v166, 16, v51
mqa 72,240,92
tq 238,7,28672
buffer_load_dwordx4 v247, s[24:27], s58 offen offset:1024 lds
mqa 76,244,92
tq 240,4,30720
mqa 68,244,88
tq 242,4,32768
buffer_load_dwordx4 v248, s[24:27], s58 offen offset:2048 lds
s_waitcnt vmcnt(11)
ds_read_b64 a[144:145], v252 offset:4096
ds_read_b64 a[146:147], v253 offset:4096
mqa 80,248,88
tq 244,5,30720
ds_read_b64 a[148:149], v252 offset:6144
ds_read_b64 a[150:151], v253 offset:6144
mqa 88,248,92
tq 246,5,32768
buffer_load_dwordx4 v249, s[24:27], s58 offen offset:3072 lds
mqa 92,252,92
tq 248,6,30720
ds_read_b64 a[152:153], v254 offset:4096
ds_read_b64 a[154:155], v255 offset:4096
mqa 84,252,88
tq 250,6,32768
buffer_load_dword v25, v19, s[72:75], 0 offen
s_waitcnt lgkmcnt(12)
mqa 64,224,112
tq 252,7,30720
s_add_u32 m0, s56, 51200
ds_read_b64 a[156:157], v254 offset:6144
mqa 72,224,116
tq 254,7,32768
buffer_load_dwordx4 v30, s[20:23], s44 offen offset:0 lds
ds_read_b64 a[158:159], v255 offset:6144
mqa 76,228,116
tq 224,4,34816
v_and_b32 v167, 0xffff0000, v51
mqa 68,228,112
tq 226,4,36864
buffer_load_dwordx4 v31, s[20:23], s44 offen offset:1024 lds
mqa 80,232,112
tq 228,5,34816
s_add_u32 m0, s56, 67584
mqa 88,232,116
tq 230,5,36864
buffer_load_dwordx4 v30, s[20:23], s48 offen offset:0 lds
mqa 92,236,116
tq 232,6,34816
mqa 84,236,112
tq 234,6,36864
buffer_load_dwordx4 v31, s[20:23], s48 offen offset:1024 lds
s_waitcnt vmcnt(9)
ds_read_b64 a[176:177], v252 offset:8192
ds_read_b64 a[178:179], v253 offset:8192
s_waitcnt lgkmcnt(9)
mqa 64,240,120
tq 236,7,34816
ds_read_b64 a[180:181], v252 offset:10240
mqa 72,240,124
tq 238,7,36864
ds_read_b64 a[182:183], v253 offset:10240
mqa 76,244,124
tq 240,4,38912
ds_read_b64 a[184:185], v254 offset:8192
mqa 68,244,120
tq 242,4,40960
ds_read_b64 a[186:187], v255 offset:8192
mqa 80,248,120
tq 244,5,38912
ds_read_b64 a[188:189], v254 offset:10240
mqa 88,248,124
tq 246,5,40960
ds_read_b64 a[190:191], v255 offset:10240
mqa 92,252,124
tq 248,6,38912
mqa 84,252,120
tq 250,6,40960
s_waitcnt vmcnt(5)
ds_read_b64 a[208:209], v252 offset:0
ds_read_b64 a[210:211], v253 offset:0
s_waitcnt lgkmcnt(13)
mqa 64,224,144
tq 252,7,38912
tq 254,7,40960
ds_read_b64 a[212:213], v252 offset:2048
ds_read_b64 a[214:215], v253 offset:2048
mqa 72,224,148
s_barrier
mqa 76,228,148
tq 224,4,59392
ds_read_b64 a[216:217], v254 offset:0
mqa 68,228,144
tq 226,4,61440
ds_read_b64 a[218:219], v255 offset:0
mqa 80,232,144
tq 228,4,63488
ds_read_b64 a[220:221], v254 offset:2048
mqa 88,232,148
tq 230,8,0
ds_read_b64 a[222:223], v255 offset:2048
mqa 92,236,148
tq 232,4,43008
s_add_u32 m0, s56, 75776
mqa 84,236,144
tq 234,4,45056
buffer_load_dwordx4 v30, s[20:23], s50 offen offset:0 lds
s_waitcnt lgkmcnt(12)
mqa 64,240,152
tq 236,4,47104
v_lshlrev_b32 v168, 16, v52
mqa 72,240,156
tq 238,4,49152
buffer_load_dwordx4 v31, s[20:23], s50 offen offset:1024 lds
mqa 76,244,156
tq 240,5,59392
s_add_u32 m0, s56, 92160
mqa 68,244,152
tq 242,5,61440
buffer_load_dwordx4 v30, s[20:23], s45 offen offset:0 lds
mqa 80,248,152
tq 244,5,63488
v_and_b32 v169, 0xffff0000, v52
mqa 88,248,156
tq 246,9,0
v_lshlrev_b32 v170, 16, v53
mqa 92,252,156
tq 248,5,43008
mqa 84,252,152
tq 250,5,45056
buffer_load_dwordx4 v31, s[20:23], s45 offen offset:1024 lds
s_waitcnt lgkmcnt(6)
mqa 64,224,176
tq 252,5,47104
s_add_u32 m0, s56, 83968
mqa 72,224,180
tq 254,5,49152
buffer_load_dwordx4 v30, s[20:23], s52 offen offset:0 lds
mqa 72,228,188
tq 224,6,59392
mqa 64,228,184
tq 226,6,61440
buffer_load_dwordx4 v31, s[20:23], s52 offen offset:1024 lds
mqa 64,232,208
tq 228,6,63488
v_and_b32 v171, 0xffff0000, v53
mqa 72,232,212
tq 230,12,0
)ASM"
R"ASM(
v_lshlrev_b32 v172, 16, v54
mqa 72,236,220
tq 232,6,43008
s_add_u32 m0, s56, 100352
mqa 64,236,216
tq 234,6,45056
buffer_load_dwordx4 v30, s[20:23], s54 offen offset:0 lds
s_waitcnt lgkmcnt(6)
mqa 68,240,176
tq 236,6,47104
v_and_b32 v173, 0xffff0000, v54
mqa 76,240,180
tq 238,6,49152
v_lshlrev_b32 v174, 16, v55
mqa 76,244,188
tq 240,7,59392
v_fma_f32 v200, v136, v72, v136
v_fma_f32 v201, v137, v73, v137
mqa 68,244,184
tq 242,7,61440
v_fma_f32 v202, v138, v74, v138
mqa 68,248,208
tq 244,7,63488
v_fma_f32 v203, v139, v75, v139
v_fma_f32 v192, v128, v64, v128
mqa 76,248,212
tq 246,13,0
v_fma_f32 v193, v129, v65, v129
mqa 76,252,220
tq 248,7,43008
v_fma_f32 v194, v130, v66, v130
mqa 68,252,216
tq 250,7,45056
buffer_load_dwordx4 v31, s[20:23], s54 offen offset:1024 lds
L_hop2_2_%=:
s_cbranch_execz L_hop6_2_%=
s_waitcnt lgkmcnt(6)
mqa 80,224,176
tq 252,7,47104
tq 254,7,49152
v_fma_f32 v195, v131, v67, v131
mqa 88,224,180
s_waitcnt vmcnt(8)
s_barrier
mqa 88,228,188
tq 224,4,51200
s_add_u32 m0, s56, 2048
v_cvt_pk_bf16_f32 v36, v200, v201
mqa 80,228,184
tq 226,4,53248
buffer_load_dwordx4 v30, s[20:23], s43 offen offset:0 lds
v_cvt_pk_bf16_f32 v37, v202, v203
mqa 80,232,208
tq 228,5,51200
v_cvt_pk_bf16_f32 v32, v192, v193
v_cvt_pk_bf16_f32 v33, v194, v195
mqa 88,232,212
tq 230,5,53248
v_fma_f32 v204, v140, v76, v140
v_fma_f32 v205, v141, v77, v141
mqa 88,236,220
tq 232,6,51200
v_fma_f32 v206, v142, v78, v142
v_fma_f32 v207, v143, v79, v143
mqa 80,236,216
tq 234,6,53248
buffer_load_dwordx4 v31, s[20:23], s43 offen offset:1024 lds
s_waitcnt lgkmcnt(6)
mqa 84,240,176
tq 236,7,51200
v_fma_f32 v196, v132, v68, v132
v_fma_f32 v197, v133, v69, v133
v_fma_f32 v198, v134, v70, v134
mqa 92,240,180
tq 238,7,53248
v_fma_f32 v199, v135, v71, v135
v_cndmask_b32 v22, v22, v23, s[60:61]
mqa 92,244,188
tq 240,4,55296
s_add_u32 m0, s56, 18432
v_cvt_pk_bf16_f32 v38, v204, v205
mqa 84,244,184
tq 242,4,57344
buffer_load_dwordx4 v30, s[20:23], s53 offen offset:0 lds
v_cvt_pk_bf16_f32 v39, v206, v207
mqa 84,248,208
tq 244,5,55296
mqa 92,248,212
tq 246,5,57344
buffer_store_dwordx4 v[36:39], v14, s[28:31], s80 offen offset:1024 sc1
v_cvt_pk_bf16_f32 v34, v196, v197
v_cvt_pk_bf16_f32 v35, v198, v199
buffer_store_dwordx4 v[32:35], v14, s[28:31], s80 offen offset:0 sc1
mqa 92,252,220
tq 248,6,55296
v_fma_f32 v216, v152, v88, v152
v_fma_f32 v217, v153, v89, v153
mqa 84,252,216
tq 250,6,57344
buffer_load_dwordx4 v31, s[20:23], s53 offen offset:1024 lds
s_waitcnt lgkmcnt(6)
mqa 96,224,32
tq 252,7,55296
v_fma_f32 v218, v154, v90, v154
v_fma_f32 v219, v155, v91, v155
mqa 104,224,36
tq 254,7,57344
v_fma_f32 v208, v144, v80, v144
v_fma_f32 v209, v145, v81, v145
mqa 108,228,36
tq 224,8,2048
s_add_u32 m0, s56, 10240
v_fma_f32 v210, v146, v82, v146
mqa 100,228,32
tq 226,8,4096
buffer_load_dwordx4 v10, s[72:75], s41 offen offset:0 lds
v_fma_f32 v211, v147, v83, v147
mqa 112,232,32
tq 228,9,2048
v_cvt_pk_bf16_f32 v44, v216, v217
v_cvt_pk_bf16_f32 v45, v218, v219
mqa 120,232,36
tq 230,9,4096
v_cvt_pk_bf16_f32 v40, v208, v209
v_cvt_pk_bf16_f32 v41, v210, v211
mqa 124,236,36
tq 232,12,2048
v_fma_f32 v220, v156, v92, v156
v_fma_f32 v221, v157, v93, v157
mqa 116,236,32
tq 234,12,4096
buffer_load_dwordx4 v11, s[72:75], s41 offen offset:1024 lds
s_waitcnt lgkmcnt(6)
mqa 96,240,40
tq 236,13,2048
v_fma_f32 v222, v158, v94, v158
v_fma_f32 v223, v159, v95, v159
mqa 104,240,44
tq 238,13,4096
v_fma_f32 v212, v148, v84, v148
v_fma_f32 v213, v149, v85, v149
mqa 108,244,44
tq 240,8,6144
s_add_u32 m0, s56, 26624
v_fma_f32 v214, v150, v86, v150
mqa 100,244,40
tq 242,8,8192
buffer_load_dwordx4 v30, s[20:23], s47 offen offset:0 lds
v_fma_f32 v215, v151, v87, v151
mqa 112,248,40
tq 244,9,6144
v_cvt_pk_bf16_f32 v46, v220, v221
v_cvt_pk_bf16_f32 v47, v222, v223
mqa 120,248,44
tq 246,9,8192
buffer_store_dwordx4 v[44:47], v14, s[28:31], s80 offen offset:3072 sc1
v_cvt_pk_bf16_f32 v42, v212, v213
mqa 124,252,44
tq 248,12,6144
v_cvt_pk_bf16_f32 v43, v214, v215
mqa 116,252,40
tq 250,12,8192
buffer_load_dwordx4 v31, s[20:23], s47 offen offset:1024 lds
buffer_store_dwordx4 v[40:43], v14, s[28:31], s80 offen offset:2048 sc1
s_waitcnt lgkmcnt(6)
mqa 96,224,96
tq 252,13,6144
tq 254,13,8192
mqa 104,224,100
s_waitcnt vmcnt(14)
s_barrier
mqa 108,228,100
tq 224,8,10240
s_add_u32 m0, s56, 34816
mqa 100,228,96
tq 226,8,12288
buffer_load_dwordx4 v30, s[20:23], s49 offen offset:0 lds
mqa 112,232,96
tq 228,9,10240
v_and_b32 v175, 0xffff0000, v55
mqa 120,232,100
tq 230,9,12288
v_lshlrev_b32 v176, 16, v56
mqa 124,236,100
tq 232,12,10240
mqa 116,236,96
tq 234,12,12288
buffer_load_dwordx4 v31, s[20:23], s49 offen offset:1024 lds
s_waitcnt lgkmcnt(6)
mqa 96,240,104
tq 236,13,10240
s_add_u32 m0, s56, 108544
mqa 104,240,108
tq 238,13,12288
buffer_load_dwordx4 v30, s[72:75], s42 offen offset:0 lds
mqa 108,244,108
tq 240,8,14336
v_and_b32 v177, 0xffff0000, v56
mqa 100,244,104
tq 242,8,16384
buffer_load_dwordx4 v31, s[72:75], s42 offen offset:1024 lds
mqa 112,248,104
tq 244,9,14336
v_lshlrev_b32 v178, 16, v57
mqa 120,248,108
tq 246,9,16384
v_and_b32 v179, 0xffff0000, v57
mqa 124,252,108
tq 248,12,14336
s_add_u32 m0, s56, 116736
mqa 116,252,104
tq 250,12,16384
buffer_load_dwordx4 v30, s[20:23], s51 offen offset:0 lds
s_waitcnt lgkmcnt(6)
mqa 96,224,128
tq 252,13,14336
v_lshlrev_b32 v180, 16, v58
mqa 104,224,132
tq 254,13,16384
v_and_b32 v181, 0xffff0000, v58
mqa 108,228,132
tq 224,8,18432
v_lshlrev_b32 v182, 16, v59
mqa 100,228,128
tq 226,8,20480
buffer_load_dwordx4 v31, s[20:23], s51 offen offset:1024 lds
mqa 112,232,128
tq 228,9,18432
v_and_b32 v183, 0xffff0000, v59
mqa 120,232,132
tq 230,9,20480
v_lshlrev_b32 v184, 16, v60
mqa 124,236,132
tq 232,12,18432
s_add_u32 m0, s56, 124928
mqa 116,236,128
tq 234,12,20480
buffer_load_dwordx4 v30, s[20:23], s55 offen offset:0 lds
s_waitcnt lgkmcnt(6)
mqa 96,240,136
tq 236,13,18432
v_and_b32 v185, 0xffff0000, v60
mqa 104,240,140
tq 238,13,20480
v_lshlrev_b32 v186, 16, v61
mqa 108,244,140
tq 240,8,22528
v_and_b32 v187, 0xffff0000, v61
mqa 100,244,136
tq 242,8,24576
buffer_load_dwordx4 v31, s[20:23], s55 offen offset:1024 lds
mqa 112,248,136
tq 244,9,22528
v_lshlrev_b32 v188, 16, v62
mqa 120,248,140
tq 246,9,24576
v_and_b32 v189, 0xffff0000, v62
mqa 124,252,140
tq 248,12,22528
s_add_u32 m0, s56, 133120
mqa 116,252,136
tq 250,12,24576
buffer_load_dwordx4 v10, s[72:75], s40 offen offset:0 lds
s_waitcnt lgkmcnt(6)
mqa 96,224,160
tq 252,13,22528
tq 254,13,24576
mqa 104,224,164
s_waitcnt vmcnt(19)
s_barrier
mqa 108,228,164
tq 224,8,34816
v_lshlrev_b32 v190, 16, v63
mqa 100,228,160
tq 226,8,36864
buffer_load_dwordx4 v11, s[72:75], s40 offen offset:1024 lds
mqa 112,232,160
tq 228,9,34816
v_and_b32 v191, 0xffff0000, v63
mqa 120,232,164
tq 230,9,36864
ds_read_b128 v[64:67], v24 offset:512
mqa 124,236,164
tq 232,12,34816
s_add_u32 m0, s56, 141312
mqa 116,236,160
tq 234,12,36864
buffer_load_dwordx4 v10, s[72:75], s50 offen offset:0 lds
s_waitcnt lgkmcnt(7)
mqa 96,240,168
tq 236,13,34816
ds_read_b128 v[72:75], v24 offset:512
mqa 104,240,172
tq 238,13,36864
ds_read_b128 v[68:71], v24 offset:576
mqa 108,244,172
tq 240,8,38912
ds_read_b128 v[76:79], v24 offset:576
mqa 100,244,168
tq 242,8,40960
buffer_load_dwordx4 v11, s[72:75], s50 offen offset:1024 lds
mqa 112,248,168
tq 244,9,38912
ds_read_b128 v[80:83], v24 offset:640
mqa 120,248,172
tq 246,9,40960
ds_read_b128 v[88:91], v24 offset:640
mqa 124,252,172
tq 248,12,38912
mqa 116,252,168
tq 250,12,40960
s_waitcnt vmcnt(12)
buffer_store_dword v22, v21, s[28:31], 0 offen offset:0 sc1
s_waitcnt lgkmcnt(10)
mqa 96,224,192
tq 252,13,38912
s_add_u32 m0, s56, 149504
mqa 104,224,196
tq 254,13,40960
buffer_load_dwordx4 v30, s[72:75], s40 offen offset:0 lds
mqa 108,228,196
tq 224,4,2048
ds_read_b128 v[84:87], v24 offset:704
mqa 100,228,192
tq 226,4,4096
buffer_load_dwordx4 v31, s[72:75], s40 offen offset:1024 lds
mqa 112,232,192
tq 228,5,2048
ds_read_b128 v[92:95], v24 offset:704
mqa 120,232,196
tq 230,5,4096
mqa 124,236,196
tq 232,6,2048
s_add_u32 m0, s56, 43008
mqa 116,236,192
tq 234,6,4096
buffer_load_dwordx4 v30, s[72:75], s41 offen offset:0 lds
s_waitcnt lgkmcnt(8)
mqa 96,240,200
tq 236,7,2048
mqa 104,240,204
tq 238,7,4096
mqa 108,244,204
tq 240,4,6144
mqa 100,244,200
tq 242,4,8192
buffer_load_dwordx4 v31, s[72:75], s41 offen offset:1024 lds
mqa 112,248,200
tq 244,5,6144
mqa 120,248,204
tq 246,5,8192
mqa 124,252,204
tq 248,6,6144
s_add_u32 m0, s56, 51200
mqa 116,252,200
tq 250,6,8192
buffer_load_dwordx4 v10, s[72:75], s43 offen offset:0 lds
s_waitcnt lgkmcnt(6)
mqa 96,224,16
tq 252,7,6144
tq 254,7,8192
mqa 104,224,20
s_barrier
mqa 108,228,20
tq 224,8,26624
mqa 100,228,16
tq 226,8,28672
buffer_load_dwordx4 v11, s[72:75], s43 offen offset:1024 lds
mqa 112,232,16
tq 228,9,26624
mqa 120,232,20
tq 230,9,28672
s_lshr_b32 s82, s42, 4
mqa 124,236,20
tq 232,12,26624
s_add_u32 m0, s56, 59392
mqa 116,236,16
tq 234,12,28672
buffer_load_dwordx4 v10, s[72:75], s42 offen offset:0 lds
s_waitcnt lgkmcnt(6)
mqa 96,240,24
tq 236,13,26624
mqa 104,240,28
tq 238,13,28672
s_lshr_b32 s83, s43, 4
mqa 108,244,28
tq 240,8,30720
mqa 100,244,24
tq 242,8,32768
buffer_load_dwordx4 v11, s[72:75], s42 offen offset:1024 lds
mqa 112,248,24
tq 244,9,30720
mqa 120,248,28
tq 246,9,32768
s_lshr_b32 s84, s44, 4
mqa 124,252,28
tq 248,12,30720
s_add_u32 m0, s56, 67584
mqa 116,252,24
tq 250,12,32768
buffer_load_dwordx4 v10, s[72:75], s44 offen offset:0 lds
s_waitcnt lgkmcnt(6)
mqa 96,224,48
tq 252,13,30720
mqa 104,224,52
tq 254,13,32768
s_lshr_b32 s85, s45, 4
mqa 108,228,52
tq 224,4,26624
mqa 100,228,48
tq 226,4,28672
buffer_load_dwordx4 v11, s[72:75], s44 offen offset:1024 lds
mqa 112,232,48
tq 228,5,26624
mqa 120,232,52
tq 230,5,28672
s_lshr_b32 s86, s46, 4
mqa 124,236,52
tq 232,6,26624
s_add_u32 m0, s56, 75776
mqa 116,236,48
tq 234,6,28672
buffer_load_dwordx4 v10, s[72:75], s46 offen offset:0 lds
s_waitcnt lgkmcnt(6)
mqa 96,240,56
tq 236,7,26624
mqa 104,240,60
tq 238,7,28672
s_lshr_b32 s87, s47, 4
mqa 108,244,60
tq 240,4,30720
mqa 100,244,56
tq 242,4,32768
buffer_load_dwordx4 v11, s[72:75], s46 offen offset:1024 lds
mqa 112,248,56
tq 244,5,30720
mqa 120,248,60
tq 246,5,32768
s_lshr_b32 s88, s48, 4
mqa 124,252,60
tq 248,6,30720
s_add_u32 m0, s56, 83968
mqa 116,252,56
tq 250,6,32768
buffer_load_dwordx4 v10, s[72:75], s51 offen offset:0 lds
s_waitcnt lgkmcnt(6)
mqa 96,224,80
tq 252,7,30720
tq 254,7,32768
mqa 104,224,84
s_waitcnt vmcnt(20)
s_barrier
mqa 108,228,84
tq 224,4,34816
mqa 100,228,80
tq 226,4,36864
buffer_load_dwordx4 v11, s[72:75], s51 offen offset:1024 lds
mqa 112,232,80
tq 228,5,34816
mqa 120,232,84
tq 230,5,36864
s_lshr_b32 s89, s49, 4
mqa 124,236,84
tq 232,6,34816
s_add_u32 m0, s56, 100352
mqa 116,236,80
tq 234,6,36864
buffer_load_dwordx4 v10, s[72:75], s48 offen offset:0 lds
s_waitcnt lgkmcnt(6)
mqa 96,240,88
tq 236,7,34816
mqa 104,240,92
tq 238,7,36864
s_lshr_b32 s90, s50, 4
mqa 108,244,92
tq 240,4,38912
mqa 100,244,88
tq 242,4,40960
buffer_load_dwordx4 v11, s[72:75], s48 offen offset:1024 lds
mqa 112,248,88
tq 244,5,38912
mqa 120,248,92
tq 246,5,40960
s_lshr_b32 s91, s51, 4
mqa 124,252,92
tq 248,6,38912
mqa 116,252,88
tq 250,6,40960
s_lshr_b32 s92, s52, 4
s_waitcnt lgkmcnt(6)
mqa 96,224,112
tq 252,7,38912
s_add_u32 m0, s56, 2048
mqa 104,224,116
tq 254,7,40960
buffer_load_dwordx4 v30, s[72:75], s44 offen offset:0 lds
mqa 108,228,116
tq 224,8,51200
mqa 100,228,112
tq 226,8,53248
buffer_load_dwordx4 v31, s[72:75], s44 offen offset:1024 lds
mqa 112,232,112
tq 228,9,51200
mqa 120,232,116
tq 230,9,53248
s_lshr_b32 s93, s53, 4
mqa 124,236,116
tq 232,12,51200
mqa 116,236,112
tq 234,12,53248
s_lshr_b32 s94, s54, 4
s_waitcnt lgkmcnt(6)
mqa 96,240,120
tq 236,13,51200
mqa 104,240,124
tq 238,13,53248
s_lshr_b32 s95, s55, 4
mqa 108,244,124
tq 240,8,55296
mqa 100,244,120
tq 242,8,57344
mqa 112,248,120
tq 244,9,55296
mqa 120,248,124
tq 246,9,57344
mqa 124,252,124
tq 248,12,55296
mqa 116,252,120
tq 250,12,57344
s_waitcnt lgkmcnt(6)
mqa 96,224,144
tq 252,13,55296
tq 254,13,57344
mqa 104,224,148
s_waitcnt vmcnt(23)
s_barrier
mqa 108,228,148
tq 224,4,18432
s_add_u32 m0, s56, 92160
mqa 100,228,144
tq 226,4,20480
buffer_load_dwordx4 v30, s[72:75], s50 offen offset:0 lds
mqa 112,232,144
tq 228,4,22528
mqa 120,232,148
tq 230,4,24576
buffer_load_dwordx4 v31, s[72:75], s50 offen offset:1024 lds
mqa 124,236,148
tq 232,8,59392
mqa 116,236,144
tq 234,8,61440
s_waitcnt lgkmcnt(6)
mqa 96,240,152
tq 236,8,63488
mqa 104,240,156
tq 238,16,0
mqa 108,244,156
tq 240,5,18432
mqa 100,244,152
tq 242,5,20480
mqa 112,248,152
tq 244,5,22528
mqa 120,248,156
tq 246,5,24576
mqa 124,252,156
tq 248,9,59392
mqa 116,252,152
tq 250,9,61440
s_waitcnt lgkmcnt(6)
mqa 96,224,176
tq 252,9,63488
mqa 104,224,180
tq 254,17,0
buffer_load_dword v23, v20, s[28:31], 0 offen offset:0 sc1
s_add_u32 m0, s56, 26624
s_nop 0
s_nop 0
L_hop3_2_%=:
s_cbranch_execz L_hop7_2_%=
buffer_load_dwordx4 v10, s[72:75], s52 offen offset:0 lds
mqa 104,228,188
tq 224,6,18432
mqa 96,228,184
tq 226,6,20480
buffer_load_dwordx4 v11, s[72:75], s52 offen offset:1024 lds
mqa 96,232,208
tq 228,6,22528
mqa 104,232,212
tq 230,6,24576
mqa 104,236,220
tq 232,12,59392
mqa 96,236,216
tq 234,12,61440
s_waitcnt lgkmcnt(6)
mqa 100,240,176
tq 236,12,63488
mqa 108,240,180
tq 238,28,0
mqa 108,244,188
tq 240,7,18432
v_fma_f32 v232, v168, v104, v168
v_fma_f32 v233, v169, v105, v169
mqa 100,244,184
tq 242,7,20480
v_fma_f32 v234, v170, v106, v170
mqa 100,248,208
tq 244,7,22528
v_fma_f32 v235, v171, v107, v171
v_fma_f32 v224, v160, v96, v160
mqa 108,248,212
tq 246,7,24576
v_fma_f32 v225, v161, v97, v161
mqa 108,252,220
tq 248,13,59392
v_fma_f32 v226, v162, v98, v162
v_fma_f32 v227, v163, v99, v163
mqa 100,252,216
tq 250,13,61440
s_waitcnt lgkmcnt(6)
mqa 112,224,176
tq 252,13,63488
tq 254,29,0
s_add_u32 m0, s56, 34816
mqa 120,224,180
buffer_load_dwordx4 v30, s[72:75], s46 offen offset:0 lds
s_waitcnt vmcnt(22)
s_barrier
mqa 120,228,188
tq 224,16,2048
v_cvt_pk_bf16_f32 v52, v232, v233
v_cvt_pk_bf16_f32 v53, v234, v235
mqa 112,228,184
tq 226,16,4096
v_cvt_pk_bf16_f32 v48, v224, v225
mqa 112,232,208
tq 228,17,2048
v_cvt_pk_bf16_f32 v49, v226, v227
v_fma_f32 v236, v172, v108, v172
mqa 120,232,212
tq 230,17,4096
buffer_load_dwordx4 v31, s[72:75], s46 offen offset:1024 lds
mqa 120,236,220
tq 232,16,6144
v_fma_f32 v237, v173, v109, v173
v_fma_f32 v238, v174, v110, v174
mqa 112,236,216
tq 234,16,8192
s_waitcnt lgkmcnt(6)
mqa 116,240,176
tq 236,17,6144
v_fma_f32 v239, v175, v111, v175
v_fma_f32 v228, v164, v100, v164
mqa 124,240,180
tq 238,17,8192
v_fma_f32 v229, v165, v101, v165
mqa 124,244,188
tq 240,28,2048
v_fma_f32 v230, v166, v102, v166
v_fma_f32 v231, v167, v103, v167
mqa 116,244,184
tq 242,28,4096
v_cvt_pk_bf16_f32 v54, v236, v237
v_cvt_pk_bf16_f32 v55, v238, v239
mqa 116,248,208
tq 244,29,2048
mqa 124,248,212
tq 246,29,4096
buffer_store_dwordx4 v[52:55], v14, s[28:31], s81 offen offset:1024 sc1
v_cvt_pk_bf16_f32 v50, v228, v229
v_cvt_pk_bf16_f32 v51, v230, v231
buffer_store_dwordx4 v[48:51], v14, s[28:31], s81 offen offset:0 sc1
mqa 124,252,220
tq 248,28,6144
ds_read_b128 v[96:99], v24 offset:768
v_fma_f32 v248, v184, v120, v184
v_fma_f32 v249, v185, v121, v185
mqa 116,252,216
tq 250,28,8192
v_fma_f32 v250, v186, v122, v186
s_waitcnt lgkmcnt(7)
mqv 64,224,32
tq 252,29,6144
v_fma_f32 v251, v187, v123, v187
mqv 72,224,36
tq 254,29,8192
s_waitcnt vmcnt(6)
v_cmp_ne_u32 vcc, v23, v22
s_cbranch_vccnz L_slow0_2_%=
L_back0_2_%=:
buffer_load_dwordx4 a[0:3], v14, s[28:31], s82 offen offset:0 sc1
mqv 76,228,36
tq 224,16,18432
ds_read_b128 v[104:107], v24 offset:768
v_fma_f32 v240, v176, v112, v176
mqv 68,228,32
tq 226,16,20480
v_fma_f32 v241, v177, v113, v177
v_fma_f32 v242, v178, v114, v178
mqv 64,232,40
tq 228,17,18432
v_fma_f32 v243, v179, v115, v179
v_cvt_pk_bf16_f32 v60, v248, v249
v_cvt_pk_bf16_f32 v61, v250, v251
mqv 72,232,44
tq 230,17,20480
buffer_load_dwordx4 a[4:7], v14, s[28:31], s82 offen offset:1024 sc1
ds_read_b128 v[100:103], v24 offset:832
mqv 76,236,44
tq 232,16,22528
v_cvt_pk_bf16_f32 v56, v240, v241
v_cvt_pk_bf16_f32 v57, v242, v243
ds_read_b128 v[108:111], v24 offset:832
mqv 68,236,40
tq 234,16,24576
v_fma_f32 v252, v188, v124, v188
s_waitcnt lgkmcnt(9)
mqv 80,240,32
tq 236,17,22528
v_fma_f32 v253, v189, v125, v189
v_fma_f32 v254, v190, v126, v190
v_fma_f32 v255, v191, v127, v191
mqv 88,240,36
tq 238,17,24576
buffer_load_dwordx4 a[8:11], v14, s[28:31], s82 offen offset:2048 sc1
ds_read_b128 v[112:115], v24 offset:896
mqv 92,244,36
tq 240,28,18432
v_fma_f32 v244, v180, v116, v180
v_fma_f32 v245, v181, v117, v181
mqv 84,244,32
tq 242,28,20480
v_fma_f32 v246, v182, v118, v182
v_fma_f32 v247, v183, v119, v183
mqv 80,248,40
tq 244,29,18432
v_cvt_pk_bf16_f32 v62, v252, v253
v_cvt_pk_bf16_f32 v63, v254, v255
mqv 88,248,44
tq 246,29,20480
buffer_load_dwordx4 a[12:15], v14, s[28:31], s82 offen offset:3072 sc1
buffer_store_dwordx4 v[60:63], v14, s[28:31], s81 offen offset:3072 sc1
ds_read_b128 v[120:123], v24 offset:896
mqv 92,252,44
tq 248,28,22528
v_cvt_pk_bf16_f32 v58, v244, v245
v_cvt_pk_bf16_f32 v59, v246, v247
mqv 84,252,40
tq 250,28,24576
buffer_store_dwordx4 v[56:59], v14, s[28:31], s81 offen offset:2048 sc1
ds_read_b128 v[116:119], v24 offset:960
s_waitcnt lgkmcnt(9)
mqv 96,224,32
tq 252,29,22528
tq 254,29,24576
ds_read_b128 v[124:127], v24 offset:960
mqv 104,224,36
buffer_load_dwordx4 a[32:35], v14, s[28:31], s84 offen offset:0 sc1
s_barrier
mqv 108,228,36
tq 224,4,10240
mqv 100,228,32
tq 226,4,12288
buffer_load_dwordx4 a[36:39], v14, s[28:31], s84 offen offset:1024 sc1
mqv 96,232,40
tq 228,5,10240
mqv 104,232,44
tq 230,5,12288
buffer_load_dwordx4 a[40:43], v14, s[28:31], s84 offen offset:2048 sc1
mqv 108,236,44
tq 232,6,10240
mqv 100,236,40
tq 234,6,12288
s_waitcnt lgkmcnt(6)
mqv 112,240,32
tq 236,7,10240
mqv 120,240,36
tq 238,7,12288
buffer_load_dwordx4 a[44:47], v14, s[28:31], s84 offen offset:3072 sc1
mqv 124,244,36
tq 240,4,14336
mqv 116,244,32
tq 242,4,16384
mqv 112,248,40
tq 244,5,14336
s_add_u32 m0, s56, 116736
mqv 120,248,44
tq 246,5,16384
buffer_load_dwordx4 v30, s[72:75], s45 offen offset:0 lds
mqv 124,252,44
tq 248,6,14336
mqv 116,252,40
tq 250,6,16384
s_waitcnt lgkmcnt(6)
mqv 64,224,48
tq 252,7,14336
mqv 72,224,52
tq 254,7,16384
buffer_load_dwordx4 v31, s[72:75], s45 offen offset:1024 lds
mqv 76,228,52
tq 224,4,43008
mqv 68,228,48
tq 226,4,45056
mqv 80,232,48
tq 228,5,43008
mqv 88,232,52
tq 230,5,45056
buffer_load_dwordx4 a[64:67], v14, s[28:31], s86 offen offset:0 sc1
mqv 92,236,52
tq 232,6,43008
mqv 84,236,48
tq 234,6,45056
s_waitcnt lgkmcnt(6)
mqv 64,240,56
tq 236,7,43008
mqv 72,240,60
tq 238,7,45056
buffer_load_dwordx4 a[68:71], v14, s[28:31], s86 offen offset:1024 sc1
mqv 76,244,60
tq 240,4,47104
mqv 68,244,56
tq 242,4,49152
mqv 80,248,56
tq 244,5,47104
mqv 88,248,60
tq 246,5,49152
buffer_load_dwordx4 a[72:75], v14, s[28:31], s86 offen offset:2048 sc1
mqv 92,252,60
tq 248,6,47104
mqv 84,252,56
tq 250,6,49152
s_waitcnt lgkmcnt(6)
mqv 96,224,48
tq 252,7,47104
tq 254,7,49152
mqv 104,224,52
s_barrier
mqv 108,228,52
tq 224,4,59392
mqv 100,228,48
tq 226,4,61440
buffer_load_dwordx4 a[76:79], v14, s[28:31], s86 offen offset:3072 sc1
mqv 112,232,48
tq 228,5,59392
mqv 120,232,52
tq 230,5,61440
buffer_load_dwordx4 a[96:99], v14, s[28:31], s88 offen offset:0 sc1
mqv 124,236,52
tq 232,6,59392
mqv 116,236,48
tq 234,6,61440
s_waitcnt lgkmcnt(6)
mqv 96,240,56
tq 236,7,59392
mqv 104,240,60
tq 238,7,61440
buffer_load_dwordx4 a[100:103], v14, s[28:31], s88 offen offset:1024 sc1
mqv 108,244,60
tq 240,4,63488
mqv 100,244,56
tq 242,8,0
mqv 112,248,56
tq 244,5,63488
mqv 120,248,60
tq 246,9,0
buffer_load_dwordx4 a[104:107], v14, s[28:31], s88 offen offset:2048 sc1
mqv 124,252,60
tq 248,6,63488
mqv 116,252,56
tq 250,12,0
s_waitcnt vmcnt(13)
buffer_store_dword v22, v21, s[28:31], 0 offen offset:32 sc1
s_waitcnt lgkmcnt(6)
mqa 64,224,0
tq 252,7,63488
mqa 72,224,4
tq 254,13,0
buffer_load_dwordx4 a[108:111], v14, s[28:31], s88 offen offset:3072 sc1
mqa 76,228,4
tq 224,8,2048
mqa 68,228,0
tq 226,8,4096
mqa 80,232,0
tq 228,9,2048
s_add_u32 m0, s56, 18432
mqa 88,232,4
tq 230,9,4096
buffer_load_dwordx4 v30, s[72:75], s53 offen offset:0 lds
mqa 92,236,4
tq 232,12,2048
mqa 84,236,0
tq 234,12,4096
s_waitcnt lgkmcnt(6)
mqa 64,240,8
tq 236,13,2048
mqa 72,240,12
tq 238,13,4096
buffer_load_dwordx4 v31, s[72:75], s53 offen offset:1024 lds
mqa 76,244,12
tq 240,8,6144
mqa 68,244,8
tq 242,8,8192
mqa 80,248,8
tq 244,9,6144
mqa 88,248,12
tq 246,9,8192
buffer_load_dwordx4 a[128:131], v14, s[28:31], s90 offen offset:0 sc1
mqa 92,252,12
tq 248,12,6144
mqa 84,252,8
tq 250,12,8192
s_waitcnt lgkmcnt(6)
s_waitcnt vmcnt(14)
mqa 64,224,32
tq 252,13,6144
tq 254,13,8192
mqa 72,224,36
s_barrier
mqa 76,228,36
tq 224,8,43008
mqa 68,228,32
tq 226,8,45056
buffer_load_dwordx4 a[132:135], v14, s[28:31], s90 offen offset:1024 sc1
mqa 80,232,32
tq 228,9,43008
mqa 88,232,36
tq 230,9,45056
buffer_load_dwordx4 a[136:139], v14, s[28:31], s90 offen offset:2048 sc1
mqa 92,236,36
tq 232,12,43008
mqa 84,236,32
tq 234,12,45056
s_waitcnt lgkmcnt(6)
mqa 64,240,40
tq 236,13,43008
mqa 72,240,44
tq 238,13,45056
buffer_load_dwordx4 a[140:143], v14, s[28:31], s90 offen offset:3072 sc1
mqa 76,244,44
tq 240,8,47104
mqa 68,244,40
tq 242,8,49152
mqa 80,248,40
tq 244,9,47104
s_add_u32 m0, s56, 124928
mqa 88,248,44
tq 246,9,49152
buffer_load_dwordx4 v10, s[72:75], s54 offen offset:0 lds
mqa 92,252,44
tq 248,12,47104
mqa 84,252,40
tq 250,12,49152
s_waitcnt lgkmcnt(6)
mqa 96,224,0
tq 252,13,47104
mqa 104,224,4
tq 254,13,49152
buffer_load_dwordx4 v11, s[72:75], s54 offen offset:1024 lds
mqa 108,228,4
tq 224,8,10240
mqa 100,228,0
tq 226,8,12288
mqa 112,232,0
tq 228,9,10240
mqa 120,232,4
tq 230,9,12288
buffer_load_dwordx4 a[160:163], v14, s[28:31], s92 offen offset:0 sc1
mqa 124,236,4
tq 232,12,10240
mqa 116,236,0
tq 234,12,12288
s_waitcnt lgkmcnt(6)
mqa 96,240,8
tq 236,13,10240
mqa 104,240,12
tq 238,13,12288
buffer_load_dwordx4 a[164:167], v14, s[28:31], s92 offen offset:1024 sc1
mqa 108,244,12
tq 240,8,14336
mqa 100,244,8
tq 242,8,16384
mqa 112,248,8
tq 244,9,14336
mqa 120,248,12
tq 246,9,16384
buffer_load_dwordx4 a[168:171], v14, s[28:31], s92 offen offset:2048 sc1
mqa 124,252,12
tq 248,12,14336
mqa 116,252,8
tq 250,12,16384
s_waitcnt lgkmcnt(6)
s_waitcnt vmcnt(16)
mqa 64,224,64
tq 252,13,14336
tq 254,13,16384
mqa 72,224,68
s_barrier
mqa 76,228,68
tq 224,8,34816
mqa 68,228,64
tq 226,8,36864
buffer_load_dwordx4 a[172:175], v14, s[28:31], s92 offen offset:3072 sc1
mqa 80,232,64
tq 228,9,34816
s_add_u32 m0, s56, 133120
mqa 88,232,68
tq 230,9,36864
buffer_load_dwordx4 v10, s[72:75], s45 offen offset:0 lds
mqa 92,236,68
tq 232,12,34816
mqa 84,236,64
tq 234,12,36864
s_waitcnt lgkmcnt(6)
mqa 64,240,72
tq 236,13,34816
mqa 72,240,76
tq 238,13,36864
buffer_load_dwordx4 v11, s[72:75], s45 offen offset:1024 lds
mqa 76,244,76
tq 240,8,38912
mqa 68,244,72
tq 242,8,40960
mqa 80,248,72
tq 244,9,38912
mqa 88,248,76
tq 246,9,40960
buffer_load_dwordx4 a[192:195], v14, s[28:31], s94 offen offset:0 sc1
mqa 92,252,76
tq 248,12,38912
mqa 84,252,72
tq 250,12,40960
s_waitcnt lgkmcnt(6)
s_waitcnt vmcnt(15)
mqa 64,224,96
tq 252,13,38912
mqa 72,224,100
tq 254,13,40960
buffer_load_dwordx4 a[196:199], v14, s[28:31], s94 offen offset:1024 sc1
mqa 76,228,100
tq 224,4,2048
mqa 68,228,96
tq 226,4,4096
mqa 80,232,96
tq 228,5,2048
mqa 88,232,100
tq 230,5,4096
buffer_load_dwordx4 a[200:203], v14, s[28:31], s94 offen offset:2048 sc1
mqa 92,236,100
tq 232,6,2048
mqa 84,236,96
tq 234,6,4096
s_waitcnt lgkmcnt(6)
mqa 64,240,104
tq 236,7,2048
mqa 72,240,108
tq 238,7,4096
buffer_load_dwordx4 a[204:207], v14, s[28:31], s94 offen offset:3072 sc1
mqa 76,244,108
tq 240,4,6144
mqa 68,244,104
tq 242,4,8192
mqa 80,248,104
tq 244,5,6144
s_add_u32 m0, s56, 10240
mqa 88,248,108
tq 246,5,8192
buffer_load_dwordx4 v10, s[76:79], s41 offen offset:0 lds
mqa 92,252,108
tq 248,6,6144
mqa 84,252,104
tq 250,6,8192
s_waitcnt lgkmcnt(6)
mqa 96,224,32
tq 252,7,6144
tq 254,7,8192
mqa 104,224,36
buffer_load_dword v23, v20, s[28:31], 0 offen offset:32 sc1
s_barrier
mqa 108,228,36
tq 224,16,10240
mqa 100,228,32
tq 226,16,12288
buffer_load_dwordx4 v11, s[76:79], s41 offen offset:1024 lds
mqa 112,232,32
tq 228,17,10240
s_add_u32 m0, s56, 149504
mqa 120,232,36
tq 230,17,12288
buffer_load_dwordx4 v10, s[72:75], s47 offen offset:0 lds
mqa 124,236,36
tq 232,28,10240
mqa 116,236,32
tq 234,28,12288
s_waitcnt lgkmcnt(6)
mqa 96,240,40
tq 236,29,10240
mqa 104,240,44
tq 238,29,12288
buffer_load_dwordx4 v11, s[72:75], s47 offen offset:1024 lds
mqa 108,244,44
tq 240,16,14336
mqa 100,244,40
tq 242,16,16384
mqa 112,248,40
tq 244,17,14336
s_add_u32 m0, s56, 43008
mqa 120,248,44
tq 246,17,16384
buffer_load_dwordx4 v10, s[72:75], s49 offen offset:0 lds
mqa 124,252,44
tq 248,28,14336
L_hop4_2_%=:
s_cbranch_execz L_hop8_2_%=
mqa 116,252,40
tq 250,28,16384
s_waitcnt lgkmcnt(6)
s_waitcnt vmcnt(18)
mqa 64,224,128
tq 252,29,14336
mqa 72,224,132
tq 254,29,16384
buffer_load_dwordx4 v11, s[72:75], s49 offen offset:1024 lds
mqa 76,228,132
tq 224,4,26624
mqa 68,228,128
tq 226,4,28672
mqa 80,232,128
tq 228,5,26624
s_add_u32 m0, s56, 59392
mqa 88,232,132
tq 230,5,28672
buffer_load_dwordx4 v10, s[72:75], s53 offen offset:0 lds
mqa 92,236,132
tq 232,6,26624
mqa 84,236,128
tq 234,6,28672
s_waitcnt lgkmcnt(6)
mqa 64,240,136
tq 236,7,26624
mqa 72,240,140
tq 238,7,28672
buffer_load_dwordx4 v11, s[72:75], s53 offen offset:1024 lds
mqa 76,244,140
tq 240,4,30720
mqa 68,244,136
tq 242,4,32768
mqa 80,248,136
tq 244,5,30720
s_add_u32 m0, s56, 67584
mqa 88,248,140
tq 246,5,32768
buffer_load_dwordx4 v10, s[72:75], s55 offen offset:0 lds
mqa 92,252,140
tq 248,6,30720
mqa 84,252,136
tq 250,6,32768
s_waitcnt lgkmcnt(6)
s_waitcnt vmcnt(16)
mqa 64,224,160
tq 252,7,30720
tq 254,7,32768
mqa 72,224,164
s_waitcnt vmcnt(8)
v_cmp_ne_u32 vcc, v23, v22
s_cbranch_vccnz L_slow1_2_%=
L_back1_2_%=:
s_barrier
mqa 76,228,164
tq 224,4,34816
mqa 68,228,160
tq 226,4,36864
buffer_load_dwordx4 v11, s[72:75], s55 offen offset:1024 lds
mqa 80,232,160
tq 228,5,34816
mqa 88,232,164
tq 230,5,36864
buffer_load_dwordx4 a[16:19], v14, s[28:31], s83 offen offset:0 sc1
mqa 92,236,164
tq 232,6,34816
mqa 84,236,160
tq 234,6,36864
buffer_load_dwordx4 a[20:23], v14, s[28:31], s83 offen offset:1024 sc1
s_waitcnt lgkmcnt(6)
mqa 64,240,168
tq 236,7,34816
mqa 72,240,172
tq 238,7,36864
buffer_load_dwordx4 a[24:27], v14, s[28:31], s83 offen offset:2048 sc1
mqa 76,244,172
tq 240,4,38912
mqa 68,244,168
tq 242,4,40960
buffer_load_dwordx4 a[28:31], v14, s[28:31], s83 offen offset:3072 sc1
mqa 80,248,168
tq 244,5,38912
mqa 88,248,172
tq 246,5,40960
mqa 92,252,172
tq 248,6,38912
mqa 84,252,168
tq 250,6,40960
buffer_load_dwordx4 a[48:51], v14, s[28:31], s85 offen offset:0 sc1
s_waitcnt lgkmcnt(6)
mqa 96,224,64
tq 252,7,38912
mqa 104,224,68
tq 254,7,40960
buffer_load_dwordx4 a[52:55], v14, s[28:31], s85 offen offset:1024 sc1
mqa 108,228,68
tq 224,8,59392
mqa 100,228,64
tq 226,8,61440
buffer_load_dwordx4 a[56:59], v14, s[28:31], s85 offen offset:2048 sc1
mqa 112,232,64
tq 228,9,59392
mqa 120,232,68
tq 230,9,61440
buffer_load_dwordx4 a[60:63], v14, s[28:31], s85 offen offset:3072 sc1
mqa 124,236,68
tq 232,12,59392
mqa 116,236,64
tq 234,12,61440
s_waitcnt lgkmcnt(6)
mqa 96,240,72
tq 236,13,59392
mqa 104,240,76
tq 238,13,61440
mqa 108,244,76
tq 240,8,63488
mqa 100,244,72
tq 242,16,0
mqa 112,248,72
tq 244,9,63488
mqa 120,248,76
tq 246,17,0
mqa 124,252,76
tq 248,12,63488
mqa 116,252,72
tq 250,28,0
buffer_load_dwordx4 a[80:83], v14, s[28:31], s87 offen offset:0 sc1
s_waitcnt lgkmcnt(6)
mqa 64,224,192
tq 252,13,63488
tq 254,29,0
mqa 72,224,196
buffer_load_dwordx4 a[84:87], v14, s[28:31], s87 offen offset:1024 sc1
s_barrier
mqa 76,228,196
tq 224,4,51200
mqa 68,228,192
tq 226,4,53248
buffer_load_dwordx4 a[88:91], v14, s[28:31], s87 offen offset:2048 sc1
mqa 80,232,192
tq 228,5,51200
mqa 88,232,196
tq 230,5,53248
buffer_load_dwordx4 a[92:95], v14, s[28:31], s87 offen offset:3072 sc1
mqa 92,236,196
tq 232,6,51200
mqa 84,236,192
tq 234,6,53248
s_waitcnt lgkmcnt(6)
mqa 64,240,200
tq 236,7,51200
mqa 72,240,204
tq 238,7,53248
mqa 76,244,204
tq 240,4,55296
mqa 68,244,200
tq 242,4,57344
mqa 80,248,200
tq 244,5,55296
mqa 88,248,204
tq 246,5,57344
mqa 92,252,204
tq 248,6,55296
mqa 84,252,200
tq 250,6,57344
buffer_load_dwordx4 a[112:115], v14, s[28:31], s89 offen offset:0 sc1
s_waitcnt lgkmcnt(6)
s_waitcnt vmcnt(9)
mqa 64,224,16
tq 252,7,55296
mqa 72,224,20
tq 254,7,57344
buffer_load_dwordx4 a[116:119], v14, s[28:31], s89 offen offset:1024 sc1
mqa 76,228,20
tq 224,16,2048
mqa 68,228,16
tq 226,16,4096
buffer_load_dwordx4 a[120:123], v14, s[28:31], s89 offen offset:2048 sc1
mqa 80,232,16
tq 228,17,2048
mqa 88,232,20
tq 230,17,4096
buffer_load_dwordx4 a[124:127], v14, s[28:31], s89 offen offset:3072 sc1
mqa 92,236,20
tq 232,28,2048
mqa 84,236,16
tq 234,28,4096
s_waitcnt lgkmcnt(6)
mqa 64,240,24
tq 236,29,2048
mqa 72,240,28
tq 238,29,4096
mqa 76,244,28
tq 240,16,6144
mqa 68,244,24
tq 242,16,8192
mqa 80,248,24
tq 244,17,6144
mqa 88,248,28
tq 246,17,8192
mqa 92,252,28
tq 248,28,6144
mqa 84,252,24
tq 250,28,8192
buffer_load_dwordx4 a[144:147], v14, s[28:31], s91 offen offset:0 sc1
s_waitcnt lgkmcnt(6)
s_waitcnt vmcnt(9)
mqa 64,224,48
tq 252,29,6144
tq 254,29,8192
mqa 72,224,52
buffer_load_dwordx4 a[148:151], v14, s[28:31], s91 offen offset:1024 sc1
s_barrier
mqa 76,228,52
tq 224,16,18432
mqa 68,228,48
tq 226,16,20480
buffer_load_dwordx4 a[152:155], v14, s[28:31], s91 offen offset:2048 sc1
mqa 80,232,48
tq 228,17,18432
mqa 88,232,52
tq 230,17,20480
buffer_load_dwordx4 a[156:159], v14, s[28:31], s91 offen offset:3072 sc1
mqa 92,236,52
tq 232,28,18432
mqa 84,236,48
tq 234,28,20480
s_waitcnt lgkmcnt(6)
mqa 64,240,56
tq 236,29,18432
mqa 72,240,60
tq 238,29,20480
buffer_load_dword v25, v19, s[76:79], 0 offen
mqa 76,244,60
tq 240,16,22528
mqa 68,244,56
tq 242,16,24576
mqa 80,248,56
tq 244,17,22528
mqa 88,248,60
tq 246,17,24576
mqa 92,252,60
tq 248,28,22528
mqa 84,252,56
tq 250,28,24576
buffer_load_dwordx4 a[176:179], v14, s[28:31], s93 offen offset:0 sc1
s_waitcnt lgkmcnt(6)
s_waitcnt vmcnt(10)
mqa 64,224,80
tq 252,29,22528
mqa 72,224,84
tq 254,29,24576
buffer_load_dwordx4 a[180:183], v14, s[28:31], s93 offen offset:1024 sc1
mqa 76,228,84
tq 224,4,43008
mqa 68,228,80
tq 226,4,45056
buffer_load_dwordx4 a[184:187], v14, s[28:31], s93 offen offset:2048 sc1
mqa 80,232,80
tq 228,5,43008
mqa 88,232,84
tq 230,5,45056
buffer_load_dwordx4 a[188:191], v14, s[28:31], s93 offen offset:3072 sc1
mqa 92,236,84
tq 232,6,43008
s_add_u32 m0, s56, 108544
mqa 84,236,80
tq 234,6,45056
buffer_load_dwordx4 v30, s[72:75], s48 offen offset:0 lds
s_waitcnt lgkmcnt(6)
mqa 64,240,88
tq 236,7,43008
mqa 72,240,92
tq 238,7,45056
buffer_load_dwordx4 v31, s[72:75], s48 offen offset:1024 lds
mqa 76,244,92
tq 240,4,47104
mqa 68,244,88
tq 242,4,49152
buffer_load_dwordx4 a[208:211], v14, s[28:31], s95 offen offset:0 sc1
mqa 80,248,88
tq 244,5,47104
mqa 88,248,92
tq 246,5,49152
buffer_load_dwordx4 a[212:215], v14, s[28:31], s95 offen offset:1024 sc1
mqa 92,252,92
tq 248,6,47104
mqa 84,252,88
tq 250,6,49152
buffer_load_dwordx4 a[216:219], v14, s[28:31], s95 offen offset:2048 sc1
s_waitcnt lgkmcnt(6)
s_waitcnt vmcnt(14)
mqa 64,224,112
tq 252,7,47104
tq 254,7,49152
mqa 72,224,116
buffer_load_dwordx4 a[220:223], v14, s[28:31], s95 offen offset:3072 sc1
s_barrier
mqa 76,228,116
tq 224,8,18432
mqa 68,228,112
tq 226,8,20480
mqa 80,232,112
tq 228,9,18432
mqa 88,232,116
tq 230,9,20480
mqa 92,236,116
tq 232,12,18432
mqa 84,236,112
tq 234,12,20480
s_waitcnt lgkmcnt(6)
mqa 64,240,120
tq 236,13,18432
mqa 72,240,124
tq 238,13,20480
mqa 76,244,124
tq 240,8,22528
mqa 68,244,120
tq 242,8,24576
mqa 80,248,120
tq 244,9,22528
mqa 88,248,124
tq 246,9,24576
mqa 92,252,124
tq 248,12,22528
mqa 84,252,120
tq 250,12,24576
s_waitcnt lgkmcnt(6)
s_waitcnt vmcnt(11)
mqa 64,224,144
tq 252,13,22528
s_add_u32 m0, s56, 75776
mqa 72,224,148
tq 254,13,24576
buffer_load_dwordx4 v30, s[72:75], s52 offen offset:0 lds
mqa 76,228,148
tq 224,4,59392
mqa 68,228,144
tq 226,4,61440
buffer_load_dwordx4 v31, s[72:75], s52 offen offset:1024 lds
mqa 80,232,144
tq 228,4,63488
mqa 88,232,148
tq 230,8,0
mqa 92,236,148
tq 232,8,2048
mqa 84,236,144
tq 234,8,4096
s_waitcnt lgkmcnt(6)
mqa 64,240,152
tq 236,8,6144
mqa 72,240,156
tq 238,8,8192
mqa 76,244,156
tq 240,5,59392
mqa 68,244,152
tq 242,5,61440
mqa 80,248,152
tq 244,5,63488
mqa 88,248,156
tq 246,9,0
mqa 92,252,156
tq 248,9,2048
mqa 84,252,152
tq 250,9,4096
s_waitcnt lgkmcnt(6)
s_waitcnt vmcnt(8)
mqa 64,224,176
tq 252,9,6144
tq 254,9,8192
mqa 72,224,180
s_waitcnt vmcnt(6)
s_barrier
mqa 72,228,188
tq 224,6,59392
mqa 64,228,184
tq 226,6,61440
s_waitcnt vmcnt(2)
mqa 64,232,208
tq 228,6,63488
s_add_u32 m0, s56, 100352
mqa 72,232,212
tq 230,12,0
buffer_load_dwordx4 v10, s[76:79], s44 offen offset:0 lds
mqa 72,236,220
tq 232,12,2048
mqa 64,236,216
tq 234,12,4096
)ASM"
R"ASM(
s_waitcnt lgkmcnt(6)
mqa 68,240,176
tq 236,12,6144
mqa 76,240,180
tq 238,12,8192
buffer_load_dwordx4 v11, s[76:79], s44 offen offset:1024 lds
mqa 76,244,188
tq 240,7,59392
v_fma_f32 v200, v136, v72, v200
v_fma_f32 v201, v137, v73, v201
mqa 68,244,184
tq 242,7,61440
v_fma_f32 v202, v138, v74, v202
mqa 68,248,208
tq 244,7,63488
v_fma_f32 v203, v139, v75, v203
v_fma_f32 v192, v128, v64, v192
mqa 76,248,212
tq 246,13,0
v_fma_f32 v193, v129, v65, v193
mqa 76,252,220
tq 248,13,2048
v_fma_f32 v194, v130, v66, v194
v_fma_f32 v195, v131, v67, v195
mqa 68,252,216
tq 250,13,4096
s_waitcnt lgkmcnt(6)
mqa 80,224,176
tq 252,13,6144
s_add_u32 m0, s56, 2048
v_cvt_pk_bf16_f32 v36, v200, v201
mqa 88,224,180
tq 254,13,8192
buffer_load_dwordx4 v30, s[72:75], s54 offen offset:0 lds
mqa 88,228,188
tq 224,8,43008
v_cvt_pk_bf16_f32 v37, v202, v203
v_cvt_pk_bf16_f32 v32, v192, v193
mqa 80,228,184
tq 226,8,45056
buffer_load_dwordx4 v31, s[72:75], s54 offen offset:1024 lds
v_cvt_pk_bf16_f32 v33, v194, v195
mqa 80,232,208
tq 228,9,43008
s_add_u32 m0, s56, 141312
v_fma_f32 v204, v140, v76, v204
mqa 88,232,212
tq 230,9,45056
buffer_load_dwordx4 v30, s[72:75], s43 offen offset:0 lds
v_fma_f32 v205, v141, v77, v205
mqa 88,236,220
tq 232,12,43008
v_fma_f32 v206, v142, v78, v206
mqa 80,236,216
tq 234,12,45056
buffer_load_dwordx4 v31, s[72:75], s43 offen offset:1024 lds
s_waitcnt lgkmcnt(6)
mqa 84,240,176
tq 236,13,43008
v_fma_f32 v207, v143, v79, v207
v_fma_f32 v196, v132, v68, v196
mqa 92,240,180
tq 238,13,45056
v_fma_f32 v197, v133, v69, v197
mqa 92,244,188
tq 240,8,47104
v_fma_f32 v198, v134, v70, v198
v_fma_f32 v199, v135, v71, v199
mqa 84,244,184
tq 242,8,49152
v_cvt_pk_bf16_f32 v38, v204, v205
v_cvt_pk_bf16_f32 v39, v206, v207
mqa 84,248,208
tq 244,9,47104
mqa 92,248,212
tq 246,9,49152
buffer_store_dwordx4 v[36:39], v15, s[28:31], s80 offen offset:1024 sc1
v_cvt_pk_bf16_f32 v34, v196, v197
v_cvt_pk_bf16_f32 v35, v198, v199
buffer_store_dwordx4 v[32:35], v15, s[28:31], s80 offen offset:0 sc1
mqa 92,252,220
tq 248,12,47104
v_fma_f32 v216, v152, v88, v216
v_fma_f32 v217, v153, v89, v217
mqa 84,252,216
tq 250,12,49152
v_fma_f32 v218, v154, v90, v218
s_waitcnt lgkmcnt(6)
mqa 96,224,96
tq 252,13,47104
tq 254,13,49152
s_add_u32 m0, s56, 26624
mqa 104,224,100
buffer_load_dwordx4 v10, s[76:79], s48 offen offset:0 lds
s_waitcnt vmcnt(9)
s_barrier
mqa 108,228,100
tq 224,8,26624
v_fma_f32 v219, v155, v91, v219
v_fma_f32 v208, v144, v80, v208
mqa 100,228,96
tq 226,8,28672
v_fma_f32 v209, v145, v81, v209
v_fma_f32 v210, v146, v82, v210
mqa 112,232,96
tq 228,9,26624
v_fma_f32 v211, v147, v83, v211
v_cvt_pk_bf16_f32 v44, v216, v217
v_cvt_pk_bf16_f32 v45, v218, v219
mqa 120,232,100
tq 230,9,28672
buffer_load_dwordx4 v11, s[76:79], s48 offen offset:1024 lds
v_cvt_pk_bf16_f32 v40, v208, v209
mqa 124,236,100
tq 232,12,26624
v_cvt_pk_bf16_f32 v41, v210, v211
v_fma_f32 v220, v156, v92, v220
mqa 116,236,96
tq 234,12,28672
v_fma_f32 v221, v157, v93, v221
s_waitcnt lgkmcnt(6)
mqa 96,240,104
tq 236,13,26624
s_add_u32 m0, s56, 34816
v_fma_f32 v222, v158, v94, v222
mqa 104,240,108
tq 238,13,28672
buffer_load_dwordx4 v30, s[72:75], s47 offen offset:0 lds
v_fma_f32 v223, v159, v95, v223
mqa 108,244,108
tq 240,8,30720
v_fma_f32 v212, v148, v84, v212
v_fma_f32 v213, v149, v85, v213
mqa 100,244,104
tq 242,8,32768
v_fma_f32 v214, v150, v86, v214
v_fma_f32 v215, v151, v87, v215
mqa 112,248,104
tq 244,9,30720
v_cvt_pk_bf16_f32 v46, v220, v221
v_cvt_pk_bf16_f32 v47, v222, v223
mqa 120,248,108
tq 246,9,32768
buffer_load_dwordx4 v31, s[72:75], s47 offen offset:1024 lds
buffer_store_dwordx4 v[44:47], v15, s[28:31], s80 offen offset:3072 sc1
mqa 124,252,108
s_nop 0
L_hop5_2_%=:
s_cbranch_execz L_ret_2_%=
tq 248,12,30720
v_cvt_pk_bf16_f32 v42, v212, v213
v_cvt_pk_bf16_f32 v43, v214, v215
mqa 116,252,104
tq 250,12,32768
buffer_store_dwordx4 v[40:43], v15, s[28:31], s80 offen offset:2048 sc1
s_waitcnt lgkmcnt(6)
mqa 96,224,128
tq 252,13,30720
s_add_u32 m0, s56, 51200
mqa 104,224,132
tq 254,13,32768
buffer_load_dwordx4 v10, s[76:79], s52 offen offset:0 lds
mqa 108,228,132
tq 224,8,10240
ds_read_b128 v[64:67], v24 offset:1024
mqa 100,228,128
tq 226,8,12288
ds_read_b128 v[72:75], v24 offset:1024
mqa 112,232,128
tq 228,9,10240
ds_read_b128 v[68:71], v24 offset:1088
mqa 120,232,132
tq 230,9,12288
buffer_load_dwordx4 v11, s[76:79], s52 offen offset:1024 lds
mqa 124,236,132
tq 232,12,10240
s_add_u32 m0, s56, 124928
ds_read_b128 v[76:79], v24 offset:1088
mqa 116,236,128
tq 234,12,12288
buffer_load_dwordx4 v30, s[72:75], s49 offen offset:0 lds
s_waitcnt lgkmcnt(10)
mqa 96,240,136
tq 236,13,10240
mqa 104,240,140
tq 238,13,12288
buffer_load_dwordx4 v31, s[72:75], s49 offen offset:1024 lds
mqa 108,244,140
tq 240,8,14336
ds_read_b128 v[80:83], v24 offset:1152
mqa 100,244,136
tq 242,8,16384
mqa 112,248,136
tq 244,9,14336
s_add_u32 m0, s56, 133120
mqa 120,248,140
tq 246,9,16384
buffer_load_dwordx4 v30, s[72:75], s51 offen offset:0 lds
mqa 124,252,140
tq 248,12,14336
ds_read_b128 v[88:91], v24 offset:1152
mqa 116,252,136
tq 250,12,16384
s_waitcnt lgkmcnt(8)
mqa 96,224,160
tq 252,13,14336
tq 254,13,16384
mqa 104,224,164
s_waitcnt vmcnt(13)
s_barrier
mqa 108,228,164
tq 224,4,2048
ds_read_b128 v[84:87], v24 offset:1216
mqa 100,228,160
tq 226,4,4096
buffer_load_dwordx4 v31, s[72:75], s51 offen offset:1024 lds
mqa 112,232,160
tq 228,5,2048
s_add_u32 m0, s56, 149504
mqa 120,232,164
tq 230,5,4096
buffer_load_dwordx4 v30, s[72:75], s55 offen offset:0 lds
mqa 124,236,164
tq 232,6,2048
ds_read_b128 v[92:95], v24 offset:1216
mqa 116,236,160
tq 234,6,4096
s_waitcnt lgkmcnt(8)
mqa 96,240,168
tq 236,7,2048
mqa 104,240,172
tq 238,7,4096
buffer_load_dwordx4 v31, s[72:75], s55 offen offset:1024 lds
mqa 108,244,172
tq 240,4,6144
mqa 100,244,168
tq 242,4,8192
mqa 112,248,168
tq 244,5,6144
s_add_u32 m0, s56, 43008
mqa 120,248,172
tq 246,5,8192
buffer_load_dwordx4 v10, s[76:79], s40 offen offset:0 lds
mqa 124,252,172
tq 248,6,6144
mqa 116,252,168
tq 250,6,8192
s_waitcnt lgkmcnt(6)
mqa 96,224,192
tq 252,7,6144
mqa 104,224,196
tq 254,7,8192
buffer_load_dwordx4 v11, s[76:79], s40 offen offset:1024 lds
mqa 108,228,196
tq 224,16,10240
mqa 100,228,192
tq 226,16,12288
mqa 112,232,192
tq 228,17,10240
s_add_u32 m0, s56, 83968
mqa 120,232,196
tq 230,17,12288
buffer_load_dwordx4 v10, s[76:79], s43 offen offset:0 lds
mqa 124,236,196
tq 232,28,10240
mqa 116,236,192
tq 234,28,12288
s_waitcnt lgkmcnt(6)
mqa 96,240,200
tq 236,29,10240
mqa 104,240,204
tq 238,29,12288
buffer_load_dwordx4 v11, s[76:79], s43 offen offset:1024 lds
mqa 108,244,204
tq 240,16,14336
mqa 100,244,200
tq 242,16,16384
mqa 112,248,200
tq 244,17,14336
s_add_u32 m0, s56, 59392
mqa 120,248,204
tq 246,17,16384
buffer_load_dwordx4 v30, s[76:79], s40 offen offset:0 lds
mqa 124,252,204
tq 248,28,14336
mqa 116,252,200
tq 250,28,16384
s_waitcnt vmcnt(13)
buffer_store_dword v22, v21, s[28:31], 0 offen offset:64 sc1
s_waitcnt lgkmcnt(6)
mqa 96,224,16
tq 252,29,14336
tq 254,29,16384
mqa 104,224,20
s_barrier
mqa 108,228,20
tq 224,8,51200
mqa 100,228,16
tq 226,8,53248
buffer_load_dwordx4 v31, s[76:79], s40 offen offset:1024 lds
mqa 112,232,16
tq 228,9,51200
s_add_u32 m0, s56, 67584
mqa 120,232,20
tq 230,9,53248
buffer_load_dwordx4 v30, s[76:79], s41 offen offset:0 lds
mqa 124,236,20
tq 232,12,51200
mqa 116,236,16
tq 234,12,53248
s_waitcnt lgkmcnt(6)
mqa 96,240,24
tq 236,13,51200
mqa 104,240,28
tq 238,13,53248
buffer_load_dwordx4 v31, s[76:79], s41 offen offset:1024 lds
mqa 108,244,28
tq 240,8,55296
mqa 100,244,24
tq 242,8,57344
mqa 112,248,24
tq 244,9,55296
s_add_u32 m0, s56, 92160
mqa 120,248,28
tq 246,9,57344
buffer_load_dwordx4 v10, s[76:79], s42 offen offset:0 lds
mqa 124,252,28
tq 248,12,55296
mqa 116,252,24
tq 250,12,57344
s_waitcnt lgkmcnt(6)
mqa 96,224,48
tq 252,13,55296
mqa 104,224,52
tq 254,13,57344
buffer_load_dwordx4 v11, s[76:79], s42 offen offset:1024 lds
mqa 108,228,52
tq 224,4,34816
mqa 100,228,48
tq 226,4,36864
mqa 112,232,48
tq 228,5,34816
s_add_u32 m0, s56, 108544
mqa 120,232,52
tq 230,5,36864
buffer_load_dwordx4 v30, s[76:79], s42 offen offset:0 lds
mqa 124,236,52
tq 232,6,34816
mqa 116,236,48
tq 234,6,36864
s_waitcnt lgkmcnt(6)
mqa 96,240,56
tq 236,7,34816
mqa 104,240,60
tq 238,7,36864
buffer_load_dwordx4 v31, s[76:79], s42 offen offset:1024 lds
mqa 108,244,60
tq 240,4,38912
mqa 100,244,56
tq 242,4,40960
mqa 112,248,56
tq 244,5,38912
s_add_u32 m0, s56, 2048
mqa 120,248,60
tq 246,5,40960
buffer_load_dwordx4 v30, s[76:79], s44 offen offset:0 lds
mqa 124,252,60
tq 248,6,38912
mqa 116,252,56
tq 250,6,40960
s_waitcnt lgkmcnt(6)
mqa 96,224,80
tq 252,7,38912
tq 254,7,40960
mqa 104,224,84
s_waitcnt vmcnt(16)
s_barrier
mqa 108,228,84
tq 224,8,59392
mqa 100,228,80
tq 226,8,61440
buffer_load_dwordx4 v31, s[76:79], s44 offen offset:1024 lds
mqa 112,232,80
tq 228,9,59392
s_add_u32 m0, s56, 75776
mqa 120,232,84
tq 230,9,61440
buffer_load_dwordx4 v10, s[76:79], s46 offen offset:0 lds
mqa 124,236,84
tq 232,12,59392
mqa 116,236,80
tq 234,12,61440
s_waitcnt lgkmcnt(6)
mqa 96,240,88
tq 236,13,59392
mqa 104,240,92
tq 238,13,61440
buffer_load_dwordx4 v11, s[76:79], s46 offen offset:1024 lds
mqa 108,244,92
tq 240,8,63488
mqa 100,244,88
tq 242,16,0
mqa 112,248,88
tq 244,9,63488
mqa 120,248,92
tq 246,17,0
mqa 124,252,92
tq 248,12,63488
mqa 116,252,88
tq 250,28,0
s_waitcnt lgkmcnt(6)
mqa 96,224,112
tq 252,13,63488
s_add_u32 m0, s56, 116736
mqa 104,224,116
tq 254,29,0
buffer_load_dwordx4 v10, s[76:79], s51 offen offset:0 lds
mqa 108,228,116
tq 224,16,2048
mqa 100,228,112
tq 226,16,4096
buffer_load_dwordx4 v11, s[76:79], s51 offen offset:1024 lds
mqa 112,232,112
tq 228,17,2048
s_add_u32 m0, s56, 141312
mqa 120,232,116
tq 230,17,4096
buffer_load_dwordx4 v30, s[76:79], s46 offen offset:0 lds
mqa 124,236,116
tq 232,28,2048
mqa 116,236,112
tq 234,28,4096
s_waitcnt lgkmcnt(6)
mqa 96,240,120
tq 236,29,2048
mqa 104,240,124
tq 238,29,4096
buffer_load_dwordx4 v31, s[76:79], s46 offen offset:1024 lds
mqa 108,244,124
tq 240,16,6144
mqa 100,244,120
tq 242,16,8192
mqa 112,248,120
tq 244,17,6144
mqa 120,248,124
tq 246,17,8192
mqa 124,252,124
tq 248,28,6144
mqa 116,252,120
tq 250,28,8192
s_waitcnt lgkmcnt(6)
mqa 96,224,144
tq 252,29,6144
tq 254,29,8192
mqa 104,224,148
s_waitcnt vmcnt(21)
s_barrier
mqa 108,228,148
tq 224,4,18432
mqa 100,228,144
tq 226,4,20480
mqa 112,232,144
tq 228,4,22528
mqa 120,232,148
tq 230,4,24576
mqa 124,236,148
tq 232,16,18432
mqa 116,236,144
tq 234,16,20480
s_waitcnt lgkmcnt(6)
mqa 96,240,152
tq 236,16,22528
mqa 104,240,156
tq 238,16,24576
mqa 108,244,156
tq 240,5,18432
mqa 100,244,152
tq 242,5,20480
mqa 112,248,152
tq 244,5,22528
mqa 120,248,156
tq 246,5,24576
mqa 124,252,156
tq 248,17,18432
mqa 116,252,152
tq 250,17,20480
s_waitcnt lgkmcnt(6)
mqa 96,224,176
tq 252,17,22528
s_add_u32 m0, s56, 34816
mqa 104,224,180
tq 254,17,24576
buffer_load_dwordx4 v30, s[76:79], s48 offen offset:0 lds
mqa 104,228,188
tq 224,6,18432
mqa 96,228,184
tq 226,6,20480
buffer_load_dwordx4 v31, s[76:79], s48 offen offset:1024 lds
mqa 96,232,208
tq 228,6,22528
mqa 104,232,212
tq 230,6,24576
mqa 104,236,220
tq 232,28,18432
mqa 96,236,216
tq 234,28,20480
s_waitcnt lgkmcnt(6)
mqa 100,240,176
tq 236,28,22528
mqa 108,240,180
tq 238,28,24576
mqa 108,244,188
tq 240,7,18432
v_fma_f32 v232, v168, v104, v232
v_fma_f32 v233, v169, v105, v233
mqa 100,244,184
tq 242,7,20480
v_fma_f32 v234, v170, v106, v234
mqa 100,248,208
tq 244,7,22528
v_fma_f32 v235, v171, v107, v235
v_fma_f32 v224, v160, v96, v224
mqa 108,248,212
tq 246,7,24576
v_fma_f32 v225, v161, v97, v225
mqa 108,252,220
tq 248,29,18432
v_fma_f32 v226, v162, v98, v226
v_fma_f32 v227, v163, v99, v227
mqa 100,252,216
tq 250,29,20480
s_waitcnt lgkmcnt(6)
mqa 112,224,176
tq 252,29,22528
tq 254,29,24576
mqa 120,224,180
buffer_load_dword v23, v20, s[28:31], 0 offen offset:64 sc1
s_add_u32 m0, s56, 124928
s_nop 0
buffer_load_dwordx4 v10, s[76:79], s50 offen offset:0 lds
s_waitcnt vmcnt(18)
s_barrier
mqa 120,228,188
tq 224,4,43008
v_cvt_pk_bf16_f32 v52, v232, v233
v_cvt_pk_bf16_f32 v53, v234, v235
mqa 112,228,184
tq 226,4,45056
v_cvt_pk_bf16_f32 v48, v224, v225
mqa 112,232,208
tq 228,5,43008
v_cvt_pk_bf16_f32 v49, v226, v227
v_fma_f32 v236, v172, v108, v236
mqa 120,232,212
tq 230,5,45056
buffer_load_dwordx4 v11, s[76:79], s50 offen offset:1024 lds
mqa 120,236,220
tq 232,4,47104
v_fma_f32 v237, v173, v109, v237
v_fma_f32 v238, v174, v110, v238
mqa 112,236,216
tq 234,4,49152
s_waitcnt lgkmcnt(6)
mqa 116,240,176
tq 236,5,47104
v_fma_f32 v239, v175, v111, v239
v_fma_f32 v228, v164, v100, v228
mqa 124,240,180
tq 238,5,49152
v_fma_f32 v229, v165, v101, v229
mqa 124,244,188
tq 240,6,43008
v_fma_f32 v230, v166, v102, v230
v_fma_f32 v231, v167, v103, v231
mqa 116,244,184
tq 242,6,45056
v_cvt_pk_bf16_f32 v54, v236, v237
v_cvt_pk_bf16_f32 v55, v238, v239
mqa 116,248,208
tq 244,7,43008
mqa 124,248,212
tq 246,7,45056
buffer_store_dwordx4 v[52:55], v15, s[28:31], s81 offen offset:1024 sc1
v_cvt_pk_bf16_f32 v50, v228, v229
v_cvt_pk_bf16_f32 v51, v230, v231
buffer_store_dwordx4 v[48:51], v15, s[28:31], s81 offen offset:0 sc1
mqa 124,252,220
tq 248,6,47104
ds_read_b128 v[96:99], v24 offset:1280
v_fma_f32 v248, v184, v120, v248
v_fma_f32 v249, v185, v121, v249
mqa 116,252,216
tq 250,6,49152
s_waitcnt lgkmcnt(7)
mqv 64,224,32
tq 252,7,47104
s_add_u32 m0, s56, 133120
v_fma_f32 v250, v186, v122, v250
mqv 72,224,36
tq 254,7,49152
buffer_load_dwordx4 v30, s[76:79], s50 offen offset:0 lds
v_fma_f32 v251, v187, v123, v251
mqv 76,228,36
tq 224,4,59392
ds_read_b128 v[104:107], v24 offset:1280
v_fma_f32 v240, v176, v112, v240
mqv 68,228,32
tq 226,4,61440
v_fma_f32 v241, v177, v113, v241
v_fma_f32 v242, v178, v114, v242
mqv 64,232,40
tq 228,5,59392
v_fma_f32 v243, v179, v115, v243
v_cvt_pk_bf16_f32 v60, v248, v249
v_cvt_pk_bf16_f32 v61, v250, v251
mqv 72,232,44
tq 230,5,61440
buffer_load_dwordx4 v31, s[76:79], s50 offen offset:1024 lds
ds_read_b128 v[100:103], v24 offset:1344
mqv 76,236,44
tq 232,4,63488
v_cvt_pk_bf16_f32 v56, v240, v241
v_cvt_pk_bf16_f32 v57, v242, v243
ds_read_b128 v[108:111], v24 offset:1344
mqv 68,236,40
tq 234,8,0
v_fma_f32 v252, v188, v124, v252
s_waitcnt lgkmcnt(9)
mqv 80,240,32
tq 236,5,63488
v_fma_f32 v253, v189, v125, v253
v_fma_f32 v254, v190, v126, v254
mqv 88,240,36
tq 238,9,0
v_fma_f32 v255, v191, v127, v255
ds_read_b128 v[112:115], v24 offset:1408
mqv 92,244,36
tq 240,6,59392
v_fma_f32 v244, v180, v116, v244
v_fma_f32 v245, v181, v117, v245
mqv 84,244,32
tq 242,6,61440
v_fma_f32 v246, v182, v118, v246
v_fma_f32 v247, v183, v119, v247
mqv 80,248,40
tq 244,7,59392
v_cvt_pk_bf16_f32 v62, v252, v253
v_cvt_pk_bf16_f32 v63, v254, v255
mqv 88,248,44
tq 246,7,61440
buffer_store_dwordx4 v[60:63], v15, s[28:31], s81 offen offset:3072 sc1
ds_read_b128 v[120:123], v24 offset:1408
mqv 92,252,44
tq 248,6,63488
v_cvt_pk_bf16_f32 v58, v244, v245
v_cvt_pk_bf16_f32 v59, v246, v247
mqv 84,252,40
tq 250,12,0
buffer_store_dwordx4 v[56:59], v15, s[28:31], s81 offen offset:2048 sc1
ds_read_b128 v[116:119], v24 offset:1472
s_waitcnt lgkmcnt(9)
mqv 96,224,32
tq 252,7,63488
tq 254,13,0
ds_read_b128 v[124:127], v24 offset:1472
mqv 104,224,36
s_waitcnt vmcnt(8)
v_cmp_ne_u32 vcc, v23, v22
s_cbranch_vccnz L_slow2_2_%=
L_back2_2_%=:
buffer_load_dwordx4 a[0:3], v15, s[28:31], s82 offen offset:0 sc1
s_barrier
mqv 108,228,36
tq 224,4,10240
mqv 100,228,32
tq 226,4,12288
buffer_load_dwordx4 a[4:7], v15, s[28:31], s82 offen offset:1024 sc1
mqv 96,232,40
tq 228,5,10240
mqv 104,232,44
s_nop 0
L_hop6_2_%=:
s_cbranch_execz L_ret_2_%=
tq 230,5,12288
buffer_load_dwordx4 a[8:11], v15, s[28:31], s82 offen offset:2048 sc1
mqv 108,236,44
tq 232,6,10240
mqv 100,236,40
tq 234,6,12288
buffer_load_dwordx4 a[12:15], v15, s[28:31], s82 offen offset:3072 sc1
s_waitcnt lgkmcnt(6)
mqv 112,240,32
tq 236,7,10240
mqv 120,240,36
tq 238,7,12288
mqv 124,244,36
tq 240,4,14336
mqv 116,244,32
tq 242,4,16384
mqv 112,248,40
tq 244,5,14336
s_add_u32 m0, s56, 18432
mqv 120,248,44
tq 246,5,16384
buffer_load_dwordx4 v30, s[76:79], s45 offen offset:0 lds
mqv 124,252,44
tq 248,6,14336
mqv 116,252,40
tq 250,6,16384
s_waitcnt lgkmcnt(6)
mqv 64,224,48
tq 252,7,14336
mqv 72,224,52
tq 254,7,16384
buffer_load_dwordx4 v31, s[76:79], s45 offen offset:1024 lds
mqv 76,228,52
tq 224,8,2048
mqv 68,228,48
tq 226,8,4096
mqv 80,232,48
tq 228,9,2048
mqv 88,232,52
tq 230,9,4096
buffer_load_dwordx4 a[32:35], v15, s[28:31], s84 offen offset:0 sc1
mqv 92,236,52
tq 232,12,2048
mqv 84,236,48
tq 234,12,4096
s_waitcnt lgkmcnt(6)
mqv 64,240,56
tq 236,13,2048
mqv 72,240,60
tq 238,13,4096
buffer_load_dwordx4 a[36:39], v15, s[28:31], s84 offen offset:1024 sc1
mqv 76,244,60
tq 240,8,6144
mqv 68,244,56
tq 242,8,8192
mqv 80,248,56
tq 244,9,6144
mqv 88,248,60
tq 246,9,8192
buffer_load_dwordx4 a[40:43], v15, s[28:31], s84 offen offset:2048 sc1
mqv 92,252,60
tq 248,12,6144
mqv 84,252,56
tq 250,12,8192
buffer_load_dwordx4 a[44:47], v15, s[28:31], s84 offen offset:3072 sc1
s_waitcnt lgkmcnt(6)
mqv 96,224,48
tq 252,13,6144
tq 254,13,8192
mqv 104,224,52
s_barrier
mqv 108,228,52
tq 224,8,26624
mqv 100,228,48
tq 226,8,28672
mqv 112,232,48
tq 228,9,26624
mqv 120,232,52
tq 230,9,28672
buffer_load_dwordx4 a[64:67], v15, s[28:31], s86 offen offset:0 sc1
mqv 124,236,52
tq 232,12,26624
mqv 116,236,48
tq 234,12,28672
s_waitcnt lgkmcnt(6)
mqv 96,240,56
tq 236,13,26624
mqv 104,240,60
tq 238,13,28672
buffer_load_dwordx4 a[68:71], v15, s[28:31], s86 offen offset:1024 sc1
mqv 108,244,60
tq 240,8,30720
mqv 100,244,56
tq 242,8,32768
mqv 112,248,56
tq 244,9,30720
mqv 120,248,60
tq 246,9,32768
buffer_load_dwordx4 a[72:75], v15, s[28:31], s86 offen offset:2048 sc1
mqv 124,252,60
tq 248,12,30720
mqv 116,252,56
tq 250,12,32768
s_waitcnt vmcnt(13)
buffer_store_dword v22, v21, s[28:31], 0 offen offset:96 sc1
s_waitcnt lgkmcnt(6)
s_waitcnt vmcnt(10)
mqa 64,224,0
tq 252,13,30720
mqa 72,224,4
tq 254,13,32768
buffer_load_dwordx4 a[76:79], v15, s[28:31], s86 offen offset:3072 sc1
mqa 76,228,4
tq 224,8,43008
mqa 68,228,0
tq 226,8,45056
mqa 80,232,0
tq 228,9,43008
s_add_u32 m0, s56, 43008
mqa 88,232,4
tq 230,9,45056
buffer_load_dwordx4 v30, s[76:79], s53 offen offset:0 lds
mqa 92,236,4
tq 232,12,43008
mqa 84,236,0
tq 234,12,45056
s_waitcnt lgkmcnt(6)
mqa 64,240,8
tq 236,13,43008
mqa 72,240,12
tq 238,13,45056
buffer_load_dwordx4 v31, s[76:79], s53 offen offset:1024 lds
mqa 76,244,12
tq 240,8,47104
mqa 68,244,8
tq 242,8,49152
mqa 80,248,8
tq 244,9,47104
mqa 88,248,12
tq 246,9,49152
buffer_load_dwordx4 a[96:99], v15, s[28:31], s88 offen offset:0 sc1
mqa 92,252,12
tq 248,12,47104
mqa 84,252,8
tq 250,12,49152
s_waitcnt lgkmcnt(6)
mqa 96,224,0
tq 252,13,47104
tq 254,13,49152
mqa 104,224,4
s_barrier
mqa 108,228,4
tq 224,8,34816
mqa 100,228,0
tq 226,8,36864
buffer_load_dwordx4 a[100:103], v15, s[28:31], s88 offen offset:1024 sc1
mqa 112,232,0
tq 228,9,34816
mqa 120,232,4
tq 230,9,36864
buffer_load_dwordx4 a[104:107], v15, s[28:31], s88 offen offset:2048 sc1
mqa 124,236,4
tq 232,12,34816
mqa 116,236,0
tq 234,12,36864
s_waitcnt lgkmcnt(6)
mqa 96,240,8
tq 236,13,34816
mqa 104,240,12
tq 238,13,36864
buffer_load_dwordx4 a[108:111], v15, s[28:31], s88 offen offset:3072 sc1
mqa 108,244,12
tq 240,8,38912
mqa 100,244,8
tq 242,8,40960
mqa 112,248,8
tq 244,9,38912
mqa 120,248,12
tq 246,9,40960
buffer_load_dwordx4 a[128:131], v15, s[28:31], s90 offen offset:0 sc1
mqa 124,252,12
tq 248,12,38912
mqa 116,252,8
tq 250,12,40960
s_waitcnt lgkmcnt(6)
s_waitcnt vmcnt(12)
mqa 64,224,32
tq 252,13,38912
mqa 72,224,36
tq 254,13,40960
buffer_load_dwordx4 a[132:135], v15, s[28:31], s90 offen offset:1024 sc1
mqa 76,228,36
tq 224,4,2048
mqa 68,228,32
tq 226,4,4096
mqa 80,232,32
tq 228,5,2048
mqa 88,232,36
tq 230,5,4096
buffer_load_dwordx4 a[136:139], v15, s[28:31], s90 offen offset:2048 sc1
mqa 92,236,36
tq 232,6,2048
mqa 84,236,32
tq 234,6,4096
s_waitcnt lgkmcnt(6)
mqa 64,240,40
tq 236,7,2048
mqa 72,240,44
tq 238,7,4096
buffer_load_dwordx4 a[140:143], v15, s[28:31], s90 offen offset:3072 sc1
mqa 76,244,44
tq 240,4,6144
mqa 68,244,40
tq 242,4,8192
mqa 80,248,40
tq 244,5,6144
s_add_u32 m0, s56, 149504
mqa 88,248,44
tq 246,5,8192
buffer_load_dwordx4 v30, s[76:79], s52 offen offset:0 lds
mqa 92,252,44
tq 248,6,6144
mqa 84,252,40
tq 250,6,8192
s_waitcnt lgkmcnt(6)
mqa 96,224,32
tq 252,7,6144
tq 254,7,8192
mqa 104,224,36
s_barrier
mqa 108,228,36
tq 224,8,10240
mqa 100,228,32
tq 226,8,12288
buffer_load_dwordx4 v31, s[76:79], s52 offen offset:1024 lds
mqa 112,232,32
tq 228,9,10240
mqa 120,232,36
tq 230,9,12288
buffer_load_dwordx4 a[160:163], v15, s[28:31], s92 offen offset:0 sc1
mqa 124,236,36
tq 232,12,10240
mqa 116,236,32
tq 234,12,12288
s_waitcnt lgkmcnt(6)
mqa 96,240,40
tq 236,13,10240
mqa 104,240,44
tq 238,13,12288
buffer_load_dwordx4 a[164:167], v15, s[28:31], s92 offen offset:1024 sc1
mqa 108,244,44
tq 240,8,14336
mqa 100,244,40
tq 242,8,16384
mqa 112,248,40
tq 244,9,14336
mqa 120,248,44
tq 246,9,16384
buffer_load_dwordx4 a[168:171], v15, s[28:31], s92 offen offset:2048 sc1
mqa 124,252,44
tq 248,12,14336
mqa 116,252,40
tq 250,12,16384
s_waitcnt lgkmcnt(6)
s_waitcnt vmcnt(15)
mqa 64,224,64
tq 252,13,14336
mqa 72,224,68
tq 254,13,16384
buffer_load_dwordx4 a[172:175], v15, s[28:31], s92 offen offset:3072 sc1
mqa 76,228,68
tq 224,16,10240
mqa 68,228,64
tq 226,16,12288
mqa 80,232,64
tq 228,17,10240
s_add_u32 m0, s56, 10240
mqa 88,232,68
tq 230,17,12288
buffer_load_dwordx4 v10, s[76:79], s54 offen offset:0 lds
mqa 92,236,68
tq 232,28,10240
mqa 84,236,64
tq 234,28,12288
s_waitcnt lgkmcnt(6)
mqa 64,240,72
tq 236,29,10240
mqa 72,240,76
tq 238,29,12288
buffer_load_dwordx4 v11, s[76:79], s54 offen offset:1024 lds
mqa 76,244,76
tq 240,16,14336
mqa 68,244,72
tq 242,16,16384
mqa 80,248,72
tq 244,17,14336
s_add_u32 m0, s56, 59392
mqa 88,248,76
tq 246,17,16384
buffer_load_dwordx4 v30, s[76:79], s54 offen offset:0 lds
mqa 92,252,76
tq 248,28,14336
mqa 84,252,72
tq 250,28,16384
s_waitcnt lgkmcnt(6)
mqa 96,224,64
tq 252,29,14336
tq 254,29,16384
mqa 104,224,68
s_barrier
mqa 108,228,68
tq 224,4,26624
mqa 100,228,64
tq 226,4,28672
buffer_load_dwordx4 v31, s[76:79], s54 offen offset:1024 lds
mqa 112,232,64
tq 228,5,26624
mqa 120,232,68
tq 230,5,28672
buffer_load_dwordx4 a[192:195], v15, s[28:31], s94 offen offset:0 sc1
mqa 124,236,68
tq 232,6,26624
mqa 116,236,64
tq 234,6,28672
s_waitcnt lgkmcnt(6)
mqa 96,240,72
tq 236,7,26624
mqa 104,240,76
tq 238,7,28672
buffer_load_dwordx4 a[196:199], v15, s[28:31], s94 offen offset:1024 sc1
mqa 108,244,76
tq 240,4,30720
mqa 100,244,72
tq 242,4,32768
mqa 112,248,72
tq 244,5,30720
mqa 120,248,76
tq 246,5,32768
buffer_load_dwordx4 a[200:203], v15, s[28:31], s94 offen offset:2048 sc1
mqa 124,252,76
tq 248,6,30720
mqa 116,252,72
tq 250,6,32768
s_waitcnt lgkmcnt(6)
s_waitcnt vmcnt(17)
mqa 64,224,96
tq 252,7,30720
mqa 72,224,100
tq 254,7,32768
buffer_load_dwordx4 a[204:207], v15, s[28:31], s94 offen offset:3072 sc1
mqa 76,228,100
tq 224,4,34816
mqa 68,228,96
tq 226,4,36864
mqa 80,232,96
tq 228,5,34816
s_add_u32 m0, s56, 67584
mqa 88,232,100
tq 230,5,36864
buffer_load_dwordx4 v10, s[76:79], s45 offen offset:0 lds
mqa 92,236,100
tq 232,6,34816
mqa 84,236,96
tq 234,6,36864
s_waitcnt lgkmcnt(6)
mqa 64,240,104
tq 236,7,34816
mqa 72,240,108
tq 238,7,36864
buffer_load_dwordx4 v11, s[76:79], s45 offen offset:1024 lds
mqa 76,244,108
tq 240,4,38912
mqa 68,244,104
tq 242,4,40960
mqa 80,248,104
tq 244,5,38912
s_add_u32 m0, s56, 92160
mqa 88,248,108
tq 246,5,40960
buffer_load_dwordx4 v10, s[76:79], s47 offen offset:0 lds
mqa 92,252,108
tq 248,6,38912
mqa 84,252,104
tq 250,6,40960
s_waitcnt lgkmcnt(6)
mqa 96,224,96
tq 252,7,38912
tq 254,7,40960
mqa 104,224,100
buffer_load_dword v23, v20, s[28:31], 0 offen offset:96 sc1
s_barrier
mqa 108,228,100
tq 224,8,59392
mqa 100,228,96
tq 226,8,61440
buffer_load_dwordx4 v11, s[76:79], s47 offen offset:1024 lds
mqa 112,232,96
tq 228,9,59392
s_add_u32 m0, s56, 100352
mqa 120,232,100
tq 230,9,61440
buffer_load_dwordx4 v10, s[76:79], s49 offen offset:0 lds
mqa 124,236,100
tq 232,12,59392
mqa 116,236,96
tq 234,12,61440
s_waitcnt lgkmcnt(6)
mqa 96,240,104
tq 236,13,59392
mqa 104,240,108
tq 238,13,61440
buffer_load_dwordx4 v11, s[76:79], s49 offen offset:1024 lds
mqa 108,244,108
tq 240,8,63488
mqa 100,244,104
tq 242,16,0
mqa 112,248,104
tq 244,9,63488
s_add_u32 m0, s56, 108544
mqa 120,248,108
tq 246,17,0
buffer_load_dwordx4 v10, s[76:79], s53 offen offset:0 lds
mqa 124,252,108
tq 248,12,63488
mqa 116,252,104
tq 250,28,0
s_waitcnt lgkmcnt(6)
s_waitcnt vmcnt(22)
mqa 64,224,128
tq 252,13,63488
mqa 72,224,132
tq 254,29,0
buffer_load_dwordx4 v11, s[76:79], s53 offen offset:1024 lds
mqa 76,228,132
tq 224,16,2048
mqa 68,228,128
tq 226,16,4096
mqa 80,232,128
tq 228,17,2048
s_add_u32 m0, s56, 2048
mqa 88,232,132
tq 230,17,4096
buffer_load_dwordx4 v10, s[76:79], s55 offen offset:0 lds
mqa 92,236,132
tq 232,28,2048
mqa 84,236,128
tq 234,28,4096
s_waitcnt lgkmcnt(6)
mqa 64,240,136
tq 236,29,2048
mqa 72,240,140
tq 238,29,4096
buffer_load_dwordx4 v11, s[76:79], s55 offen offset:1024 lds
mqa 76,244,140
tq 240,16,6144
mqa 68,244,136
tq 242,16,8192
mqa 80,248,136
tq 244,17,6144
s_add_u32 m0, s56, 75776
mqa 88,248,140
tq 246,17,8192
buffer_load_dwordx4 v30, s[76:79], s43 offen offset:0 lds
mqa 92,252,140
tq 248,28,6144
mqa 84,252,136
tq 250,28,8192
s_waitcnt lgkmcnt(6)
mqa 96,224,128
tq 252,29,6144
tq 254,29,8192
mqa 104,224,132
s_waitcnt vmcnt(8)
v_cmp_ne_u32 vcc, v23, v22
s_cbranch_vccnz L_slow3_2_%=
L_back3_2_%=:
s_barrier
mqa 108,228,132
tq 224,4,51200
mqa 100,228,128
tq 226,4,53248
buffer_load_dwordx4 v31, s[76:79], s43 offen offset:1024 lds
mqa 112,232,128
tq 228,5,51200
mqa 120,232,132
tq 230,5,53248
buffer_load_dwordx4 a[16:19], v15, s[28:31], s83 offen offset:0 sc1
mqa 124,236,132
tq 232,6,51200
mqa 116,236,128
tq 234,6,53248
s_waitcnt lgkmcnt(6)
mqa 96,240,136
tq 236,7,51200
mqa 104,240,140
tq 238,7,53248
buffer_load_dwordx4 a[20:23], v15, s[28:31], s83 offen offset:1024 sc1
mqa 108,244,140
tq 240,4,55296
mqa 100,244,136
tq 242,4,57344
mqa 112,248,136
tq 244,5,55296
mqa 120,248,140
tq 246,5,57344
buffer_load_dwordx4 a[24:27], v15, s[28:31], s83 offen offset:2048 sc1
mqa 124,252,140
tq 248,6,55296
mqa 116,252,136
tq 250,6,57344
s_waitcnt lgkmcnt(6)
mqa 64,224,160
tq 252,7,55296
mqa 72,224,164
tq 254,7,57344
buffer_load_dwordx4 a[28:31], v15, s[28:31], s83 offen offset:3072 sc1
mqa 76,228,164
tq 224,16,18432
mqa 68,228,160
tq 226,16,20480
mqa 80,232,160
tq 228,17,18432
mqa 88,232,164
tq 230,17,20480
buffer_load_dwordx4 a[48:51], v15, s[28:31], s85 offen offset:0 sc1
mqa 92,236,164
tq 232,28,18432
mqa 84,236,160
tq 234,28,20480
s_waitcnt lgkmcnt(6)
mqa 64,240,168
tq 236,29,18432
mqa 72,240,172
s_nop 0
L_hop7_2_%=:
s_cbranch_execz L_ret_2_%=
tq 238,29,20480
buffer_load_dwordx4 a[52:55], v15, s[28:31], s85 offen offset:1024 sc1
mqa 76,244,172
tq 240,16,22528
mqa 68,244,168
tq 242,16,24576
mqa 80,248,168
tq 244,17,22528
mqa 88,248,172
tq 246,17,24576
buffer_load_dwordx4 a[56:59], v15, s[28:31], s85 offen offset:2048 sc1
mqa 92,252,172
tq 248,28,22528
mqa 84,252,168
tq 250,28,24576
s_waitcnt lgkmcnt(6)
mqa 96,224,160
tq 252,29,22528
tq 254,29,24576
mqa 104,224,164
s_barrier
mqa 108,228,164
tq 224,4,10240
mqa 100,228,160
tq 226,4,12288
buffer_load_dwordx4 a[60:63], v15, s[28:31], s85 offen offset:3072 sc1
mqa 112,232,160
tq 228,5,10240
mqa 120,232,164
tq 230,5,12288
buffer_load_dwordx4 a[80:83], v15, s[28:31], s87 offen offset:0 sc1
mqa 124,236,164
tq 232,6,10240
mqa 116,236,160
tq 234,6,12288
s_waitcnt lgkmcnt(6)
mqa 96,240,168
tq 236,7,10240
mqa 104,240,172
tq 238,7,12288
buffer_load_dwordx4 a[84:87], v15, s[28:31], s87 offen offset:1024 sc1
mqa 108,244,172
tq 240,4,14336
mqa 100,244,168
tq 242,4,16384
mqa 112,248,168
tq 244,5,14336
mqa 120,248,172
tq 246,5,16384
buffer_load_dwordx4 a[88:91], v15, s[28:31], s87 offen offset:2048 sc1
mqa 124,252,172
tq 248,6,14336
mqa 116,252,168
tq 250,6,16384
s_waitcnt lgkmcnt(6)
mqa 64,224,192
tq 252,7,14336
mqa 72,224,196
tq 254,7,16384
buffer_load_dwordx4 a[92:95], v15, s[28:31], s87 offen offset:3072 sc1
mqa 76,228,196
tq 224,4,59392
mqa 68,228,192
tq 226,4,61440
mqa 80,232,192
tq 228,5,59392
mqa 88,232,196
tq 230,5,61440
buffer_load_dwordx4 a[112:115], v15, s[28:31], s89 offen offset:0 sc1
mqa 92,236,196
tq 232,6,59392
mqa 84,236,192
tq 234,6,61440
s_waitcnt lgkmcnt(6)
mqa 64,240,200
tq 236,7,59392
mqa 72,240,204
tq 238,7,61440
buffer_load_dwordx4 a[116:119], v15, s[28:31], s89 offen offset:1024 sc1
mqa 76,244,204
tq 240,4,63488
mqa 68,244,200
tq 242,8,0
mqa 80,248,200
tq 244,5,63488
mqa 88,248,204
tq 246,9,0
buffer_load_dwordx4 a[120:123], v15, s[28:31], s89 offen offset:2048 sc1
mqa 92,252,204
tq 248,6,63488
mqa 84,252,200
tq 250,12,0
s_waitcnt lgkmcnt(6)
mqa 96,224,192
tq 252,7,63488
tq 254,13,0
mqa 104,224,196
s_barrier
mqa 108,228,196
tq 224,8,18432
mqa 100,228,192
tq 226,8,20480
buffer_load_dwordx4 a[124:127], v15, s[28:31], s89 offen offset:3072 sc1
mqa 112,232,192
tq 228,9,18432
mqa 120,232,196
tq 230,9,20480
buffer_load_dwordx4 a[144:147], v15, s[28:31], s91 offen offset:0 sc1
mqa 124,236,196
tq 232,12,18432
mqa 116,236,192
tq 234,12,20480
s_waitcnt lgkmcnt(6)
mqa 96,240,200
tq 236,13,18432
mqa 104,240,204
tq 238,13,20480
buffer_load_dwordx4 a[148:151], v15, s[28:31], s91 offen offset:1024 sc1
mqa 108,244,204
tq 240,8,22528
mqa 100,244,200
tq 242,8,24576
mqa 112,248,200
tq 244,9,22528
mqa 120,248,204
tq 246,9,24576
buffer_load_dwordx4 a[152:155], v15, s[28:31], s91 offen offset:2048 sc1
mqa 124,252,204
tq 248,12,22528
mqa 116,252,200
tq 250,12,24576
s_waitcnt lgkmcnt(6)
s_waitcnt vmcnt(15)
mqa 64,224,16
tq 252,13,22528
mqa 72,224,20
tq 254,13,24576
buffer_load_dwordx4 a[156:159], v15, s[28:31], s91 offen offset:3072 sc1
mqa 76,228,20
tq 224,8,2048
mqa 68,228,16
tq 226,8,4096
mqa 80,232,16
tq 228,9,2048
s_add_u32 m0, s56, 26624
mqa 88,232,20
tq 230,9,4096
buffer_load_dwordx4 v30, s[76:79], s47 offen offset:0 lds
mqa 92,236,20
tq 232,12,2048
mqa 84,236,16
tq 234,12,4096
s_waitcnt lgkmcnt(6)
mqa 64,240,24
tq 236,13,2048
mqa 72,240,28
tq 238,13,4096
buffer_load_dwordx4 v31, s[76:79], s47 offen offset:1024 lds
mqa 76,244,28
tq 240,8,6144
mqa 68,244,24
tq 242,8,8192
mqa 80,248,24
tq 244,9,6144
s_add_u32 m0, s56, 141312
mqa 88,248,28
tq 246,9,8192
buffer_load_dwordx4 v30, s[76:79], s49 offen offset:0 lds
mqa 92,252,28
tq 248,12,6144
mqa 84,252,24
tq 250,12,8192
s_waitcnt lgkmcnt(6)
s_waitcnt vmcnt(15)
mqa 64,224,48
tq 252,13,6144
tq 254,13,8192
mqa 72,224,52
s_barrier
mqa 76,228,52
tq 224,8,26624
mqa 68,228,48
tq 226,8,28672
buffer_load_dwordx4 v31, s[76:79], s49 offen offset:1024 lds
mqa 80,232,48
tq 228,9,26624
mqa 88,232,52
tq 230,9,28672
buffer_load_dwordx4 a[176:179], v15, s[28:31], s93 offen offset:0 sc1
mqa 92,236,52
tq 232,12,26624
mqa 84,236,48
tq 234,12,28672
s_waitcnt lgkmcnt(6)
mqa 64,240,56
tq 236,13,26624
mqa 72,240,60
tq 238,13,28672
buffer_load_dwordx4 a[180:183], v15, s[28:31], s93 offen offset:1024 sc1
mqa 76,244,60
tq 240,8,30720
mqa 68,244,56
tq 242,8,32768
mqa 80,248,56
tq 244,9,30720
mqa 88,248,60
tq 246,9,32768
buffer_load_dwordx4 a[184:187], v15, s[28:31], s93 offen offset:2048 sc1
mqa 92,252,60
tq 248,12,30720
mqa 84,252,56
tq 250,12,32768
buffer_load_dwordx4 a[188:191], v15, s[28:31], s93 offen offset:3072 sc1
s_waitcnt lgkmcnt(6)
s_waitcnt vmcnt(16)
mqa 64,224,80
tq 252,13,30720
mqa 72,224,84
tq 254,13,32768
mqa 76,228,84
tq 224,8,34816
mqa 68,228,80
tq 226,8,36864
buffer_load_dwordx4 a[208:211], v15, s[28:31], s95 offen offset:0 sc1
mqa 80,232,80
tq 228,9,34816
mqa 88,232,84
tq 230,9,36864
buffer_load_dwordx4 a[212:215], v15, s[28:31], s95 offen offset:1024 sc1
mqa 92,236,84
tq 232,12,34816
mqa 84,236,80
tq 234,12,36864
buffer_load_dwordx4 a[216:219], v15, s[28:31], s95 offen offset:2048 sc1
s_waitcnt lgkmcnt(6)
mqa 64,240,88
tq 236,13,34816
mqa 72,240,92
tq 238,13,36864
buffer_load_dwordx4 a[220:223], v15, s[28:31], s95 offen offset:3072 sc1
mqa 76,244,92
tq 240,8,38912
mqa 68,244,88
tq 242,8,40960
mqa 80,248,88
tq 244,9,38912
mqa 88,248,92
tq 246,9,40960
mqa 92,252,92
tq 248,12,38912
mqa 84,252,88
tq 250,12,40960
s_waitcnt lgkmcnt(6)
s_waitcnt vmcnt(16)
mqa 64,224,112
tq 252,13,38912
tq 254,13,40960
mqa 72,224,116
s_barrier
mqa 76,228,116
tq 224,8,51200
mqa 68,228,112
tq 226,8,53248
mqa 80,232,112
tq 228,9,51200
s_add_u32 m0, s56, 34816
mqa 88,232,116
tq 230,9,53248
buffer_load_dwordx4 v30, s[76:79], s51 offen offset:0 lds
mqa 92,236,116
tq 232,12,51200
mqa 84,236,112
tq 234,12,53248
s_waitcnt lgkmcnt(6)
mqa 64,240,120
tq 236,13,51200
mqa 72,240,124
tq 238,13,53248
buffer_load_dwordx4 v31, s[76:79], s51 offen offset:1024 lds
mqa 76,244,124
tq 240,8,55296
)ASM"
R"ASM(
mqa 68,244,120
tq 242,8,57344
mqa 80,248,120
tq 244,9,55296
s_add_u32 m0, s56, 124928
mqa 88,248,124
tq 246,9,57344
buffer_load_dwordx4 v30, s[76:79], s55 offen offset:0 lds
mqa 92,252,124
tq 248,12,55296
mqa 84,252,120
tq 250,12,57344
s_waitcnt lgkmcnt(6)
s_waitcnt vmcnt(15)
mqa 64,224,144
tq 252,13,55296
mqa 72,224,148
tq 254,13,57344
buffer_load_dwordx4 v31, s[76:79], s55 offen offset:1024 lds
mqa 76,228,148
tq 224,8,43008
mqa 68,228,144
tq 226,8,45056
mqa 80,232,144
tq 228,8,47104
mqa 88,232,148
tq 230,8,49152
mqa 92,236,148
tq 232,4,2048
mqa 84,236,144
tq 234,4,4096
s_waitcnt lgkmcnt(6)
mqa 64,240,152
tq 236,4,6144
mqa 72,240,156
tq 238,4,8192
mqa 76,244,156
tq 240,9,43008
mqa 68,244,152
tq 242,9,45056
mqa 80,248,152
tq 244,9,47104
mqa 88,248,156
tq 246,9,49152
mqa 92,252,156
tq 248,5,2048
mqa 84,252,152
tq 250,5,4096
s_waitcnt lgkmcnt(6)
s_waitcnt vmcnt(8)
mqa 64,224,176
tq 252,5,6144
tq 254,5,8192
mqa 72,224,180
s_barrier
mqa 72,228,188
tq 224,12,43008
mqa 64,228,184
tq 226,12,45056
s_waitcnt vmcnt(4)
mqa 64,232,208
tq 228,12,47104
mqa 72,232,212
tq 230,12,49152
mqa 72,236,220
tq 232,6,2048
mqa 64,236,216
tq 234,6,4096
s_waitcnt lgkmcnt(6)
mqa 68,240,176
tq 236,6,6144
mqa 76,240,180
tq 238,6,8192
mqa 76,244,188
tq 240,13,43008
v_fma_f32 v200, v136, v72, v200
mqa 68,244,184
tq 242,13,45056
v_fma_f32 v201, v137, v73, v201
mqa 68,248,208
tq 244,13,47104
v_fma_f32 v202, v138, v74, v202
mqa 76,248,212
tq 246,13,49152
v_fma_f32 v203, v139, v75, v203
mqa 76,252,220
tq 248,7,2048
v_fma_f32 v192, v128, v64, v192
mqa 68,252,216
tq 250,7,4096
s_waitcnt lgkmcnt(6)
mqa 80,224,176
tq 252,7,6144
v_fma_f32 v193, v129, v65, v193
mqa 88,224,180
tq 254,7,8192
v_fma_f32 v194, v130, v66, v194
mqa 88,228,188
tq 224,8,10240
v_fma_f32 v195, v131, v67, v195
mqa 80,228,184
tq 226,8,12288
v_cvt_pk_bf16_f32 v36, v200, v201
mqa 80,232,208
tq 228,8,14336
v_cvt_pk_bf16_f32 v37, v202, v203
mqa 88,232,212
tq 230,8,16384
v_cvt_pk_bf16_f32 v32, v192, v193
mqa 88,236,220
tq 232,4,18432
v_cvt_pk_bf16_f32 v33, v194, v195
mqa 80,236,216
tq 234,4,20480
s_waitcnt lgkmcnt(6)
mqa 84,240,176
tq 236,4,22528
v_fma_f32 v204, v140, v76, v204
mqa 92,240,180
tq 238,4,24576
v_fma_f32 v205, v141, v77, v205
mqa 92,244,188
tq 240,4,26624
v_fma_f32 v206, v142, v78, v206
mqa 84,244,184
tq 242,4,28672
v_fma_f32 v207, v143, v79, v207
mqa 84,248,208
tq 244,4,30720
v_fma_f32 v196, v132, v68, v196
mqa 92,248,212
tq 246,4,32768
v_fma_f32 v197, v133, v69, v197
mqa 92,252,220
tq 248,16,10240
v_fma_f32 v198, v134, v70, v198
mqa 84,252,216
tq 250,16,12288
s_waitcnt lgkmcnt(6)
mqa 96,224,16
tq 252,16,14336
tq 254,16,16384
mqa 104,224,20
s_waitcnt vmcnt(0)
s_barrier
mqa 104,228,28
tq 224,4,34816
v_fma_f32 v199, v135, v71, v199
v_cvt_pk_bf16_f32 v38, v204, v205
mqa 96,228,24
tq 226,4,36864
v_cvt_pk_bf16_f32 v39, v206, v207
mqa 96,232,48
tq 228,4,38912
v_permlane16_swap_b32 v36, v38
v_permlane16_swap_b32 v37, v39
mqa 104,232,52
tq 230,4,40960
buffer_store_dwordx4 v[36:39], v18, s[32:35], s70 offen offset:0 sc1
mqa 104,236,60
tq 232,4,43008
v_cvt_pk_bf16_f32 v34, v196, v197
v_cvt_pk_bf16_f32 v35, v198, v199
mqa 96,236,56
tq 234,4,45056
s_waitcnt lgkmcnt(6)
mqa 96,240,80
tq 236,4,47104
v_permlane16_swap_b32 v32, v34
mqa 104,240,84
tq 238,4,49152
v_permlane16_swap_b32 v33, v35
mqa 104,244,92
tq 240,8,59392
mqa 96,244,88
tq 242,8,61440
buffer_store_dwordx4 v[32:35], v18, s[32:35], 0 offen offset:0 sc1
v_fma_f32 v216, v152, v88, v216
mqa 96,248,112
tq 244,8,63488
v_fma_f32 v217, v153, v89, v217
mqa 104,248,116
tq 246,16,0
v_fma_f32 v218, v154, v90, v218
mqa 104,252,124
tq 248,9,10240
v_fma_f32 v219, v155, v91, v219
mqa 96,252,120
tq 250,9,12288
s_waitcnt lgkmcnt(6)
mqa 96,224,144
tq 252,9,14336
v_fma_f32 v208, v144, v80, v208
mqa 104,224,148
tq 254,9,16384
v_fma_f32 v209, v145, v81, v209
mqa 104,228,156
tq 224,5,18432
v_fma_f32 v210, v146, v82, v210
mqa 96,228,152
tq 226,5,20480
v_fma_f32 v211, v147, v83, v211
mqa 96,232,176
tq 228,5,22528
v_cvt_pk_bf16_f32 v44, v216, v217
mqa 104,232,180
tq 230,5,24576
v_cvt_pk_bf16_f32 v45, v218, v219
mqa 104,236,188
tq 232,5,26624
v_cvt_pk_bf16_f32 v40, v208, v209
mqa 96,236,184
tq 234,5,28672
s_waitcnt lgkmcnt(6)
mqa 96,240,208
tq 236,5,30720
v_cvt_pk_bf16_f32 v41, v210, v211
mqa 104,240,212
tq 238,5,32768
v_fma_f32 v220, v156, v92, v220
mqa 104,244,220
tq 240,17,10240
v_fma_f32 v221, v157, v93, v221
mqa 96,244,216
tq 242,17,12288
v_fma_f32 v222, v158, v94, v222
mqa 100,248,16
tq 244,17,14336
v_fma_f32 v223, v159, v95, v223
mqa 108,248,20
tq 246,17,16384
v_fma_f32 v212, v148, v84, v212
mqa 108,252,28
tq 248,5,34816
v_fma_f32 v213, v149, v85, v213
mqa 100,252,24
tq 250,5,36864
s_waitcnt lgkmcnt(6)
mqa 100,224,48
tq 252,5,38912
tq 254,5,40960
mqa 108,224,52
s_barrier
mqa 108,228,60
tq 224,5,43008
v_fma_f32 v214, v150, v86, v214
mqa 100,228,56
tq 226,5,45056
v_fma_f32 v215, v151, v87, v215
mqa 100,232,80
tq 228,5,47104
v_cvt_pk_bf16_f32 v46, v220, v221
s_nop 0
L_hop8_2_%=:
s_cbranch_execz L_ret_2_%=
mqa 108,232,84
tq 230,5,49152
v_cvt_pk_bf16_f32 v47, v222, v223
mqa 108,236,92
tq 232,9,59392
v_permlane16_swap_b32 v44, v46
mqa 100,236,88
tq 234,9,61440
s_waitcnt lgkmcnt(6)
mqa 100,240,112
tq 236,9,63488
v_permlane16_swap_b32 v45, v47
mqa 108,240,116
tq 238,17,0
buffer_store_dwordx4 v[44:47], v18, s[32:35], s70 offen offset:64 sc1
mqa 108,244,124
tq 240,12,10240
v_cvt_pk_bf16_f32 v42, v212, v213
mqa 100,244,120
tq 242,12,12288
v_cvt_pk_bf16_f32 v43, v214, v215
mqa 100,248,144
tq 244,12,14336
v_permlane16_swap_b32 v40, v42
mqa 108,248,148
tq 246,12,16384
v_permlane16_swap_b32 v41, v43
mqa 108,252,156
tq 248,6,18432
mqa 100,252,152
tq 250,6,20480
buffer_store_dwordx4 v[40:43], v18, s[32:35], 0 offen offset:64 sc1
s_waitcnt lgkmcnt(6)
mqa 100,224,176
tq 252,6,22528
v_fma_f32 v232, v168, v104, v232
mqa 108,224,180
tq 254,6,24576
v_fma_f32 v233, v169, v105, v233
mqa 108,228,188
tq 224,6,26624
v_fma_f32 v234, v170, v106, v234
mqa 100,228,184
tq 226,6,28672
v_fma_f32 v235, v171, v107, v235
mqa 100,232,208
tq 228,6,30720
v_fma_f32 v224, v160, v96, v224
mqa 108,232,212
tq 230,6,32768
v_fma_f32 v225, v161, v97, v225
mqa 108,236,220
tq 232,28,10240
v_fma_f32 v226, v162, v98, v226
mqa 100,236,216
tq 234,28,12288
s_waitcnt lgkmcnt(6)
mqa 112,240,16
tq 236,28,14336
v_fma_f32 v227, v163, v99, v227
mqa 120,240,20
tq 238,28,16384
v_cvt_pk_bf16_f32 v52, v232, v233
mqa 120,244,28
tq 240,6,34816
v_cvt_pk_bf16_f32 v53, v234, v235
mqa 112,244,24
tq 242,6,36864
v_cvt_pk_bf16_f32 v48, v224, v225
mqa 112,248,48
tq 244,6,38912
v_cvt_pk_bf16_f32 v49, v226, v227
mqa 120,248,52
tq 246,6,40960
v_fma_f32 v236, v172, v108, v236
mqa 120,252,60
tq 248,6,43008
v_fma_f32 v237, v173, v109, v237
mqa 112,252,56
tq 250,6,45056
s_waitcnt lgkmcnt(6)
mqa 112,224,80
tq 252,6,47104
tq 254,6,49152
mqa 120,224,84
s_barrier
mqa 120,228,92
tq 224,12,59392
v_fma_f32 v238, v174, v110, v238
mqa 112,228,88
tq 226,12,61440
v_fma_f32 v239, v175, v111, v239
mqa 112,232,112
tq 228,12,63488
v_fma_f32 v228, v164, v100, v228
mqa 120,232,116
tq 230,28,0
v_fma_f32 v229, v165, v101, v229
mqa 120,236,124
tq 232,13,10240
v_fma_f32 v230, v166, v102, v230
mqa 112,236,120
tq 234,13,12288
s_waitcnt lgkmcnt(6)
mqa 112,240,144
tq 236,13,14336
v_fma_f32 v231, v167, v103, v231
mqa 120,240,148
tq 238,13,16384
v_cvt_pk_bf16_f32 v54, v236, v237
mqa 120,244,156
tq 240,7,18432
v_cvt_pk_bf16_f32 v55, v238, v239
mqa 112,244,152
tq 242,7,20480
v_permlane16_swap_b32 v52, v54
mqa 112,248,176
tq 244,7,22528
v_permlane16_swap_b32 v53, v55
mqa 120,248,180
tq 246,7,24576
buffer_store_dwordx4 v[52:55], v3, s[32:35], s70 offen offset:0 sc1
mqa 120,252,188
tq 248,7,26624
v_cvt_pk_bf16_f32 v50, v228, v229
mqa 112,252,184
tq 250,7,28672
s_waitcnt lgkmcnt(6)
mqa 112,224,208
tq 252,7,30720
v_cvt_pk_bf16_f32 v51, v230, v231
mqa 120,224,212
tq 254,7,32768
v_permlane16_swap_b32 v48, v50
mqa 120,228,220
tq 224,29,10240
v_permlane16_swap_b32 v49, v51
mqa 112,228,216
tq 226,29,12288
buffer_store_dwordx4 v[48:51], v3, s[32:35], 0 offen offset:0 sc1
mqa 116,232,16
tq 228,29,14336
mqa 124,232,20
tq 230,29,16384
mqa 124,236,28
tq 232,7,34816
v_fma_f32 v248, v184, v120, v248
mqa 116,236,24
tq 234,7,36864
s_waitcnt lgkmcnt(6)
mqa 116,240,48
tq 236,7,38912
v_fma_f32 v249, v185, v121, v249
mqa 124,240,52
tq 238,7,40960
v_fma_f32 v250, v186, v122, v250
mqa 124,244,60
tq 240,7,43008
v_fma_f32 v251, v187, v123, v251
mqa 116,244,56
tq 242,7,45056
v_fma_f32 v240, v176, v112, v240
mqa 116,248,80
tq 244,7,47104
v_fma_f32 v241, v177, v113, v241
mqa 124,248,84
tq 246,7,49152
v_fma_f32 v242, v178, v114, v242
mqa 124,252,92
tq 248,13,59392
v_fma_f32 v243, v179, v115, v243
mqa 116,252,88
tq 250,13,61440
s_waitcnt lgkmcnt(6)
mqa 124,224,116
tq 252,13,63488
v_cvt_pk_bf16_f32 v60, v248, v249
mqa 124,228,124
tq 254,29,0
v_cvt_pk_bf16_f32 v61, v250, v251
mqa 124,232,148
v_cvt_pk_bf16_f32 v56, v240, v241
v_cvt_pk_bf16_f32 v57, v242, v243
mqa 124,236,156
s_waitcnt lgkmcnt(4)
mqa 124,240,180
mqa 124,244,188
s_waitcnt lgkmcnt(0)
mqa 124,248,212
mqa 124,252,220
mqa 116,224,112
mqa 116,228,120
mqa 116,232,144
s_nop 4
v_fma_f32 v252, v188, v124, v252
v_fma_f32 v253, v189, v125, v253
mqa 116,236,152
v_fma_f32 v254, v190, v126, v254
v_fma_f32 v255, v191, v127, v255
mqa 116,240,176
v_cvt_pk_bf16_f32 v62, v252, v253
v_cvt_pk_bf16_f32 v63, v254, v255
mqa 116,244,184
v_permlane16_swap_b32 v60, v62
v_permlane16_swap_b32 v61, v63
mqa 116,248,208
buffer_store_dwordx4 v[60:63], v3, s[32:35], s70 offen offset:64 sc1
mqa 116,252,216
s_nop 7
v_fma_f32 v244, v180, v116, v244
v_fma_f32 v245, v181, v117, v245
v_fma_f32 v246, v182, v118, v246
v_fma_f32 v247, v183, v119, v247
v_cvt_pk_bf16_f32 v58, v244, v245
v_cvt_pk_bf16_f32 v59, v246, v247
s_nop 0
v_permlane16_swap_b32 v56, v58
v_permlane16_swap_b32 v57, v59
buffer_store_dwordx4 v[56:59], v3, s[32:35], 0 offen offset:64 sc1
s_endpgm
L_slow0_2_%=:
s_mov_b32 s62, 0
L_loop0_2_%=:
s_and_b32 s59, vcc_lo, 0xff00
s_cbranch_scc0 L_norep0_2_%=
v_cndmask_b32 v22, v22, v23, s[60:61]
buffer_store_dword v22, v21, s[28:31], 0 offen offset:0 sc1
L_norep0_2_%=:
buffer_load_dword v23, v20, s[28:31], 0 offen offset:0 sc1
s_waitcnt vmcnt(0)
v_cmp_ne_u32 vcc, v23, v22
s_cbranch_vccz L_back0_2_%=
s_add_u32 s62, s62, 1
s_cmp_lt_u32 s62, 100000
s_cbranch_scc1 L_loop0_2_%=
s_branch L_back0_2_%=
L_slow1_2_%=:
s_mov_b32 s62, 0
L_loop1_2_%=:
s_and_b32 s59, vcc_lo, 0xff00
s_cbranch_scc0 L_norep1_2_%=
v_cndmask_b32 v22, v22, v23, s[60:61]
buffer_store_dword v22, v21, s[28:31], 0 offen offset:0 sc1
buffer_store_dword v22, v21, s[28:31], 0 offen offset:32 sc1
L_norep1_2_%=:
buffer_load_dword v23, v20, s[28:31], 0 offen offset:32 sc1
s_waitcnt vmcnt(0)
v_cmp_ne_u32 vcc, v23, v22
s_cbranch_vccz L_back1_2_%=
s_add_u32 s62, s62, 1
s_cmp_lt_u32 s62, 100000
s_cbranch_scc1 L_loop1_2_%=
s_branch L_back1_2_%=
L_slow2_2_%=:
s_mov_b32 s62, 0
L_loop2_2_%=:
s_and_b32 s59, vcc_lo, 0xff00
s_cbranch_scc0 L_norep2_2_%=
v_cndmask_b32 v22, v22, v23, s[60:61]
buffer_store_dword v22, v21, s[28:31], 0 offen offset:0 sc1
buffer_store_dword v22, v21, s[28:31], 0 offen offset:32 sc1
buffer_store_dword v22, v21, s[28:31], 0 offen offset:64 sc1
L_norep2_2_%=:
buffer_load_dword v23, v20, s[28:31], 0 offen offset:64 sc1
s_waitcnt vmcnt(0)
v_cmp_ne_u32 vcc, v23, v22
s_cbranch_vccz L_back2_2_%=
s_add_u32 s62, s62, 1
s_cmp_lt_u32 s62, 100000
s_cbranch_scc1 L_loop2_2_%=
s_branch L_back2_2_%=
L_slow3_2_%=:
s_mov_b32 s62, 0
L_loop3_2_%=:
s_and_b32 s59, vcc_lo, 0xff00
s_cbranch_scc0 L_norep3_2_%=
v_cndmask_b32 v22, v22, v23, s[60:61]
buffer_store_dword v22, v21, s[28:31], 0 offen offset:0 sc1
buffer_store_dword v22, v21, s[28:31], 0 offen offset:32 sc1
buffer_store_dword v22, v21, s[28:31], 0 offen offset:64 sc1
buffer_store_dword v22, v21, s[28:31], 0 offen offset:96 sc1
L_norep3_2_%=:
buffer_load_dword v23, v20, s[28:31], 0 offen offset:96 sc1
s_waitcnt vmcnt(0)
v_cmp_ne_u32 vcc, v23, v22
s_cbranch_vccz L_back3_2_%=
s_add_u32 s62, s62, 1
s_cmp_lt_u32 s62, 100000
s_cbranch_scc1 L_loop3_2_%=
s_branch L_back3_2_%=
.p2align 12
L_var3_%=:
L_ret_3_%=:
s_mov_b64 exec, -1
s_bfe_u32 s46, s64, 262152
s_lshl_b32 s46, s46, 17
s_xor_b32 s47, s46, 0x20000
s_add_u32 m0, s56, 26624
s_nop 0
buffer_load_dwordx4 v10, s[20:23], s46 offen offset:0 lds
buffer_load_dwordx4 v11, s[20:23], s46 offen offset:1024 lds
buffer_load_dword v25, v19, s[20:23], 0 offen
buffer_load_dword v25, v228, s[24:27], 0 offen
buffer_load_dword v229, v224, s[28:31], 0 offen sc1
s_waitcnt vmcnt(20)
v_lshlrev_b32 v231, 16, v230
v_and_b32 v23, 0xffff0000, v230
ds_write_b32 v227, v231
ds_write_b32 v227, v23 offset:4
s_waitcnt vmcnt(16)
ds_read_b64 a[0:1], v252 offset:0
ds_read_b64 a[2:3], v253 offset:0
ds_read_b64 a[4:5], v252 offset:2048
ds_read_b64 a[6:7], v253 offset:2048
ds_read_b64 a[8:9], v254 offset:0
ds_read_b64 a[10:11], v255 offset:0
ds_read_b64 a[12:13], v254 offset:2048
ds_read_b64 a[14:15], v255 offset:2048
s_waitcnt lgkmcnt(8)
s_waitcnt vmcnt(12)
s_barrier
ds_read_b128 v[64:67], v24 offset:0
ds_read_b128 v[68:71], v24 offset:64
ds_read_b128 v[72:75], v24 offset:0
ds_read_b128 v[76:79], v24 offset:64
ds_read_b128 v[80:83], v24 offset:128
ds_read_b128 v[84:87], v24 offset:192
ds_read_b128 v[88:91], v24 offset:128
ds_read_b128 v[92:95], v24 offset:192
tq 224,4,2048
tq 226,4,4096
tq 228,5,2048
tq 230,5,4096
tq 232,4,6144
tq 234,4,8192
tq 236,5,6144
tq 238,5,8192
tq 240,6,2048
tq 242,6,4096
tq 244,7,2048
tq 246,7,4096
tq 248,6,6144
tq 250,6,8192
s_nop 1
s_waitcnt lgkmcnt(6)
mqa 64,224,0
tq 252,7,6144
s_lshr_b32 s58, s46, 10
s_add_u32 m0, s57, 116736
mqa 72,224,4
tq 254,7,8192
buffer_load_dwordx4 v246, s[24:27], s58 offen offset:0 lds
mqa 76,228,4
tq 224,4,10240
ds_read_b128 v[96:99], v24 offset:256
ds_read_b128 v[100:103], v24 offset:320
ds_read_b128 v[104:107], v24 offset:256
mqa 68,228,0
tq 226,4,12288
buffer_load_dwordx4 v247, s[24:27], s58 offen offset:1024 lds
ds_read_b128 v[108:111], v24 offset:320
mqa 64,232,8
tq 228,5,10240
ds_read_b128 v[112:115], v24 offset:384
ds_read_b128 v[116:119], v24 offset:448
ds_read_b128 v[120:123], v24 offset:384
mqa 72,232,12
tq 230,5,12288
buffer_load_dwordx4 v248, s[24:27], s58 offen offset:2048 lds
ds_read_b128 v[124:127], v24 offset:448
mqa 76,236,12
tq 232,4,14336
mqa 68,236,8
tq 234,4,16384
buffer_load_dwordx4 v249, s[24:27], s58 offen offset:3072 lds
s_waitcnt lgkmcnt(14)
mqa 80,240,0
tq 236,5,14336
s_add_u32 m0, s56, 34816
mqa 88,240,4
tq 238,5,16384
buffer_load_dwordx4 v30, s[20:23], s46 offen offset:0 lds
mqa 92,244,4
tq 240,6,10240
mqa 84,244,0
tq 242,6,12288
buffer_load_dwordx4 v31, s[20:23], s46 offen offset:1024 lds
s_waitcnt vmcnt(13)
ds_read_b64 a[32:33], v252 offset:4096
ds_read_b64 a[34:35], v253 offset:4096
mqa 80,248,8
tq 244,7,10240
s_bfe_u32 s48, s64, 262156
s_lshl_b32 s48, s48, 17
s_xor_b32 s49, s48, 0x20000
s_add_u32 m0, s56, 43008
mqa 88,248,12
tq 246,7,12288
buffer_load_dwordx4 v10, s[20:23], s48 offen offset:0 lds
ds_read_b64 a[36:37], v252 offset:6144
ds_read_b64 a[38:39], v253 offset:6144
mqa 92,252,12
tq 248,6,14336
ds_read_b64 a[40:41], v254 offset:4096
ds_read_b64 a[42:43], v255 offset:4096
mqa 84,252,8
tq 250,6,16384
buffer_load_dwordx4 v11, s[20:23], s48 offen offset:1024 lds
s_waitcnt lgkmcnt(12)
mqa 96,224,0
tq 252,7,14336
tq 254,7,16384
ds_read_b64 a[44:45], v254 offset:6144
ds_read_b64 a[46:47], v255 offset:6144
mqa 104,224,4
s_lshr_b32 s58, s48, 10
s_add_u32 m0, s57, 108544
s_nop 0
buffer_load_dwordx4 v246, s[24:27], s58 offen offset:0 lds
s_waitcnt vmcnt(12)
s_barrier
mqa 108,228,4
tq 224,4,18432
mqa 100,228,0
tq 226,4,20480
buffer_load_dwordx4 v247, s[24:27], s58 offen offset:1024 lds
mqa 96,232,8
tq 228,5,18432
mqa 104,232,12
tq 230,5,20480
buffer_load_dwordx4 v248, s[24:27], s58 offen offset:2048 lds
mqa 108,236,12
tq 232,6,18432
mqa 100,236,8
tq 234,6,20480
buffer_load_dwordx4 v249, s[24:27], s58 offen offset:3072 lds
s_waitcnt lgkmcnt(8)
mqa 112,240,0
tq 236,7,18432
mqa 120,240,4
tq 238,7,20480
mqa 124,244,4
tq 240,4,22528
mqa 116,244,0
s_waitcnt lgkmcnt(9)
s_lshr_b32 s58, s40, 10
s_add_u32 m0, s57, 112640
tq 242,4,24576
buffer_load_dwordx4 v246, s[24:27], s58 offen offset:0 lds
s_waitcnt vmcnt(9)
ds_read_b64 a[64:65], v252 offset:8192
ds_read_b64 a[66:67], v253 offset:8192
mqa 112,248,8
tq 244,5,22528
ds_read_b64 a[68:69], v252 offset:10240
ds_read_b64 a[70:71], v253 offset:10240
mqa 120,248,12
tq 246,5,24576
buffer_load_dwordx4 v247, s[24:27], s58 offen offset:1024 lds
mqa 124,252,12
tq 248,6,22528
ds_read_b64 a[72:73], v254 offset:8192
ds_read_b64 a[74:75], v255 offset:8192
mqa 116,252,8
tq 250,6,24576
buffer_load_dwordx4 v248, s[24:27], s58 offen offset:2048 lds
s_waitcnt lgkmcnt(12)
mqa 64,224,32
tq 252,7,22528
ds_read_b64 a[76:77], v254 offset:10240
ds_read_b64 a[78:79], v255 offset:10240
mqa 72,224,36
tq 254,7,24576
buffer_load_dwordx4 v249, s[24:27], s58 offen offset:3072 lds
mqa 76,228,36
tq 224,4,26624
s_add_u32 m0, s56, 51200
mqa 68,228,32
tq 226,4,28672
buffer_load_dwordx4 v10, s[20:23], s40 offen offset:0 lds
mqa 80,232,32
tq 228,5,26624
mqa 88,232,36
tq 230,5,28672
buffer_load_dwordx4 v11, s[20:23], s40 offen offset:1024 lds
mqa 92,236,36
tq 232,6,26624
s_add_u32 m0, s56, 59392
mqa 84,236,32
tq 234,6,28672
buffer_load_dwordx4 v30, s[20:23], s40 offen offset:0 lds
s_waitcnt lgkmcnt(6)
mqa 64,240,40
tq 236,7,26624
mqa 72,240,44
tq 238,7,28672
buffer_load_dwordx4 v31, s[20:23], s40 offen offset:1024 lds
mqa 76,244,44
tq 240,4,30720
mqa 68,244,40
tq 242,4,32768
mqa 80,248,40
tq 244,5,30720
mqa 88,248,44
tq 246,5,32768
mqa 92,252,44
tq 248,6,30720
mqa 84,252,40
tq 250,6,32768
s_waitcnt lgkmcnt(6)
mqa 64,224,64
tq 252,7,30720
tq 254,7,32768
mqa 72,224,68
v_readlane_b32 s63, v229, 32
L_nonce_3_%=:
s_add_u32 s63, s63, 1
v_cmp_eq_u32 vcc, s63, v229
s_cbranch_vccnz L_nonce_3_%=
v_mov_b32 v230, s63
s_mov_b32 exec_lo, 0
s_mov_b32 exec_hi, 15
buffer_store_dword v230, v224, s[28:31], 0 offen sc1
s_mov_b64 exec, -1
v_and_b32 v22, v22, v230
s_waitcnt vmcnt(13)
s_barrier
mqa 76,228,68
tq 224,4,34816
mqa 68,228,64
tq 226,4,36864
mqa 80,232,64
tq 228,5,34816
mqa 88,232,68
tq 230,5,36864
mqa 92,236,68
tq 232,6,34816
mqa 84,236,64
tq 234,6,36864
s_waitcnt lgkmcnt(6)
mqa 64,240,72
tq 236,7,34816
mqa 72,240,76
tq 238,7,36864
mqa 76,244,76
tq 240,4,38912
s_bfe_u32 s50, s64, 262160
s_lshl_b32 s50, s50, 17
s_xor_b32 s51, s50, 0x20000
mqa 68,244,72
s_lshr_b32 s58, s50, 10
s_add_u32 m0, s57, 116736
tq 242,4,40960
buffer_load_dwordx4 v246, s[24:27], s58 offen offset:0 lds
s_waitcnt vmcnt(10)
ds_read_b64 a[96:97], v252 offset:0
ds_read_b64 a[98:99], v253 offset:0
mqa 80,248,72
tq 244,5,38912
ds_read_b64 a[100:101], v252 offset:2048
ds_read_b64 a[102:103], v253 offset:2048
mqa 88,248,76
tq 246,5,40960
buffer_load_dwordx4 v247, s[24:27], s58 offen offset:1024 lds
mqa 92,252,76
tq 248,6,38912
ds_read_b64 a[104:105], v254 offset:0
ds_read_b64 a[106:107], v255 offset:0
mqa 84,252,72
tq 250,6,40960
buffer_load_dwordx4 v248, s[24:27], s58 offen offset:2048 lds
s_waitcnt lgkmcnt(12)
mqa 96,224,64
tq 252,7,38912
ds_read_b64 a[108:109], v254 offset:2048
ds_read_b64 a[110:111], v255 offset:2048
mqa 104,224,68
tq 254,7,40960
buffer_load_dwordx4 v249, s[24:27], s58 offen offset:3072 lds
mqa 108,228,68
tq 224,4,43008
s_add_u32 m0, s56, 67584
mqa 100,228,64
tq 226,4,45056
buffer_load_dwordx4 v10, s[20:23], s50 offen offset:0 lds
mqa 112,232,64
tq 228,5,43008
mqa 120,232,68
tq 230,5,45056
buffer_load_dwordx4 v11, s[20:23], s50 offen offset:1024 lds
mqa 124,236,68
tq 232,6,43008
s_bfe_u32 s52, s64, 262164
s_lshl_b32 s52, s52, 17
s_xor_b32 s53, s52, 0x20000
mqa 116,236,64
s_add_u32 m0, s56, 75776
tq 234,6,45056
buffer_load_dwordx4 v10, s[20:23], s52 offen offset:0 lds
s_waitcnt lgkmcnt(6)
mqa 96,240,72
tq 236,7,43008
mqa 104,240,76
tq 238,7,45056
buffer_load_dwordx4 v11, s[20:23], s52 offen offset:1024 lds
mqa 108,244,76
tq 240,4,47104
mqa 100,244,72
s_lshr_b32 s58, s52, 10
s_add_u32 m0, s57, 108544
tq 242,4,49152
buffer_load_dwordx4 v246, s[24:27], s58 offen offset:0 lds
s_waitcnt vmcnt(14)
ds_read_b64 v[32:33], v252 offset:4096
ds_read_b64 v[34:35], v253 offset:4096
mqa 112,248,72
tq 244,5,47104
ds_read_b64 v[36:37], v252 offset:6144
ds_read_b64 v[38:39], v253 offset:6144
mqa 120,248,76
tq 246,5,49152
buffer_load_dwordx4 v247, s[24:27], s58 offen offset:1024 lds
mqa 124,252,76
tq 248,6,47104
ds_read_b64 v[40:41], v254 offset:4096
ds_read_b64 v[42:43], v255 offset:4096
mqa 116,252,72
tq 250,6,49152
buffer_load_dwordx4 v248, s[24:27], s58 offen offset:2048 lds
s_waitcnt lgkmcnt(12)
mqa 64,224,96
tq 252,7,47104
tq 254,7,49152
ds_read_b64 v[44:45], v254 offset:6144
ds_read_b64 v[46:47], v255 offset:6144
mqa 72,224,100
buffer_load_dwordx4 v249, s[24:27], s58 offen offset:3072 lds
s_waitcnt vmcnt(13)
s_barrier
mqa 76,228,100
tq 224,4,51200
mqa 68,228,96
tq 226,4,53248
mqa 80,232,96
tq 228,5,51200
mqa 88,232,100
tq 230,5,53248
mqa 92,236,100
tq 232,6,51200
mqa 84,236,96
tq 234,6,53248
s_waitcnt lgkmcnt(8)
mqa 64,240,104
tq 236,7,51200
mqa 72,240,108
tq 238,7,53248
mqa 76,244,108
tq 240,4,55296
s_bfe_u32 s54, s64, 262168
s_lshl_b32 s54, s54, 17
s_xor_b32 s55, s54, 0x20000
mqa 68,244,104
s_waitcnt lgkmcnt(9)
s_lshr_b32 s58, s54, 10
s_add_u32 m0, s57, 112640
tq 242,4,57344
buffer_load_dwordx4 v246, s[24:27], s58 offen offset:0 lds
mqa 80,248,104
tq 244,5,55296
mqa 88,248,108
tq 246,5,57344
buffer_load_dwordx4 v247, s[24:27], s58 offen offset:1024 lds
mqa 92,252,108
tq 248,6,55296
mqa 84,252,104
tq 250,6,57344
buffer_load_dwordx4 v248, s[24:27], s58 offen offset:2048 lds
s_waitcnt lgkmcnt(6)
mqv 64,224,32
tq 252,7,55296
mqv 72,224,36
tq 254,7,57344
buffer_load_dwordx4 v249, s[24:27], s58 offen offset:3072 lds
mqv 76,228,36
tq 224,4,59392
s_add_u32 m0, s56, 83968
mqv 68,228,32
tq 226,4,61440
buffer_load_dwordx4 v10, s[20:23], s54 offen offset:0 lds
mqv 80,232,32
tq 228,5,59392
v_lshlrev_b32 v128, 16, v32
mqv 88,232,36
tq 230,5,61440
buffer_load_dwordx4 v11, s[20:23], s54 offen offset:1024 lds
mqv 92,236,36
tq 232,6,59392
s_add_u32 m0, s56, 92160
mqv 84,236,32
tq 234,6,61440
buffer_load_dwordx4 v10, s[20:23], s41 offen offset:0 lds
s_waitcnt lgkmcnt(6)
mqv 64,240,40
tq 236,7,59392
v_and_b32 v129, 0xffff0000, v32
mqv 72,240,44
tq 238,7,61440
buffer_load_dwordx4 v11, s[20:23], s41 offen offset:1024 lds
mqv 76,244,44
tq 240,4,63488
mqv 68,244,40
tq 242,8,0
s_waitcnt vmcnt(16)
ds_read_b64 a[128:129], v252 offset:8192
ds_read_b64 a[130:131], v253 offset:8192
mqv 80,248,40
tq 244,5,63488
ds_read_b64 a[132:133], v252 offset:10240
mqv 88,248,44
tq 246,9,0
ds_read_b64 a[134:135], v253 offset:10240
mqv 92,252,44
tq 248,6,63488
ds_read_b64 a[136:137], v254 offset:8192
ds_read_b64 a[138:139], v255 offset:8192
mqv 84,252,40
tq 250,12,0
s_waitcnt lgkmcnt(12)
mqv 96,224,32
tq 252,7,63488
tq 254,13,0
ds_read_b64 a[140:141], v254 offset:10240
ds_read_b64 a[142:143], v255 offset:10240
mqv 104,224,36
s_waitcnt vmcnt(12)
s_barrier
mqv 108,228,36
tq 224,8,2048
v_lshlrev_b32 v130, 16, v33
mqv 100,228,32
tq 226,8,4096
v_and_b32 v131, 0xffff0000, v33
mqv 112,232,32
tq 228,9,2048
v_lshlrev_b32 v132, 16, v34
mqv 120,232,36
tq 230,9,4096
v_and_b32 v133, 0xffff0000, v34
mqv 124,236,36
tq 232,12,2048
mqv 116,236,32
s_waitcnt lgkmcnt(5)
s_lshr_b32 s58, s41, 10
s_add_u32 m0, s57, 116736
tq 234,12,4096
buffer_load_dwordx4 v246, s[24:27], s58 offen offset:0 lds
mqv 96,240,40
tq 236,13,2048
v_lshlrev_b32 v134, 16, v35
mqv 104,240,44
tq 238,13,4096
buffer_load_dwordx4 v247, s[24:27], s58 offen offset:1024 lds
mqv 108,244,44
tq 240,8,6144
mqv 100,244,40
tq 242,8,8192
buffer_load_dwordx4 v248, s[24:27], s58 offen offset:2048 lds
s_waitcnt vmcnt(11)
ds_read_b64 a[160:161], v252 offset:0
ds_read_b64 a[162:163], v253 offset:0
mqv 112,248,40
tq 244,9,6144
ds_read_b64 a[164:165], v252 offset:2048
ds_read_b64 a[166:167], v253 offset:2048
mqv 120,248,44
tq 246,9,8192
buffer_load_dwordx4 v249, s[24:27], s58 offen offset:3072 lds
mqv 124,252,44
tq 248,12,6144
ds_read_b64 a[168:169], v254 offset:0
ds_read_b64 a[170:171], v255 offset:0
mqv 116,252,40
tq 250,12,8192
s_waitcnt lgkmcnt(12)
mqa 64,224,128
tq 252,13,6144
s_add_u32 m0, s56, 100352
ds_read_b64 a[172:173], v254 offset:2048
mqa 72,224,132
tq 254,13,8192
buffer_load_dwordx4 v30, s[20:23], s41 offen offset:0 lds
ds_read_b64 a[174:175], v255 offset:2048
mqa 76,228,132
tq 224,8,10240
v_and_b32 v135, 0xffff0000, v35
mqa 68,228,128
tq 226,8,12288
buffer_load_dwordx4 v31, s[20:23], s41 offen offset:1024 lds
mqa 80,232,128
tq 228,9,10240
s_add_u32 m0, s56, 2048
mqa 88,232,132
tq 230,9,12288
buffer_load_dwordx4 v10, s[20:23], s43 offen offset:0 lds
mqa 92,236,132
tq 232,12,10240
mqa 84,236,128
tq 234,12,12288
buffer_load_dwordx4 v11, s[20:23], s43 offen offset:1024 lds
s_waitcnt lgkmcnt(7)
mqa 64,240,136
tq 236,13,10240
v_lshlrev_b32 v136, 16, v36
mqa 72,240,140
tq 238,13,12288
v_and_b32 v137, 0xffff0000, v36
mqa 76,244,140
tq 240,8,14336
mqa 68,244,136
s_waitcnt lgkmcnt(9)
s_lshr_b32 s58, s43, 10
s_add_u32 m0, s57, 108544
tq 242,8,16384
buffer_load_dwordx4 v246, s[24:27], s58 offen offset:0 lds
s_waitcnt vmcnt(13)
ds_read_b64 a[192:193], v252 offset:4096
ds_read_b64 a[194:195], v253 offset:4096
mqa 80,248,136
tq 244,9,14336
ds_read_b64 a[196:197], v252 offset:6144
ds_read_b64 a[198:199], v253 offset:6144
mqa 88,248,140
tq 246,9,16384
buffer_load_dwordx4 v247, s[24:27], s58 offen offset:1024 lds
mqa 92,252,140
tq 248,12,14336
ds_read_b64 a[200:201], v254 offset:4096
ds_read_b64 a[202:203], v255 offset:4096
mqa 84,252,136
tq 250,12,16384
buffer_load_dwordx4 v248, s[24:27], s58 offen offset:2048 lds
s_waitcnt lgkmcnt(12)
s_nop 0
L_hop1_3_%=:
s_cbranch_execz L_hop5_3_%=
mqa 64,224,160
tq 252,13,14336
tq 254,13,16384
ds_read_b64 a[204:205], v254 offset:6144
ds_read_b64 a[206:207], v255 offset:6144
mqa 72,224,164
buffer_load_dwordx4 v249, s[24:27], s58 offen offset:3072 lds
s_waitcnt vmcnt(12)
s_barrier
mqa 76,228,164
tq 224,8,18432
v_lshlrev_b32 v138, 16, v37
mqa 68,228,160
tq 226,8,20480
v_and_b32 v139, 0xffff0000, v37
mqa 80,232,160
tq 228,9,18432
v_lshlrev_b32 v140, 16, v38
mqa 88,232,164
tq 230,9,20480
v_and_b32 v141, 0xffff0000, v38
mqa 92,236,164
tq 232,12,18432
v_lshlrev_b32 v142, 16, v39
mqa 84,236,160
tq 234,12,20480
s_waitcnt lgkmcnt(8)
mqa 64,240,168
tq 236,13,18432
v_and_b32 v143, 0xffff0000, v39
mqa 72,240,172
tq 238,13,20480
v_lshlrev_b32 v144, 16, v40
mqa 76,244,172
tq 240,8,22528
mqa 68,244,168
s_waitcnt lgkmcnt(9)
s_lshr_b32 s58, s45, 10
s_add_u32 m0, s57, 112640
tq 242,8,24576
buffer_load_dwordx4 v246, s[24:27], s58 offen offset:0 lds
s_waitcnt vmcnt(9)
ds_read_b64 v[48:49], v252 offset:8192
ds_read_b64 v[50:51], v253 offset:8192
mqa 80,248,168
tq 244,9,22528
ds_read_b64 v[52:53], v252 offset:10240
ds_read_b64 v[54:55], v253 offset:10240
mqa 88,248,172
tq 246,9,24576
buffer_load_dwordx4 v247, s[24:27], s58 offen offset:1024 lds
mqa 92,252,172
tq 248,12,22528
ds_read_b64 v[56:57], v254 offset:8192
ds_read_b64 v[58:59], v255 offset:8192
mqa 84,252,168
tq 250,12,24576
buffer_load_dwordx4 v248, s[24:27], s58 offen offset:2048 lds
s_waitcnt lgkmcnt(12)
mqa 64,224,192
tq 252,13,22528
ds_read_b64 v[60:61], v254 offset:10240
ds_read_b64 v[62:63], v255 offset:10240
mqa 72,224,196
tq 254,13,24576
buffer_load_dwordx4 v249, s[24:27], s58 offen offset:3072 lds
mqa 76,228,196
tq 224,8,26624
s_add_u32 m0, s56, 10240
mqa 68,228,192
tq 226,8,28672
buffer_load_dwordx4 v10, s[20:23], s45 offen offset:0 lds
mqa 80,232,192
tq 228,9,26624
v_and_b32 v145, 0xffff0000, v40
mqa 88,232,196
tq 230,9,28672
buffer_load_dwordx4 v11, s[20:23], s45 offen offset:1024 lds
mqa 92,236,196
tq 232,12,26624
s_add_u32 m0, s56, 18432
mqa 84,236,192
tq 234,12,28672
buffer_load_dwordx4 v10, s[20:23], s47 offen offset:0 lds
s_waitcnt lgkmcnt(6)
mqa 64,240,200
tq 236,13,26624
v_lshlrev_b32 v146, 16, v41
mqa 72,240,204
tq 238,13,28672
buffer_load_dwordx4 v11, s[20:23], s47 offen offset:1024 lds
mqa 76,244,204
tq 240,8,30720
mqa 68,244,200
s_lshr_b32 s58, s47, 10
s_add_u32 m0, s57, 116736
tq 242,8,32768
buffer_load_dwordx4 v246, s[24:27], s58 offen offset:0 lds
mqa 80,248,200
tq 244,9,30720
v_and_b32 v147, 0xffff0000, v41
mqa 88,248,204
tq 246,9,32768
buffer_load_dwordx4 v247, s[24:27], s58 offen offset:1024 lds
mqa 92,252,204
tq 248,12,30720
mqa 84,252,200
tq 250,12,32768
buffer_load_dwordx4 v248, s[24:27], s58 offen offset:2048 lds
s_waitcnt lgkmcnt(6)
mqv 64,224,48
tq 252,13,30720
tq 254,13,32768
mqv 72,224,52
buffer_load_dwordx4 v249, s[24:27], s58 offen offset:3072 lds
s_waitcnt vmcnt(16)
s_barrier
mqv 76,228,52
tq 224,8,34816
v_lshlrev_b32 v148, 16, v42
mqv 68,228,48
tq 226,8,36864
v_and_b32 v149, 0xffff0000, v42
mqv 80,232,48
tq 228,9,34816
v_lshlrev_b32 v150, 16, v43
mqv 88,232,52
tq 230,9,36864
v_and_b32 v151, 0xffff0000, v43
mqv 92,236,52
tq 232,12,34816
v_lshlrev_b32 v152, 16, v44
mqv 84,236,48
tq 234,12,36864
s_waitcnt lgkmcnt(6)
mqv 64,240,56
tq 236,13,34816
v_and_b32 v153, 0xffff0000, v44
mqv 72,240,60
tq 238,13,36864
v_lshlrev_b32 v154, 16, v45
mqv 76,244,60
tq 240,8,38912
mqv 68,244,56
tq 242,8,40960
s_waitcnt vmcnt(12)
ds_read_b64 a[16:17], v252 offset:0
ds_read_b64 a[18:19], v253 offset:0
mqv 80,248,56
tq 244,9,38912
ds_read_b64 a[20:21], v252 offset:2048
mqv 88,248,60
tq 246,9,40960
ds_read_b64 a[22:23], v253 offset:2048
mqv 92,252,60
tq 248,12,38912
ds_read_b64 a[24:25], v254 offset:0
ds_read_b64 a[26:27], v255 offset:0
mqv 84,252,56
tq 250,12,40960
s_waitcnt lgkmcnt(12)
mqv 96,224,48
tq 252,13,38912
ds_read_b64 a[28:29], v254 offset:2048
ds_read_b64 a[30:31], v255 offset:2048
mqv 104,224,52
tq 254,13,40960
buffer_load_dword v23, v20, s[28:31], 0 offen sc1
s_add_u32 m0, s56, 26624
s_nop 0
buffer_load_dwordx4 v10, s[20:23], s49 offen offset:0 lds
mqv 108,228,52
tq 224,4,2048
v_and_b32 v155, 0xffff0000, v45
mqv 100,228,48
tq 226,4,4096
buffer_load_dwordx4 v11, s[20:23], s49 offen offset:1024 lds
mqv 112,232,48
tq 228,5,2048
s_add_u32 m0, s56, 34816
mqv 120,232,52
tq 230,5,4096
buffer_load_dwordx4 v10, s[20:23], s51 offen offset:0 lds
mqv 124,236,52
tq 232,6,2048
mqv 116,236,48
tq 234,6,4096
buffer_load_dwordx4 v11, s[20:23], s51 offen offset:1024 lds
s_waitcnt lgkmcnt(6)
mqv 96,240,56
tq 236,7,2048
mqv 104,240,60
s_lshr_b32 s58, s49, 10
s_add_u32 m0, s57, 108544
tq 238,7,4096
buffer_load_dwordx4 v246, s[24:27], s58 offen offset:0 lds
mqv 108,244,60
tq 240,4,6144
mqv 100,244,56
tq 242,4,8192
buffer_load_dwordx4 v247, s[24:27], s58 offen offset:1024 lds
s_waitcnt vmcnt(15)
ds_read_b64 a[48:49], v252 offset:4096
ds_read_b64 a[50:51], v253 offset:4096
mqv 112,248,56
tq 244,5,6144
ds_read_b64 a[52:53], v252 offset:6144
ds_read_b64 a[54:55], v253 offset:6144
mqv 120,248,60
tq 246,5,8192
buffer_load_dwordx4 v248, s[24:27], s58 offen offset:2048 lds
mqv 124,252,60
tq 248,6,6144
ds_read_b64 a[56:57], v254 offset:4096
ds_read_b64 a[58:59], v255 offset:4096
mqv 116,252,56
tq 250,6,8192
buffer_load_dwordx4 v249, s[24:27], s58 offen offset:3072 lds
s_waitcnt lgkmcnt(12)
mqa 64,224,16
tq 252,7,6144
tq 254,7,8192
ds_read_b64 a[60:61], v254 offset:6144
ds_read_b64 a[62:63], v255 offset:6144
mqa 72,224,20
s_waitcnt vmcnt(13)
s_barrier
mqa 76,228,20
tq 224,4,10240
v_lshlrev_b32 v156, 16, v46
mqa 68,228,16
tq 226,4,12288
v_and_b32 v157, 0xffff0000, v46
mqa 80,232,16
tq 228,5,10240
v_lshlrev_b32 v158, 16, v47
mqa 88,232,20
tq 230,5,12288
v_and_b32 v159, 0xffff0000, v47
mqa 92,236,20
tq 232,6,10240
mqa 84,236,16
s_waitcnt lgkmcnt(5)
s_lshr_b32 s58, s51, 10
s_add_u32 m0, s57, 112640
tq 234,6,12288
buffer_load_dwordx4 v246, s[24:27], s58 offen offset:0 lds
mqa 64,240,24
tq 236,7,10240
v_lshlrev_b32 v160, 16, v48
mqa 72,240,28
tq 238,7,12288
buffer_load_dwordx4 v247, s[24:27], s58 offen offset:1024 lds
mqa 76,244,28
tq 240,4,14336
mqa 68,244,24
tq 242,4,16384
buffer_load_dwordx4 v248, s[24:27], s58 offen offset:2048 lds
s_waitcnt vmcnt(12)
ds_read_b64 a[80:81], v252 offset:8192
ds_read_b64 a[82:83], v253 offset:8192
mqa 80,248,24
tq 244,5,14336
ds_read_b64 a[84:85], v252 offset:10240
ds_read_b64 a[86:87], v253 offset:10240
mqa 88,248,28
tq 246,5,16384
buffer_load_dwordx4 v249, s[24:27], s58 offen offset:3072 lds
mqa 92,252,28
tq 248,6,14336
ds_read_b64 a[88:89], v254 offset:8192
ds_read_b64 a[90:91], v255 offset:8192
mqa 84,252,24
tq 250,6,16384
s_waitcnt lgkmcnt(12)
mqa 64,224,48
tq 252,7,14336
s_add_u32 m0, s56, 43008
ds_read_b64 a[92:93], v254 offset:10240
mqa 72,224,52
tq 254,7,16384
buffer_load_dwordx4 v10, s[20:23], s53 offen offset:0 lds
ds_read_b64 a[94:95], v255 offset:10240
mqa 76,228,52
tq 224,4,18432
v_and_b32 v161, 0xffff0000, v48
mqa 68,228,48
tq 226,4,20480
buffer_load_dwordx4 v11, s[20:23], s53 offen offset:1024 lds
mqa 80,232,48
tq 228,5,18432
s_add_u32 m0, s56, 51200
mqa 88,232,52
tq 230,5,20480
buffer_load_dwordx4 v10, s[20:23], s55 offen offset:0 lds
mqa 92,236,52
tq 232,6,18432
mqa 84,236,48
tq 234,6,20480
buffer_load_dwordx4 v11, s[20:23], s55 offen offset:1024 lds
s_waitcnt lgkmcnt(7)
mqa 64,240,56
tq 236,7,18432
mqa 72,240,60
s_waitcnt lgkmcnt(7)
s_lshr_b32 s58, s53, 10
s_add_u32 m0, s57, 116736
tq 238,7,20480
buffer_load_dwordx4 v246, s[24:27], s58 offen offset:0 lds
mqa 76,244,60
tq 240,4,22528
mqa 68,244,56
tq 242,4,24576
buffer_load_dwordx4 v247, s[24:27], s58 offen offset:1024 lds
s_waitcnt vmcnt(10)
ds_read_b64 a[112:113], v252 offset:0
ds_read_b64 a[114:115], v253 offset:0
mqa 80,248,56
tq 244,5,22528
ds_read_b64 a[116:117], v252 offset:2048
ds_read_b64 a[118:119], v253 offset:2048
mqa 88,248,60
tq 246,5,24576
buffer_load_dwordx4 v248, s[24:27], s58 offen offset:2048 lds
mqa 92,252,60
tq 248,6,22528
ds_read_b64 a[120:121], v254 offset:0
ds_read_b64 a[122:123], v255 offset:0
mqa 84,252,56
tq 250,6,24576
buffer_load_dwordx4 v249, s[24:27], s58 offen offset:3072 lds
s_waitcnt lgkmcnt(12)
mqa 64,224,80
tq 252,7,22528
tq 254,7,24576
ds_read_b64 a[124:125], v254 offset:2048
ds_read_b64 a[126:127], v255 offset:2048
mqa 72,224,84
s_barrier
mqa 76,228,84
tq 224,4,26624
v_lshlrev_b32 v162, 16, v49
mqa 68,228,80
tq 226,4,28672
v_and_b32 v163, 0xffff0000, v49
mqa 80,232,80
tq 228,5,26624
v_lshlrev_b32 v164, 16, v50
mqa 88,232,84
tq 230,5,28672
v_and_b32 v165, 0xffff0000, v50
mqa 92,236,84
tq 232,6,26624
mqa 84,236,80
s_waitcnt lgkmcnt(5)
s_lshr_b32 s58, s55, 10
s_add_u32 m0, s57, 108544
tq 234,6,28672
buffer_load_dwordx4 v246, s[24:27], s58 offen offset:0 lds
mqa 64,240,88
tq 236,7,26624
v_lshlrev_b32 v166, 16, v51
mqa 72,240,92
tq 238,7,28672
buffer_load_dwordx4 v247, s[24:27], s58 offen offset:1024 lds
mqa 76,244,92
tq 240,4,30720
mqa 68,244,88
tq 242,4,32768
buffer_load_dwordx4 v248, s[24:27], s58 offen offset:2048 lds
s_waitcnt vmcnt(11)
ds_read_b64 a[144:145], v252 offset:4096
ds_read_b64 a[146:147], v253 offset:4096
mqa 80,248,88
tq 244,5,30720
ds_read_b64 a[148:149], v252 offset:6144
ds_read_b64 a[150:151], v253 offset:6144
mqa 88,248,92
tq 246,5,32768
buffer_load_dwordx4 v249, s[24:27], s58 offen offset:3072 lds
mqa 92,252,92
tq 248,6,30720
ds_read_b64 a[152:153], v254 offset:4096
ds_read_b64 a[154:155], v255 offset:4096
mqa 84,252,88
tq 250,6,32768
buffer_load_dword v25, v19, s[72:75], 0 offen
s_waitcnt lgkmcnt(12)
mqa 64,224,112
tq 252,7,30720
s_add_u32 m0, s56, 59392
ds_read_b64 a[156:157], v254 offset:6144
mqa 72,224,116
tq 254,7,32768
buffer_load_dwordx4 v30, s[20:23], s44 offen offset:0 lds
ds_read_b64 a[158:159], v255 offset:6144
mqa 76,228,116
tq 224,4,34816
v_and_b32 v167, 0xffff0000, v51
mqa 68,228,112
)ASM"
R"ASM(
tq 226,4,36864
buffer_load_dwordx4 v31, s[20:23], s44 offen offset:1024 lds
mqa 80,232,112
tq 228,5,34816
s_add_u32 m0, s56, 67584
mqa 88,232,116
tq 230,5,36864
buffer_load_dwordx4 v30, s[20:23], s48 offen offset:0 lds
mqa 92,236,116
tq 232,6,34816
mqa 84,236,112
tq 234,6,36864
buffer_load_dwordx4 v31, s[20:23], s48 offen offset:1024 lds
s_waitcnt vmcnt(9)
ds_read_b64 a[176:177], v252 offset:8192
ds_read_b64 a[178:179], v253 offset:8192
s_waitcnt lgkmcnt(9)
mqa 64,240,120
tq 236,7,34816
ds_read_b64 a[180:181], v252 offset:10240
mqa 72,240,124
tq 238,7,36864
ds_read_b64 a[182:183], v253 offset:10240
mqa 76,244,124
tq 240,4,38912
ds_read_b64 a[184:185], v254 offset:8192
mqa 68,244,120
tq 242,4,40960
ds_read_b64 a[186:187], v255 offset:8192
mqa 80,248,120
tq 244,5,38912
ds_read_b64 a[188:189], v254 offset:10240
mqa 88,248,124
tq 246,5,40960
ds_read_b64 a[190:191], v255 offset:10240
mqa 92,252,124
tq 248,6,38912
mqa 84,252,120
tq 250,6,40960
s_waitcnt vmcnt(5)
ds_read_b64 a[208:209], v252 offset:0
ds_read_b64 a[210:211], v253 offset:0
s_waitcnt lgkmcnt(13)
mqa 64,224,144
tq 252,7,38912
tq 254,7,40960
ds_read_b64 a[212:213], v252 offset:2048
ds_read_b64 a[214:215], v253 offset:2048
mqa 72,224,148
s_barrier
mqa 76,228,148
tq 224,4,43008
ds_read_b64 a[216:217], v254 offset:0
mqa 68,228,144
tq 226,4,45056
ds_read_b64 a[218:219], v255 offset:0
mqa 80,232,144
tq 228,4,47104
ds_read_b64 a[220:221], v254 offset:2048
mqa 88,232,148
tq 230,4,49152
ds_read_b64 a[222:223], v255 offset:2048
mqa 92,236,148
tq 232,4,51200
s_add_u32 m0, s56, 75776
mqa 84,236,144
tq 234,4,53248
buffer_load_dwordx4 v30, s[20:23], s52 offen offset:0 lds
s_waitcnt lgkmcnt(12)
mqa 64,240,152
tq 236,4,55296
v_lshlrev_b32 v168, 16, v52
mqa 72,240,156
tq 238,4,57344
buffer_load_dwordx4 v31, s[20:23], s52 offen offset:1024 lds
mqa 76,244,156
tq 240,5,43008
s_add_u32 m0, s56, 83968
mqa 68,244,152
tq 242,5,45056
buffer_load_dwordx4 v30, s[20:23], s47 offen offset:0 lds
mqa 80,248,152
tq 244,5,47104
v_and_b32 v169, 0xffff0000, v52
mqa 88,248,156
tq 246,5,49152
v_lshlrev_b32 v170, 16, v53
mqa 92,252,156
tq 248,5,51200
mqa 84,252,152
tq 250,5,53248
buffer_load_dwordx4 v31, s[20:23], s47 offen offset:1024 lds
s_waitcnt lgkmcnt(6)
mqa 64,224,176
tq 252,5,55296
s_add_u32 m0, s56, 92160
mqa 72,224,180
tq 254,5,57344
buffer_load_dwordx4 v30, s[20:23], s50 offen offset:0 lds
mqa 72,228,188
tq 224,6,43008
mqa 64,228,184
tq 226,6,45056
buffer_load_dwordx4 v31, s[20:23], s50 offen offset:1024 lds
mqa 64,232,208
tq 228,6,47104
v_and_b32 v171, 0xffff0000, v53
mqa 72,232,212
tq 230,6,49152
v_lshlrev_b32 v172, 16, v54
mqa 72,236,220
tq 232,6,51200
s_add_u32 m0, s56, 100352
mqa 64,236,216
tq 234,6,53248
buffer_load_dwordx4 v30, s[20:23], s54 offen offset:0 lds
s_waitcnt lgkmcnt(6)
mqa 68,240,176
tq 236,6,55296
v_and_b32 v173, 0xffff0000, v54
mqa 76,240,180
tq 238,6,57344
v_lshlrev_b32 v174, 16, v55
mqa 76,244,188
tq 240,7,43008
v_fma_f32 v200, v136, v72, v136
v_fma_f32 v201, v137, v73, v137
mqa 68,244,184
tq 242,7,45056
v_fma_f32 v202, v138, v74, v138
mqa 68,248,208
tq 244,7,47104
v_fma_f32 v203, v139, v75, v139
v_fma_f32 v192, v128, v64, v128
mqa 76,248,212
tq 246,7,49152
v_fma_f32 v193, v129, v65, v129
mqa 76,252,220
tq 248,7,51200
v_fma_f32 v194, v130, v66, v130
mqa 68,252,216
tq 250,7,53248
buffer_load_dwordx4 v31, s[20:23], s54 offen offset:1024 lds
L_hop2_3_%=:
s_cbranch_execz L_hop6_3_%=
s_waitcnt lgkmcnt(6)
mqa 80,224,176
tq 252,7,55296
tq 254,7,57344
v_fma_f32 v195, v131, v67, v131
mqa 88,224,180
s_waitcnt vmcnt(8)
s_barrier
mqa 88,228,188
tq 224,4,59392
s_add_u32 m0, s56, 2048
v_cvt_pk_bf16_f32 v36, v200, v201
mqa 80,228,184
tq 226,4,61440
buffer_load_dwordx4 v30, s[20:23], s43 offen offset:0 lds
v_cvt_pk_bf16_f32 v37, v202, v203
mqa 80,232,208
tq 228,5,59392
v_cvt_pk_bf16_f32 v32, v192, v193
v_cvt_pk_bf16_f32 v33, v194, v195
mqa 88,232,212
tq 230,5,61440
v_fma_f32 v204, v140, v76, v140
v_fma_f32 v205, v141, v77, v141
mqa 88,236,220
tq 232,6,59392
v_fma_f32 v206, v142, v78, v142
v_fma_f32 v207, v143, v79, v143
mqa 80,236,216
tq 234,6,61440
buffer_load_dwordx4 v31, s[20:23], s43 offen offset:1024 lds
s_waitcnt lgkmcnt(6)
mqa 84,240,176
tq 236,7,59392
v_fma_f32 v196, v132, v68, v132
v_fma_f32 v197, v133, v69, v133
v_fma_f32 v198, v134, v70, v134
mqa 92,240,180
tq 238,7,61440
v_fma_f32 v199, v135, v71, v135
v_cndmask_b32 v22, v22, v23, s[60:61]
mqa 92,244,188
tq 240,4,63488
s_add_u32 m0, s56, 10240
v_cvt_pk_bf16_f32 v38, v204, v205
mqa 84,244,184
tq 242,8,0
buffer_load_dwordx4 v30, s[20:23], s55 offen offset:0 lds
v_cvt_pk_bf16_f32 v39, v206, v207
mqa 84,248,208
tq 244,5,63488
mqa 92,248,212
tq 246,9,0
buffer_store_dwordx4 v[36:39], v14, s[28:31], s80 offen offset:1024 sc1
v_cvt_pk_bf16_f32 v34, v196, v197
v_cvt_pk_bf16_f32 v35, v198, v199
buffer_store_dwordx4 v[32:35], v14, s[28:31], s80 offen offset:0 sc1
mqa 92,252,220
tq 248,6,63488
v_fma_f32 v216, v152, v88, v152
v_fma_f32 v217, v153, v89, v153
mqa 84,252,216
tq 250,12,0
buffer_load_dwordx4 v31, s[20:23], s55 offen offset:1024 lds
s_waitcnt lgkmcnt(6)
mqa 96,224,32
tq 252,7,63488
v_fma_f32 v218, v154, v90, v154
v_fma_f32 v219, v155, v91, v155
mqa 104,224,36
tq 254,13,0
v_fma_f32 v208, v144, v80, v144
v_fma_f32 v209, v145, v81, v145
mqa 108,228,36
tq 224,8,2048
s_add_u32 m0, s56, 18432
v_fma_f32 v210, v146, v82, v146
mqa 100,228,32
tq 226,8,4096
buffer_load_dwordx4 v30, s[20:23], s45 offen offset:0 lds
v_fma_f32 v211, v147, v83, v147
mqa 112,232,32
tq 228,9,2048
v_cvt_pk_bf16_f32 v44, v216, v217
v_cvt_pk_bf16_f32 v45, v218, v219
mqa 120,232,36
tq 230,9,4096
v_cvt_pk_bf16_f32 v40, v208, v209
v_cvt_pk_bf16_f32 v41, v210, v211
mqa 124,236,36
tq 232,12,2048
v_fma_f32 v220, v156, v92, v156
v_fma_f32 v221, v157, v93, v157
mqa 116,236,32
tq 234,12,4096
buffer_load_dwordx4 v31, s[20:23], s45 offen offset:1024 lds
s_waitcnt lgkmcnt(6)
mqa 96,240,40
tq 236,13,2048
v_fma_f32 v222, v158, v94, v158
v_fma_f32 v223, v159, v95, v159
mqa 104,240,44
tq 238,13,4096
v_fma_f32 v212, v148, v84, v148
v_fma_f32 v213, v149, v85, v149
mqa 108,244,44
tq 240,8,6144
s_add_u32 m0, s56, 26624
v_fma_f32 v214, v150, v86, v150
mqa 100,244,40
tq 242,8,8192
buffer_load_dwordx4 v30, s[72:75], s41 offen offset:0 lds
v_fma_f32 v215, v151, v87, v151
mqa 112,248,40
tq 244,9,6144
v_cvt_pk_bf16_f32 v46, v220, v221
v_cvt_pk_bf16_f32 v47, v222, v223
mqa 120,248,44
tq 246,9,8192
buffer_store_dwordx4 v[44:47], v14, s[28:31], s80 offen offset:3072 sc1
v_cvt_pk_bf16_f32 v42, v212, v213
mqa 124,252,44
tq 248,12,6144
v_cvt_pk_bf16_f32 v43, v214, v215
mqa 116,252,40
tq 250,12,8192
buffer_load_dwordx4 v31, s[72:75], s41 offen offset:1024 lds
buffer_store_dwordx4 v[40:43], v14, s[28:31], s80 offen offset:2048 sc1
s_waitcnt lgkmcnt(6)
mqa 96,224,96
tq 252,13,6144
tq 254,13,8192
mqa 104,224,100
s_waitcnt vmcnt(14)
s_barrier
mqa 108,228,100
tq 224,8,26624
s_add_u32 m0, s56, 34816
mqa 100,228,96
tq 226,8,28672
buffer_load_dwordx4 v30, s[20:23], s49 offen offset:0 lds
mqa 112,232,96
tq 228,9,26624
v_and_b32 v175, 0xffff0000, v55
mqa 120,232,100
tq 230,9,28672
v_lshlrev_b32 v176, 16, v56
mqa 124,236,100
tq 232,12,26624
mqa 116,236,96
tq 234,12,28672
buffer_load_dwordx4 v31, s[20:23], s49 offen offset:1024 lds
s_waitcnt lgkmcnt(6)
mqa 96,240,104
tq 236,13,26624
s_add_u32 m0, s56, 108544
mqa 104,240,108
tq 238,13,28672
buffer_load_dwordx4 v30, s[20:23], s51 offen offset:0 lds
mqa 108,244,108
tq 240,8,30720
v_and_b32 v177, 0xffff0000, v56
mqa 100,244,104
tq 242,8,32768
buffer_load_dwordx4 v31, s[20:23], s51 offen offset:1024 lds
mqa 112,248,104
tq 244,9,30720
v_lshlrev_b32 v178, 16, v57
mqa 120,248,108
tq 246,9,32768
v_and_b32 v179, 0xffff0000, v57
mqa 124,252,108
tq 248,12,30720
s_add_u32 m0, s56, 116736
mqa 116,252,104
tq 250,12,32768
buffer_load_dwordx4 v10, s[72:75], s46 offen offset:0 lds
s_waitcnt lgkmcnt(6)
mqa 96,224,128
tq 252,13,30720
v_lshlrev_b32 v180, 16, v58
mqa 104,224,132
tq 254,13,32768
v_and_b32 v181, 0xffff0000, v58
mqa 108,228,132
tq 224,8,10240
v_lshlrev_b32 v182, 16, v59
mqa 100,228,128
tq 226,8,12288
buffer_load_dwordx4 v11, s[72:75], s46 offen offset:1024 lds
mqa 112,232,128
tq 228,9,10240
v_and_b32 v183, 0xffff0000, v59
mqa 120,232,132
tq 230,9,12288
v_lshlrev_b32 v184, 16, v60
mqa 124,236,132
tq 232,12,10240
s_add_u32 m0, s56, 124928
mqa 116,236,128
tq 234,12,12288
buffer_load_dwordx4 v30, s[20:23], s53 offen offset:0 lds
s_waitcnt lgkmcnt(6)
mqa 96,240,136
tq 236,13,10240
v_and_b32 v185, 0xffff0000, v60
mqa 104,240,140
tq 238,13,12288
v_lshlrev_b32 v186, 16, v61
mqa 108,244,140
tq 240,8,14336
v_and_b32 v187, 0xffff0000, v61
mqa 100,244,136
tq 242,8,16384
buffer_load_dwordx4 v31, s[20:23], s53 offen offset:1024 lds
mqa 112,248,136
tq 244,9,14336
v_lshlrev_b32 v188, 16, v62
mqa 120,248,140
tq 246,9,16384
v_and_b32 v189, 0xffff0000, v62
mqa 124,252,140
tq 248,12,14336
s_add_u32 m0, s56, 133120
mqa 116,252,136
tq 250,12,16384
buffer_load_dwordx4 v10, s[72:75], s40 offen offset:0 lds
s_waitcnt lgkmcnt(6)
mqa 96,224,160
tq 252,13,14336
tq 254,13,16384
mqa 104,224,164
s_waitcnt vmcnt(19)
s_barrier
mqa 108,228,164
tq 224,8,34816
v_lshlrev_b32 v190, 16, v63
mqa 100,228,160
tq 226,8,36864
buffer_load_dwordx4 v11, s[72:75], s40 offen offset:1024 lds
mqa 112,232,160
tq 228,9,34816
v_and_b32 v191, 0xffff0000, v63
mqa 120,232,164
tq 230,9,36864
ds_read_b128 v[64:67], v24 offset:512
mqa 124,236,164
tq 232,12,34816
s_add_u32 m0, s56, 141312
mqa 116,236,160
tq 234,12,36864
buffer_load_dwordx4 v30, s[72:75], s40 offen offset:0 lds
s_waitcnt lgkmcnt(7)
mqa 96,240,168
tq 236,13,34816
ds_read_b128 v[72:75], v24 offset:512
mqa 104,240,172
tq 238,13,36864
ds_read_b128 v[68:71], v24 offset:576
mqa 108,244,172
tq 240,8,38912
ds_read_b128 v[76:79], v24 offset:576
mqa 100,244,168
tq 242,8,40960
buffer_load_dwordx4 v31, s[72:75], s40 offen offset:1024 lds
mqa 112,248,168
tq 244,9,38912
ds_read_b128 v[80:83], v24 offset:640
mqa 120,248,172
tq 246,9,40960
ds_read_b128 v[88:91], v24 offset:640
mqa 124,252,172
tq 248,12,38912
mqa 116,252,168
tq 250,12,40960
s_waitcnt vmcnt(12)
buffer_store_dword v22, v21, s[28:31], 0 offen offset:0 sc1
s_waitcnt lgkmcnt(10)
mqa 96,224,192
tq 252,13,38912
s_add_u32 m0, s56, 149504
mqa 104,224,196
tq 254,13,40960
buffer_load_dwordx4 v10, s[72:75], s52 offen offset:0 lds
mqa 108,228,196
tq 224,4,2048
ds_read_b128 v[84:87], v24 offset:704
mqa 100,228,192
tq 226,4,4096
buffer_load_dwordx4 v11, s[72:75], s52 offen offset:1024 lds
mqa 112,232,192
tq 228,5,2048
ds_read_b128 v[92:95], v24 offset:704
mqa 120,232,196
tq 230,5,4096
mqa 124,236,196
tq 232,6,2048
s_add_u32 m0, s56, 43008
mqa 116,236,192
tq 234,6,4096
buffer_load_dwordx4 v10, s[72:75], s41 offen offset:0 lds
s_waitcnt lgkmcnt(8)
mqa 96,240,200
tq 236,7,2048
mqa 104,240,204
tq 238,7,4096
mqa 108,244,204
tq 240,4,6144
mqa 100,244,200
tq 242,4,8192
buffer_load_dwordx4 v11, s[72:75], s41 offen offset:1024 lds
mqa 112,248,200
tq 244,5,6144
mqa 120,248,204
tq 246,5,8192
mqa 124,252,204
tq 248,6,6144
s_add_u32 m0, s56, 51200
mqa 116,252,200
tq 250,6,8192
buffer_load_dwordx4 v10, s[72:75], s42 offen offset:0 lds
s_waitcnt lgkmcnt(6)
mqa 96,224,16
tq 252,7,6144
tq 254,7,8192
mqa 104,224,20
s_barrier
mqa 108,228,20
tq 224,4,18432
mqa 100,228,16
tq 226,4,20480
buffer_load_dwordx4 v11, s[72:75], s42 offen offset:1024 lds
mqa 112,232,16
tq 228,5,18432
mqa 120,232,20
tq 230,5,20480
s_lshr_b32 s82, s42, 4
mqa 124,236,20
tq 232,6,18432
s_add_u32 m0, s56, 59392
mqa 116,236,16
tq 234,6,20480
buffer_load_dwordx4 v10, s[72:75], s45 offen offset:0 lds
s_waitcnt lgkmcnt(6)
mqa 96,240,24
tq 236,7,18432
mqa 104,240,28
tq 238,7,20480
s_lshr_b32 s83, s43, 4
mqa 108,244,28
tq 240,4,22528
mqa 100,244,24
tq 242,4,24576
buffer_load_dwordx4 v11, s[72:75], s45 offen offset:1024 lds
mqa 112,248,24
tq 244,5,22528
mqa 120,248,28
tq 246,5,24576
s_lshr_b32 s84, s44, 4
mqa 124,252,28
tq 248,6,22528
s_add_u32 m0, s56, 67584
mqa 116,252,24
tq 250,6,24576
buffer_load_dwordx4 v10, s[72:75], s44 offen offset:0 lds
s_waitcnt lgkmcnt(6)
mqa 96,224,48
tq 252,7,22528
mqa 104,224,52
tq 254,7,24576
s_lshr_b32 s85, s45, 4
mqa 108,228,52
tq 224,8,18432
mqa 100,228,48
tq 226,8,20480
buffer_load_dwordx4 v11, s[72:75], s44 offen offset:1024 lds
mqa 112,232,48
tq 228,9,18432
mqa 120,232,52
tq 230,9,20480
s_lshr_b32 s86, s46, 4
mqa 124,236,52
tq 232,12,18432
s_add_u32 m0, s56, 92160
mqa 116,236,48
tq 234,12,20480
buffer_load_dwordx4 v30, s[72:75], s42 offen offset:0 lds
s_waitcnt lgkmcnt(6)
mqa 96,240,56
tq 236,13,18432
mqa 104,240,60
tq 238,13,20480
s_lshr_b32 s87, s47, 4
mqa 108,244,60
tq 240,8,22528
mqa 100,244,56
tq 242,8,24576
buffer_load_dwordx4 v31, s[72:75], s42 offen offset:1024 lds
mqa 112,248,56
tq 244,9,22528
mqa 120,248,60
tq 246,9,24576
s_lshr_b32 s88, s48, 4
mqa 124,252,60
tq 248,12,22528
s_add_u32 m0, s56, 75776
mqa 116,252,56
tq 250,12,24576
buffer_load_dwordx4 v10, s[72:75], s53 offen offset:0 lds
s_waitcnt lgkmcnt(6)
mqa 96,224,80
tq 252,13,22528
tq 254,13,24576
mqa 104,224,84
s_waitcnt vmcnt(22)
s_barrier
mqa 108,228,84
tq 224,4,34816
mqa 100,228,80
tq 226,4,36864
buffer_load_dwordx4 v11, s[72:75], s53 offen offset:1024 lds
mqa 112,232,80
tq 228,5,34816
mqa 120,232,84
tq 230,5,36864
s_lshr_b32 s89, s49, 4
mqa 124,236,84
tq 232,6,34816
s_add_u32 m0, s56, 100352
mqa 116,236,80
tq 234,6,36864
buffer_load_dwordx4 v10, s[72:75], s48 offen offset:0 lds
s_waitcnt lgkmcnt(6)
mqa 96,240,88
tq 236,7,34816
mqa 104,240,92
tq 238,7,36864
s_lshr_b32 s90, s50, 4
mqa 108,244,92
tq 240,4,38912
mqa 100,244,88
tq 242,4,40960
buffer_load_dwordx4 v11, s[72:75], s48 offen offset:1024 lds
mqa 112,248,88
tq 244,5,38912
mqa 120,248,92
tq 246,5,40960
s_lshr_b32 s91, s51, 4
mqa 124,252,92
tq 248,6,38912
mqa 116,252,88
tq 250,6,40960
s_lshr_b32 s92, s52, 4
s_waitcnt lgkmcnt(6)
mqa 96,224,112
tq 252,7,38912
s_add_u32 m0, s56, 2048
mqa 104,224,116
tq 254,7,40960
buffer_load_dwordx4 v30, s[72:75], s44 offen offset:0 lds
mqa 108,228,116
tq 224,8,43008
mqa 100,228,112
tq 226,8,45056
buffer_load_dwordx4 v31, s[72:75], s44 offen offset:1024 lds
mqa 112,232,112
tq 228,9,43008
mqa 120,232,116
tq 230,9,45056
s_lshr_b32 s93, s53, 4
mqa 124,236,116
tq 232,12,43008
mqa 116,236,112
tq 234,12,45056
s_lshr_b32 s94, s54, 4
s_waitcnt lgkmcnt(6)
mqa 96,240,120
tq 236,13,43008
mqa 104,240,124
tq 238,13,45056
s_lshr_b32 s95, s55, 4
mqa 108,244,124
tq 240,8,47104
mqa 100,244,120
tq 242,8,49152
mqa 112,248,120
tq 244,9,47104
mqa 120,248,124
tq 246,9,49152
mqa 124,252,124
tq 248,12,47104
mqa 116,252,120
tq 250,12,49152
s_waitcnt lgkmcnt(6)
mqa 96,224,144
tq 252,13,47104
tq 254,13,49152
s_add_u32 m0, s56, 18432
mqa 104,224,148
buffer_load_dwordx4 v10, s[72:75], s50 offen offset:0 lds
s_waitcnt vmcnt(24)
s_barrier
mqa 108,228,148
tq 224,8,59392
mqa 100,228,144
tq 226,8,61440
buffer_load_dwordx4 v11, s[72:75], s50 offen offset:1024 lds
mqa 112,232,144
tq 228,8,63488
s_add_u32 m0, s56, 34816
mqa 120,232,148
tq 230,16,0
buffer_load_dwordx4 v30, s[72:75], s52 offen offset:0 lds
mqa 124,236,148
tq 232,4,10240
mqa 116,236,144
tq 234,4,12288
s_waitcnt lgkmcnt(6)
mqa 96,240,152
tq 236,4,14336
mqa 104,240,156
tq 238,4,16384
buffer_load_dwordx4 v31, s[72:75], s52 offen offset:1024 lds
mqa 108,244,156
tq 240,9,59392
mqa 100,244,152
tq 242,9,61440
mqa 112,248,152
tq 244,9,63488
mqa 120,248,156
tq 246,17,0
mqa 124,252,156
tq 248,5,10240
mqa 116,252,152
tq 250,5,12288
s_waitcnt lgkmcnt(6)
mqa 96,224,176
tq 252,5,14336
mqa 104,224,180
tq 254,5,16384
L_hop3_3_%=:
s_cbranch_execz L_hop7_3_%=
buffer_load_dword v23, v20, s[28:31], 0 offen offset:0 sc1
mqa 104,228,188
tq 224,12,59392
mqa 96,228,184
tq 226,12,61440
mqa 96,232,208
tq 228,12,63488
mqa 104,232,212
tq 230,28,0
mqa 104,236,220
tq 232,6,10240
mqa 96,236,216
tq 234,6,12288
s_waitcnt lgkmcnt(6)
mqa 100,240,176
tq 236,6,14336
mqa 108,240,180
tq 238,6,16384
mqa 108,244,188
tq 240,13,59392
v_fma_f32 v232, v168, v104, v168
v_fma_f32 v233, v169, v105, v169
mqa 100,244,184
tq 242,13,61440
v_fma_f32 v234, v170, v106, v170
mqa 100,248,208
tq 244,13,63488
v_fma_f32 v235, v171, v107, v171
v_fma_f32 v224, v160, v96, v160
mqa 108,248,212
tq 246,29,0
v_fma_f32 v225, v161, v97, v161
mqa 108,252,220
tq 248,7,10240
v_fma_f32 v226, v162, v98, v162
v_fma_f32 v227, v163, v99, v163
mqa 100,252,216
tq 250,7,12288
s_waitcnt lgkmcnt(6)
mqa 112,224,176
tq 252,7,14336
tq 254,7,16384
s_add_u32 m0, s56, 83968
mqa 120,224,180
buffer_load_dwordx4 v30, s[72:75], s46 offen offset:0 lds
s_waitcnt vmcnt(25)
s_barrier
mqa 120,228,188
tq 224,16,2048
v_cvt_pk_bf16_f32 v52, v232, v233
v_cvt_pk_bf16_f32 v53, v234, v235
mqa 112,228,184
tq 226,16,4096
v_cvt_pk_bf16_f32 v48, v224, v225
mqa 112,232,208
tq 228,17,2048
v_cvt_pk_bf16_f32 v49, v226, v227
v_fma_f32 v236, v172, v108, v172
mqa 120,232,212
tq 230,17,4096
buffer_load_dwordx4 v31, s[72:75], s46 offen offset:1024 lds
mqa 120,236,220
tq 232,16,6144
v_fma_f32 v237, v173, v109, v173
v_fma_f32 v238, v174, v110, v174
mqa 112,236,216
tq 234,16,8192
s_waitcnt lgkmcnt(6)
mqa 116,240,176
tq 236,17,6144
v_fma_f32 v239, v175, v111, v175
v_fma_f32 v228, v164, v100, v164
mqa 124,240,180
tq 238,17,8192
v_fma_f32 v229, v165, v101, v165
mqa 124,244,188
tq 240,28,2048
v_fma_f32 v230, v166, v102, v166
v_fma_f32 v231, v167, v103, v167
mqa 116,244,184
tq 242,28,4096
v_cvt_pk_bf16_f32 v54, v236, v237
v_cvt_pk_bf16_f32 v55, v238, v239
mqa 116,248,208
tq 244,29,2048
mqa 124,248,212
tq 246,29,4096
buffer_store_dwordx4 v[52:55], v14, s[28:31], s81 offen offset:1024 sc1
v_cvt_pk_bf16_f32 v50, v228, v229
v_cvt_pk_bf16_f32 v51, v230, v231
buffer_store_dwordx4 v[48:51], v14, s[28:31], s81 offen offset:0 sc1
mqa 124,252,220
tq 248,28,6144
ds_read_b128 v[96:99], v24 offset:768
v_fma_f32 v248, v184, v120, v184
v_fma_f32 v249, v185, v121, v185
mqa 116,252,216
tq 250,28,8192
v_fma_f32 v250, v186, v122, v186
s_waitcnt lgkmcnt(7)
mqv 64,224,32
tq 252,29,6144
v_fma_f32 v251, v187, v123, v187
mqv 72,224,36
tq 254,29,8192
s_waitcnt vmcnt(4)
v_cmp_ne_u32 vcc, v23, v22
s_cbranch_vccnz L_slow0_3_%=
L_back0_3_%=:
buffer_load_dwordx4 a[0:3], v14, s[28:31], s82 offen offset:0 sc1
mqv 76,228,36
tq 224,16,10240
ds_read_b128 v[104:107], v24 offset:768
v_fma_f32 v240, v176, v112, v176
mqv 68,228,32
tq 226,16,12288
v_fma_f32 v241, v177, v113, v177
v_fma_f32 v242, v178, v114, v178
mqv 64,232,40
tq 228,17,10240
v_fma_f32 v243, v179, v115, v179
v_cvt_pk_bf16_f32 v60, v248, v249
v_cvt_pk_bf16_f32 v61, v250, v251
mqv 72,232,44
tq 230,17,12288
buffer_load_dwordx4 a[4:7], v14, s[28:31], s82 offen offset:1024 sc1
ds_read_b128 v[100:103], v24 offset:832
mqv 76,236,44
tq 232,16,14336
v_cvt_pk_bf16_f32 v56, v240, v241
v_cvt_pk_bf16_f32 v57, v242, v243
ds_read_b128 v[108:111], v24 offset:832
mqv 68,236,40
tq 234,16,16384
v_fma_f32 v252, v188, v124, v188
s_waitcnt lgkmcnt(9)
mqv 80,240,32
tq 236,17,14336
v_fma_f32 v253, v189, v125, v189
v_fma_f32 v254, v190, v126, v190
v_fma_f32 v255, v191, v127, v191
mqv 88,240,36
tq 238,17,16384
buffer_load_dwordx4 a[8:11], v14, s[28:31], s82 offen offset:2048 sc1
ds_read_b128 v[112:115], v24 offset:896
mqv 92,244,36
tq 240,28,10240
v_fma_f32 v244, v180, v116, v180
v_fma_f32 v245, v181, v117, v181
mqv 84,244,32
tq 242,28,12288
v_fma_f32 v246, v182, v118, v182
v_fma_f32 v247, v183, v119, v183
mqv 80,248,40
tq 244,29,10240
v_cvt_pk_bf16_f32 v62, v252, v253
v_cvt_pk_bf16_f32 v63, v254, v255
mqv 88,248,44
tq 246,29,12288
buffer_load_dwordx4 a[12:15], v14, s[28:31], s82 offen offset:3072 sc1
buffer_store_dwordx4 v[60:63], v14, s[28:31], s81 offen offset:3072 sc1
ds_read_b128 v[120:123], v24 offset:896
mqv 92,252,44
tq 248,28,14336
v_cvt_pk_bf16_f32 v58, v244, v245
v_cvt_pk_bf16_f32 v59, v246, v247
mqv 84,252,40
tq 250,28,16384
buffer_store_dwordx4 v[56:59], v14, s[28:31], s81 offen offset:2048 sc1
ds_read_b128 v[116:119], v24 offset:960
s_waitcnt lgkmcnt(9)
mqv 96,224,32
tq 252,29,14336
tq 254,29,16384
ds_read_b128 v[124:127], v24 offset:960
mqv 104,224,36
buffer_load_dwordx4 a[32:35], v14, s[28:31], s84 offen offset:0 sc1
s_barrier
mqv 108,228,36
tq 224,4,43008
mqv 100,228,32
tq 226,4,45056
buffer_load_dwordx4 a[36:39], v14, s[28:31], s84 offen offset:1024 sc1
mqv 96,232,40
tq 228,5,43008
mqv 104,232,44
tq 230,5,45056
buffer_load_dwordx4 a[40:43], v14, s[28:31], s84 offen offset:2048 sc1
mqv 108,236,44
tq 232,6,43008
mqv 100,236,40
tq 234,6,45056
s_waitcnt lgkmcnt(6)
mqv 112,240,32
tq 236,7,43008
mqv 120,240,36
tq 238,7,45056
buffer_load_dwordx4 a[44:47], v14, s[28:31], s84 offen offset:3072 sc1
mqv 124,244,36
tq 240,4,47104
mqv 116,244,32
tq 242,4,49152
mqv 112,248,40
tq 244,5,47104
s_add_u32 m0, s56, 108544
mqv 120,248,44
tq 246,5,49152
buffer_load_dwordx4 v30, s[72:75], s47 offen offset:0 lds
mqv 124,252,44
tq 248,6,47104
mqv 116,252,40
tq 250,6,49152
s_waitcnt lgkmcnt(6)
mqv 64,224,48
tq 252,7,47104
mqv 72,224,52
tq 254,7,49152
buffer_load_dwordx4 v31, s[72:75], s47 offen offset:1024 lds
mqv 76,228,52
tq 224,4,26624
mqv 68,228,48
tq 226,4,28672
mqv 80,232,48
tq 228,5,26624
mqv 88,232,52
tq 230,5,28672
buffer_load_dwordx4 a[64:67], v14, s[28:31], s86 offen offset:0 sc1
mqv 92,236,52
tq 232,6,26624
mqv 84,236,48
tq 234,6,28672
s_waitcnt lgkmcnt(6)
mqv 64,240,56
tq 236,7,26624
mqv 72,240,60
tq 238,7,28672
buffer_load_dwordx4 a[68:71], v14, s[28:31], s86 offen offset:1024 sc1
mqv 76,244,60
tq 240,4,30720
mqv 68,244,56
tq 242,4,32768
mqv 80,248,56
tq 244,5,30720
mqv 88,248,60
tq 246,5,32768
buffer_load_dwordx4 a[72:75], v14, s[28:31], s86 offen offset:2048 sc1
mqv 92,252,60
tq 248,6,30720
mqv 84,252,56
tq 250,6,32768
s_waitcnt lgkmcnt(6)
mqv 96,224,48
tq 252,7,30720
tq 254,7,32768
mqv 104,224,52
s_barrier
mqv 108,228,52
tq 224,4,51200
mqv 100,228,48
tq 226,4,53248
buffer_load_dwordx4 a[76:79], v14, s[28:31], s86 offen offset:3072 sc1
mqv 112,232,48
tq 228,5,51200
mqv 120,232,52
tq 230,5,53248
buffer_load_dwordx4 a[96:99], v14, s[28:31], s88 offen offset:0 sc1
mqv 124,236,52
tq 232,6,51200
mqv 116,236,48
tq 234,6,53248
s_waitcnt lgkmcnt(6)
mqv 96,240,56
tq 236,7,51200
mqv 104,240,60
tq 238,7,53248
buffer_load_dwordx4 a[100:103], v14, s[28:31], s88 offen offset:1024 sc1
mqv 108,244,60
tq 240,4,55296
mqv 100,244,56
tq 242,4,57344
mqv 112,248,56
tq 244,5,55296
mqv 120,248,60
tq 246,5,57344
buffer_load_dwordx4 a[104:107], v14, s[28:31], s88 offen offset:2048 sc1
mqv 124,252,60
tq 248,6,55296
mqv 116,252,56
tq 250,6,57344
s_waitcnt vmcnt(13)
buffer_store_dword v22, v21, s[28:31], 0 offen offset:32 sc1
s_waitcnt lgkmcnt(6)
mqa 64,224,0
tq 252,7,55296
mqa 72,224,4
tq 254,7,57344
buffer_load_dwordx4 a[108:111], v14, s[28:31], s88 offen offset:3072 sc1
mqa 76,228,4
tq 224,8,2048
mqa 68,228,0
tq 226,8,4096
mqa 80,232,0
tq 228,9,2048
mqa 88,232,4
tq 230,9,4096
buffer_load_dwordx4 a[128:131], v14, s[28:31], s90 offen offset:0 sc1
mqa 92,236,4
tq 232,12,2048
mqa 84,236,0
tq 234,12,4096
s_waitcnt lgkmcnt(6)
mqa 64,240,8
tq 236,13,2048
mqa 72,240,12
tq 238,13,4096
buffer_load_dwordx4 a[132:135], v14, s[28:31], s90 offen offset:1024 sc1
mqa 76,244,12
tq 240,8,6144
mqa 68,244,8
tq 242,8,8192
mqa 80,248,8
tq 244,9,6144
mqa 88,248,12
tq 246,9,8192
buffer_load_dwordx4 a[136:139], v14, s[28:31], s90 offen offset:2048 sc1
mqa 92,252,12
tq 248,12,6144
mqa 84,252,8
tq 250,12,8192
s_waitcnt lgkmcnt(6)
s_waitcnt vmcnt(14)
mqa 64,224,32
tq 252,13,6144
tq 254,13,8192
mqa 72,224,36
s_barrier
mqa 76,228,36
tq 224,8,26624
mqa 68,228,32
tq 226,8,28672
buffer_load_dwordx4 a[140:143], v14, s[28:31], s90 offen offset:3072 sc1
mqa 80,232,32
tq 228,9,26624
s_add_u32 m0, s56, 10240
mqa 88,232,36
tq 230,9,28672
buffer_load_dwordx4 v30, s[72:75], s55 offen offset:0 lds
mqa 92,236,36
tq 232,12,26624
mqa 84,236,32
tq 234,12,28672
s_waitcnt lgkmcnt(6)
mqa 64,240,40
tq 236,13,26624
mqa 72,240,44
tq 238,13,28672
buffer_load_dwordx4 v31, s[72:75], s55 offen offset:1024 lds
mqa 76,244,44
tq 240,8,30720
mqa 68,244,40
tq 242,8,32768
mqa 80,248,40
tq 244,9,30720
s_add_u32 m0, s56, 124928
mqa 88,248,44
tq 246,9,32768
buffer_load_dwordx4 v10, s[72:75], s54 offen offset:0 lds
mqa 92,252,44
tq 248,12,30720
mqa 84,252,40
tq 250,12,32768
s_waitcnt lgkmcnt(6)
mqa 96,224,0
tq 252,13,30720
mqa 104,224,4
tq 254,13,32768
buffer_load_dwordx4 v11, s[72:75], s54 offen offset:1024 lds
mqa 108,228,4
tq 224,8,51200
mqa 100,228,0
tq 226,8,53248
mqa 112,232,0
tq 228,9,51200
mqa 120,232,4
tq 230,9,53248
buffer_load_dwordx4 a[160:163], v14, s[28:31], s92 offen offset:0 sc1
mqa 124,236,4
tq 232,12,51200
mqa 116,236,0
tq 234,12,53248
s_waitcnt lgkmcnt(6)
mqa 96,240,8
tq 236,13,51200
mqa 104,240,12
tq 238,13,53248
buffer_load_dwordx4 a[164:167], v14, s[28:31], s92 offen offset:1024 sc1
mqa 108,244,12
tq 240,8,55296
mqa 100,244,8
tq 242,8,57344
mqa 112,248,8
tq 244,9,55296
mqa 120,248,12
tq 246,9,57344
buffer_load_dwordx4 a[168:171], v14, s[28:31], s92 offen offset:2048 sc1
mqa 124,252,12
tq 248,12,55296
mqa 116,252,8
tq 250,12,57344
s_waitcnt lgkmcnt(6)
s_waitcnt vmcnt(16)
mqa 64,224,64
tq 252,13,55296
tq 254,13,57344
mqa 72,224,68
s_barrier
mqa 76,228,68
tq 224,8,34816
mqa 68,228,64
tq 226,8,36864
buffer_load_dwordx4 a[172:175], v14, s[28:31], s92 offen offset:3072 sc1
mqa 80,232,64
tq 228,9,34816
s_add_u32 m0, s56, 133120
mqa 88,232,68
tq 230,9,36864
buffer_load_dwordx4 v10, s[72:75], s43 offen offset:0 lds
mqa 92,236,68
tq 232,12,34816
mqa 84,236,64
tq 234,12,36864
s_waitcnt lgkmcnt(6)
mqa 64,240,72
tq 236,13,34816
mqa 72,240,76
tq 238,13,36864
buffer_load_dwordx4 v11, s[72:75], s43 offen offset:1024 lds
mqa 76,244,76
tq 240,8,38912
mqa 68,244,72
tq 242,8,40960
mqa 80,248,72
tq 244,9,38912
mqa 88,248,76
tq 246,9,40960
buffer_load_dwordx4 a[192:195], v14, s[28:31], s94 offen offset:0 sc1
mqa 92,252,76
tq 248,12,38912
mqa 84,252,72
tq 250,12,40960
s_waitcnt lgkmcnt(6)
s_waitcnt vmcnt(15)
mqa 64,224,96
tq 252,13,38912
mqa 72,224,100
tq 254,13,40960
buffer_load_dwordx4 a[196:199], v14, s[28:31], s94 offen offset:1024 sc1
mqa 76,228,100
tq 224,4,2048
mqa 68,228,96
tq 226,4,4096
mqa 80,232,96
tq 228,5,2048
mqa 88,232,100
tq 230,5,4096
buffer_load_dwordx4 a[200:203], v14, s[28:31], s94 offen offset:2048 sc1
mqa 92,236,100
tq 232,6,2048
mqa 84,236,96
tq 234,6,4096
s_waitcnt lgkmcnt(6)
mqa 64,240,104
tq 236,7,2048
mqa 72,240,108
tq 238,7,4096
buffer_load_dwordx4 a[204:207], v14, s[28:31], s94 offen offset:3072 sc1
mqa 76,244,108
tq 240,4,6144
mqa 68,244,104
tq 242,4,8192
mqa 80,248,104
tq 244,5,6144
s_add_u32 m0, s56, 43008
mqa 88,248,108
tq 246,5,8192
buffer_load_dwordx4 v10, s[72:75], s47 offen offset:0 lds
mqa 92,252,108
tq 248,6,6144
mqa 84,252,104
tq 250,6,8192
s_waitcnt lgkmcnt(6)
mqa 96,224,32
tq 252,7,6144
tq 254,7,8192
mqa 104,224,36
buffer_load_dword v23, v20, s[28:31], 0 offen offset:32 sc1
s_barrier
mqa 108,228,36
tq 224,4,18432
mqa 100,228,32
tq 226,4,20480
buffer_load_dwordx4 v11, s[72:75], s47 offen offset:1024 lds
mqa 112,232,32
tq 228,5,18432
s_add_u32 m0, s56, 141312
mqa 120,232,36
tq 230,5,20480
buffer_load_dwordx4 v30, s[76:79], s41 offen offset:0 lds
mqa 124,236,36
tq 232,6,18432
mqa 116,236,32
tq 234,6,20480
s_waitcnt lgkmcnt(6)
mqa 96,240,40
tq 236,7,18432
mqa 104,240,44
tq 238,7,20480
buffer_load_dwordx4 v31, s[76:79], s41 offen offset:1024 lds
mqa 108,244,44
tq 240,4,22528
mqa 100,244,40
tq 242,4,24576
mqa 112,248,40
tq 244,5,22528
s_add_u32 m0, s56, 26624
mqa 120,248,44
tq 246,5,24576
buffer_load_dwordx4 v10, s[72:75], s49 offen offset:0 lds
mqa 124,252,44
tq 248,6,22528
mqa 116,252,40
L_hop4_3_%=:
s_cbranch_execz L_hop8_3_%=
tq 250,6,24576
s_waitcnt lgkmcnt(6)
s_waitcnt vmcnt(20)
mqa 64,224,128
tq 252,7,22528
mqa 72,224,132
tq 254,7,24576
buffer_load_dwordx4 v11, s[72:75], s49 offen offset:1024 lds
mqa 76,228,132
tq 224,16,18432
mqa 68,228,128
tq 226,16,20480
mqa 80,232,128
tq 228,17,18432
s_add_u32 m0, s56, 51200
mqa 88,232,132
tq 230,17,20480
buffer_load_dwordx4 v10, s[72:75], s51 offen offset:0 lds
mqa 92,236,132
tq 232,28,18432
mqa 84,236,128
tq 234,28,20480
s_waitcnt lgkmcnt(6)
mqa 64,240,136
tq 236,29,18432
mqa 72,240,140
tq 238,29,20480
buffer_load_dwordx4 v11, s[72:75], s51 offen offset:1024 lds
mqa 76,244,140
tq 240,16,22528
mqa 68,244,136
tq 242,16,24576
mqa 80,248,136
tq 244,17,22528
s_add_u32 m0, s56, 67584
mqa 88,248,140
tq 246,17,24576
buffer_load_dwordx4 v10, s[72:75], s55 offen offset:0 lds
mqa 92,252,140
tq 248,28,22528
mqa 84,252,136
tq 250,28,24576
s_waitcnt lgkmcnt(6)
s_waitcnt vmcnt(16)
mqa 64,224,160
tq 252,29,22528
tq 254,29,24576
mqa 72,224,164
s_waitcnt vmcnt(8)
v_cmp_ne_u32 vcc, v23, v22
s_cbranch_vccnz L_slow1_3_%=
L_back1_3_%=:
s_barrier
mqa 76,228,164
tq 224,8,18432
mqa 68,228,160
tq 226,8,20480
buffer_load_dwordx4 v11, s[72:75], s55 offen offset:1024 lds
mqa 80,232,160
tq 228,9,18432
mqa 88,232,164
tq 230,9,20480
buffer_load_dwordx4 a[16:19], v14, s[28:31], s83 offen offset:0 sc1
mqa 92,236,164
tq 232,12,18432
mqa 84,236,160
tq 234,12,20480
buffer_load_dwordx4 a[20:23], v14, s[28:31], s83 offen offset:1024 sc1
s_waitcnt lgkmcnt(6)
mqa 64,240,168
tq 236,13,18432
mqa 72,240,172
tq 238,13,20480
buffer_load_dwordx4 a[24:27], v14, s[28:31], s83 offen offset:2048 sc1
mqa 76,244,172
tq 240,8,22528
mqa 68,244,168
tq 242,8,24576
buffer_load_dwordx4 a[28:31], v14, s[28:31], s83 offen offset:3072 sc1
mqa 80,248,168
tq 244,9,22528
mqa 88,248,172
tq 246,9,24576
mqa 92,252,172
tq 248,12,22528
mqa 84,252,168
tq 250,12,24576
buffer_load_dwordx4 a[48:51], v14, s[28:31], s85 offen offset:0 sc1
s_waitcnt lgkmcnt(6)
mqa 96,224,64
tq 252,13,22528
mqa 104,224,68
tq 254,13,24576
buffer_load_dwordx4 a[52:55], v14, s[28:31], s85 offen offset:1024 sc1
mqa 108,228,68
tq 224,8,59392
mqa 100,228,64
tq 226,8,61440
buffer_load_dwordx4 a[56:59], v14, s[28:31], s85 offen offset:2048 sc1
mqa 112,232,64
tq 228,9,59392
mqa 120,232,68
tq 230,9,61440
buffer_load_dwordx4 a[60:63], v14, s[28:31], s85 offen offset:3072 sc1
mqa 124,236,68
tq 232,12,59392
mqa 116,236,64
tq 234,12,61440
s_waitcnt lgkmcnt(6)
mqa 96,240,72
tq 236,13,59392
mqa 104,240,76
tq 238,13,61440
mqa 108,244,76
tq 240,8,63488
mqa 100,244,72
tq 242,16,0
mqa 112,248,72
tq 244,9,63488
mqa 120,248,76
tq 246,17,0
mqa 124,252,76
tq 248,12,63488
mqa 116,252,72
tq 250,28,0
buffer_load_dwordx4 a[80:83], v14, s[28:31], s87 offen offset:0 sc1
s_waitcnt lgkmcnt(6)
mqa 64,224,192
tq 252,13,63488
tq 254,29,0
mqa 72,224,196
buffer_load_dwordx4 a[84:87], v14, s[28:31], s87 offen offset:1024 sc1
s_barrier
mqa 76,228,196
tq 224,16,2048
mqa 68,228,192
tq 226,16,4096
buffer_load_dwordx4 a[88:91], v14, s[28:31], s87 offen offset:2048 sc1
mqa 80,232,192
tq 228,17,2048
mqa 88,232,196
tq 230,17,4096
buffer_load_dwordx4 a[92:95], v14, s[28:31], s87 offen offset:3072 sc1
mqa 92,236,196
tq 232,28,2048
mqa 84,236,192
tq 234,28,4096
s_waitcnt lgkmcnt(6)
mqa 64,240,200
tq 236,29,2048
mqa 72,240,204
tq 238,29,4096
mqa 76,244,204
tq 240,16,6144
mqa 68,244,200
tq 242,16,8192
mqa 80,248,200
tq 244,17,6144
mqa 88,248,204
tq 246,17,8192
mqa 92,252,204
tq 248,28,6144
mqa 84,252,200
tq 250,28,8192
buffer_load_dwordx4 a[112:115], v14, s[28:31], s89 offen offset:0 sc1
s_waitcnt lgkmcnt(6)
s_waitcnt vmcnt(9)
mqa 64,224,16
tq 252,29,6144
mqa 72,224,20
tq 254,29,8192
buffer_load_dwordx4 a[116:119], v14, s[28:31], s89 offen offset:1024 sc1
mqa 76,228,20
tq 224,4,59392
mqa 68,228,16
tq 226,4,61440
buffer_load_dwordx4 a[120:123], v14, s[28:31], s89 offen offset:2048 sc1
mqa 80,232,16
tq 228,5,59392
mqa 88,232,20
tq 230,5,61440
buffer_load_dwordx4 a[124:127], v14, s[28:31], s89 offen offset:3072 sc1
mqa 92,236,20
tq 232,6,59392
mqa 84,236,16
tq 234,6,61440
s_waitcnt lgkmcnt(6)
mqa 64,240,24
tq 236,7,59392
mqa 72,240,28
tq 238,7,61440
mqa 76,244,28
tq 240,4,63488
mqa 68,244,24
tq 242,8,0
mqa 80,248,24
tq 244,5,63488
mqa 88,248,28
tq 246,9,0
mqa 92,252,28
tq 248,6,63488
mqa 84,252,24
tq 250,12,0
buffer_load_dwordx4 a[144:147], v14, s[28:31], s91 offen offset:0 sc1
s_waitcnt lgkmcnt(6)
s_waitcnt vmcnt(9)
mqa 64,224,48
tq 252,7,63488
tq 254,13,0
mqa 72,224,52
buffer_load_dwordx4 a[148:151], v14, s[28:31], s91 offen offset:1024 sc1
s_barrier
mqa 76,228,52
tq 224,4,43008
mqa 68,228,48
tq 226,4,45056
buffer_load_dwordx4 a[152:155], v14, s[28:31], s91 offen offset:2048 sc1
mqa 80,232,48
tq 228,5,43008
mqa 88,232,52
tq 230,5,45056
buffer_load_dwordx4 a[156:159], v14, s[28:31], s91 offen offset:3072 sc1
mqa 92,236,52
tq 232,6,43008
mqa 84,236,48
tq 234,6,45056
s_waitcnt lgkmcnt(6)
mqa 64,240,56
tq 236,7,43008
mqa 72,240,60
tq 238,7,45056
buffer_load_dword v25, v19, s[76:79], 0 offen
mqa 76,244,60
tq 240,4,47104
mqa 68,244,56
tq 242,4,49152
mqa 80,248,56
tq 244,5,47104
mqa 88,248,60
tq 246,5,49152
mqa 92,252,60
tq 248,6,47104
mqa 84,252,56
tq 250,6,49152
buffer_load_dwordx4 a[176:179], v14, s[28:31], s93 offen offset:0 sc1
s_waitcnt lgkmcnt(6)
s_waitcnt vmcnt(10)
mqa 64,224,80
tq 252,7,47104
mqa 72,224,84
tq 254,7,49152
buffer_load_dwordx4 a[180:183], v14, s[28:31], s93 offen offset:1024 sc1
mqa 76,228,84
tq 224,4,26624
mqa 68,228,80
tq 226,4,28672
buffer_load_dwordx4 a[184:187], v14, s[28:31], s93 offen offset:2048 sc1
mqa 80,232,80
tq 228,5,26624
mqa 88,232,84
tq 230,5,28672
buffer_load_dwordx4 a[188:191], v14, s[28:31], s93 offen offset:3072 sc1
mqa 92,236,84
tq 232,6,26624
s_add_u32 m0, s56, 92160
mqa 84,236,80
tq 234,6,28672
buffer_load_dwordx4 v30, s[72:75], s48 offen offset:0 lds
s_waitcnt lgkmcnt(6)
mqa 64,240,88
tq 236,7,26624
mqa 72,240,92
tq 238,7,28672
buffer_load_dwordx4 v31, s[72:75], s48 offen offset:1024 lds
mqa 76,244,92
tq 240,4,30720
mqa 68,244,88
tq 242,4,32768
buffer_load_dwordx4 a[208:211], v14, s[28:31], s95 offen offset:0 sc1
mqa 80,248,88
)ASM"
R"ASM(
tq 244,5,30720
mqa 88,248,92
tq 246,5,32768
buffer_load_dwordx4 a[212:215], v14, s[28:31], s95 offen offset:1024 sc1
mqa 92,252,92
tq 248,6,30720
mqa 84,252,88
tq 250,6,32768
buffer_load_dwordx4 a[216:219], v14, s[28:31], s95 offen offset:2048 sc1
s_waitcnt lgkmcnt(6)
s_waitcnt vmcnt(14)
mqa 64,224,112
tq 252,7,30720
tq 254,7,32768
mqa 72,224,116
buffer_load_dwordx4 a[220:223], v14, s[28:31], s95 offen offset:3072 sc1
s_barrier
mqa 76,228,116
tq 224,4,51200
mqa 68,228,112
tq 226,4,53248
mqa 80,232,112
tq 228,5,51200
mqa 88,232,116
tq 230,5,53248
mqa 92,236,116
tq 232,6,51200
mqa 84,236,112
tq 234,6,53248
s_waitcnt lgkmcnt(6)
mqa 64,240,120
tq 236,7,51200
mqa 72,240,124
tq 238,7,53248
mqa 76,244,124
tq 240,4,55296
mqa 68,244,120
tq 242,4,57344
mqa 80,248,120
tq 244,5,55296
mqa 88,248,124
tq 246,5,57344
mqa 92,252,124
tq 248,6,55296
mqa 84,252,120
tq 250,6,57344
s_waitcnt lgkmcnt(6)
s_waitcnt vmcnt(11)
mqa 64,224,144
tq 252,7,55296
s_add_u32 m0, s56, 100352
mqa 72,224,148
tq 254,7,57344
buffer_load_dwordx4 v30, s[72:75], s50 offen offset:0 lds
mqa 76,228,148
tq 224,8,10240
mqa 68,228,144
tq 226,8,12288
buffer_load_dwordx4 v31, s[72:75], s50 offen offset:1024 lds
mqa 80,232,144
tq 228,8,14336
mqa 88,232,148
tq 230,8,16384
mqa 92,236,148
tq 232,8,2048
mqa 84,236,144
tq 234,8,4096
s_waitcnt lgkmcnt(6)
mqa 64,240,152
tq 236,8,6144
mqa 72,240,156
tq 238,8,8192
mqa 76,244,156
tq 240,9,10240
mqa 68,244,152
tq 242,9,12288
mqa 80,248,152
tq 244,9,14336
mqa 88,248,156
tq 246,9,16384
mqa 92,252,156
tq 248,9,2048
mqa 84,252,152
tq 250,9,4096
s_waitcnt lgkmcnt(6)
s_waitcnt vmcnt(8)
mqa 64,224,176
tq 252,9,6144
tq 254,9,8192
mqa 72,224,180
s_waitcnt vmcnt(6)
s_barrier
mqa 72,228,188
tq 224,12,10240
mqa 64,228,184
tq 226,12,12288
s_waitcnt vmcnt(2)
mqa 64,232,208
tq 228,12,14336
s_add_u32 m0, s56, 116736
mqa 72,232,212
tq 230,12,16384
buffer_load_dwordx4 v30, s[76:79], s44 offen offset:0 lds
mqa 72,236,220
tq 232,12,2048
mqa 64,236,216
tq 234,12,4096
s_waitcnt lgkmcnt(6)
mqa 68,240,176
tq 236,12,6144
mqa 76,240,180
tq 238,12,8192
buffer_load_dwordx4 v31, s[76:79], s44 offen offset:1024 lds
mqa 76,244,188
tq 240,13,10240
v_fma_f32 v200, v136, v72, v200
v_fma_f32 v201, v137, v73, v201
mqa 68,244,184
tq 242,13,12288
v_fma_f32 v202, v138, v74, v202
mqa 68,248,208
tq 244,13,14336
v_fma_f32 v203, v139, v75, v203
v_fma_f32 v192, v128, v64, v192
mqa 76,248,212
tq 246,13,16384
v_fma_f32 v193, v129, v65, v193
mqa 76,252,220
tq 248,13,2048
v_fma_f32 v194, v130, v66, v194
v_fma_f32 v195, v131, v67, v195
mqa 68,252,216
tq 250,13,4096
s_waitcnt lgkmcnt(6)
mqa 80,224,176
tq 252,13,6144
s_add_u32 m0, s56, 2048
v_cvt_pk_bf16_f32 v36, v200, v201
mqa 88,224,180
tq 254,13,8192
buffer_load_dwordx4 v30, s[72:75], s54 offen offset:0 lds
mqa 88,228,188
tq 224,8,26624
v_cvt_pk_bf16_f32 v37, v202, v203
v_cvt_pk_bf16_f32 v32, v192, v193
mqa 80,228,184
tq 226,8,28672
buffer_load_dwordx4 v31, s[72:75], s54 offen offset:1024 lds
v_cvt_pk_bf16_f32 v33, v194, v195
mqa 80,232,208
tq 228,9,26624
s_add_u32 m0, s56, 18432
v_fma_f32 v204, v140, v76, v204
mqa 88,232,212
tq 230,9,28672
buffer_load_dwordx4 v30, s[72:75], s43 offen offset:0 lds
v_fma_f32 v205, v141, v77, v205
mqa 88,236,220
tq 232,12,26624
v_fma_f32 v206, v142, v78, v206
mqa 80,236,216
tq 234,12,28672
buffer_load_dwordx4 v31, s[72:75], s43 offen offset:1024 lds
s_waitcnt lgkmcnt(6)
mqa 84,240,176
tq 236,13,26624
v_fma_f32 v207, v143, v79, v207
v_fma_f32 v196, v132, v68, v196
mqa 92,240,180
tq 238,13,28672
v_fma_f32 v197, v133, v69, v197
mqa 92,244,188
tq 240,8,30720
v_fma_f32 v198, v134, v70, v198
v_fma_f32 v199, v135, v71, v199
mqa 84,244,184
tq 242,8,32768
v_cvt_pk_bf16_f32 v38, v204, v205
v_cvt_pk_bf16_f32 v39, v206, v207
mqa 84,248,208
tq 244,9,30720
mqa 92,248,212
tq 246,9,32768
buffer_store_dwordx4 v[36:39], v15, s[28:31], s80 offen offset:1024 sc1
v_cvt_pk_bf16_f32 v34, v196, v197
v_cvt_pk_bf16_f32 v35, v198, v199
buffer_store_dwordx4 v[32:35], v15, s[28:31], s80 offen offset:0 sc1
mqa 92,252,220
tq 248,12,30720
v_fma_f32 v216, v152, v88, v216
v_fma_f32 v217, v153, v89, v217
mqa 84,252,216
tq 250,12,32768
v_fma_f32 v218, v154, v90, v218
s_waitcnt lgkmcnt(6)
mqa 96,224,96
tq 252,13,30720
tq 254,13,32768
s_add_u32 m0, s56, 83968
mqa 104,224,100
buffer_load_dwordx4 v30, s[76:79], s48 offen offset:0 lds
s_waitcnt vmcnt(9)
s_barrier
mqa 108,228,100
tq 224,8,34816
v_fma_f32 v219, v155, v91, v219
v_fma_f32 v208, v144, v80, v208
mqa 100,228,96
tq 226,8,36864
v_fma_f32 v209, v145, v81, v209
v_fma_f32 v210, v146, v82, v210
mqa 112,232,96
tq 228,9,34816
v_fma_f32 v211, v147, v83, v211
v_cvt_pk_bf16_f32 v44, v216, v217
v_cvt_pk_bf16_f32 v45, v218, v219
mqa 120,232,100
tq 230,9,36864
buffer_load_dwordx4 v31, s[76:79], s48 offen offset:1024 lds
v_cvt_pk_bf16_f32 v40, v208, v209
mqa 124,236,100
tq 232,12,34816
v_cvt_pk_bf16_f32 v41, v210, v211
v_fma_f32 v220, v156, v92, v220
mqa 116,236,96
tq 234,12,36864
v_fma_f32 v221, v157, v93, v221
s_waitcnt lgkmcnt(6)
mqa 96,240,104
tq 236,13,34816
s_add_u32 m0, s56, 149504
v_fma_f32 v222, v158, v94, v222
mqa 104,240,108
tq 238,13,36864
buffer_load_dwordx4 v30, s[72:75], s45 offen offset:0 lds
v_fma_f32 v223, v159, v95, v223
mqa 108,244,108
tq 240,8,38912
v_fma_f32 v212, v148, v84, v212
v_fma_f32 v213, v149, v85, v213
mqa 100,244,104
tq 242,8,40960
v_fma_f32 v214, v150, v86, v214
v_fma_f32 v215, v151, v87, v215
mqa 112,248,104
tq 244,9,38912
v_cvt_pk_bf16_f32 v46, v220, v221
v_cvt_pk_bf16_f32 v47, v222, v223
mqa 120,248,108
tq 246,9,40960
buffer_load_dwordx4 v31, s[72:75], s45 offen offset:1024 lds
buffer_store_dwordx4 v[44:47], v15, s[28:31], s80 offen offset:3072 sc1
mqa 124,252,108
tq 248,12,38912
s_nop 0
L_hop5_3_%=:
s_cbranch_execz L_ret_3_%=
v_cvt_pk_bf16_f32 v42, v212, v213
v_cvt_pk_bf16_f32 v43, v214, v215
mqa 116,252,104
tq 250,12,40960
buffer_store_dwordx4 v[40:43], v15, s[28:31], s80 offen offset:2048 sc1
s_waitcnt lgkmcnt(6)
mqa 96,224,128
tq 252,13,38912
s_add_u32 m0, s56, 124928
mqa 104,224,132
tq 254,13,40960
buffer_load_dwordx4 v30, s[76:79], s52 offen offset:0 lds
mqa 108,228,132
tq 224,4,34816
ds_read_b128 v[64:67], v24 offset:1024
mqa 100,228,128
tq 226,4,36864
ds_read_b128 v[72:75], v24 offset:1024
mqa 112,232,128
tq 228,5,34816
ds_read_b128 v[68:71], v24 offset:1088
mqa 120,232,132
tq 230,5,36864
buffer_load_dwordx4 v31, s[76:79], s52 offen offset:1024 lds
mqa 124,236,132
tq 232,6,34816
s_add_u32 m0, s56, 133120
ds_read_b128 v[76:79], v24 offset:1088
mqa 116,236,128
tq 234,6,36864
buffer_load_dwordx4 v30, s[72:75], s49 offen offset:0 lds
s_waitcnt lgkmcnt(10)
mqa 96,240,136
tq 236,7,34816
mqa 104,240,140
tq 238,7,36864
buffer_load_dwordx4 v31, s[72:75], s49 offen offset:1024 lds
mqa 108,244,140
tq 240,4,38912
ds_read_b128 v[80:83], v24 offset:1152
mqa 100,244,136
tq 242,4,40960
mqa 112,248,136
tq 244,5,38912
s_add_u32 m0, s56, 43008
mqa 120,248,140
tq 246,5,40960
buffer_load_dwordx4 v30, s[72:75], s51 offen offset:0 lds
mqa 124,252,140
tq 248,6,38912
ds_read_b128 v[88:91], v24 offset:1152
mqa 116,252,136
tq 250,6,40960
s_waitcnt lgkmcnt(8)
mqa 96,224,160
tq 252,7,38912
tq 254,7,40960
mqa 104,224,164
s_waitcnt vmcnt(13)
s_barrier
mqa 108,228,164
tq 224,4,2048
ds_read_b128 v[84:87], v24 offset:1216
mqa 100,228,160
tq 226,4,4096
buffer_load_dwordx4 v31, s[72:75], s51 offen offset:1024 lds
mqa 112,232,160
tq 228,5,2048
s_add_u32 m0, s56, 59392
mqa 120,232,164
tq 230,5,4096
buffer_load_dwordx4 v30, s[72:75], s53 offen offset:0 lds
mqa 124,236,164
tq 232,6,2048
ds_read_b128 v[92:95], v24 offset:1216
mqa 116,236,160
tq 234,6,4096
s_waitcnt lgkmcnt(8)
mqa 96,240,168
tq 236,7,2048
mqa 104,240,172
tq 238,7,4096
buffer_load_dwordx4 v31, s[72:75], s53 offen offset:1024 lds
mqa 108,244,172
tq 240,4,6144
mqa 100,244,168
tq 242,4,8192
mqa 112,248,168
tq 244,5,6144
s_add_u32 m0, s56, 26624
mqa 120,248,172
tq 246,5,8192
buffer_load_dwordx4 v10, s[76:79], s40 offen offset:0 lds
mqa 124,252,172
tq 248,6,6144
mqa 116,252,168
tq 250,6,8192
s_waitcnt lgkmcnt(6)
mqa 96,224,192
tq 252,7,6144
mqa 104,224,196
tq 254,7,8192
buffer_load_dwordx4 v11, s[76:79], s40 offen offset:1024 lds
mqa 108,228,196
tq 224,4,18432
mqa 100,228,192
tq 226,4,20480
mqa 112,232,192
tq 228,5,18432
s_add_u32 m0, s56, 51200
mqa 120,232,196
tq 230,5,20480
buffer_load_dwordx4 v30, s[76:79], s40 offen offset:0 lds
mqa 124,236,196
tq 232,6,18432
mqa 116,236,192
tq 234,6,20480
s_waitcnt lgkmcnt(6)
mqa 96,240,200
tq 236,7,18432
mqa 104,240,204
tq 238,7,20480
buffer_load_dwordx4 v31, s[76:79], s40 offen offset:1024 lds
mqa 108,244,204
tq 240,4,22528
mqa 100,244,200
tq 242,4,24576
mqa 112,248,200
tq 244,5,22528
s_add_u32 m0, s56, 67584
mqa 120,248,204
tq 246,5,24576
buffer_load_dwordx4 v10, s[76:79], s41 offen offset:0 lds
mqa 124,252,204
tq 248,6,22528
mqa 116,252,200
tq 250,6,24576
s_waitcnt vmcnt(13)
buffer_store_dword v22, v21, s[28:31], 0 offen offset:64 sc1
s_waitcnt lgkmcnt(6)
mqa 96,224,16
tq 252,7,22528
tq 254,7,24576
mqa 104,224,20
s_barrier
mqa 108,228,20
tq 224,16,18432
mqa 100,228,16
tq 226,16,20480
buffer_load_dwordx4 v11, s[76:79], s41 offen offset:1024 lds
mqa 112,232,16
tq 228,17,18432
s_add_u32 m0, s56, 75776
mqa 120,232,20
tq 230,17,20480
buffer_load_dwordx4 v10, s[76:79], s45 offen offset:0 lds
mqa 124,236,20
tq 232,28,18432
mqa 116,236,16
tq 234,28,20480
s_waitcnt lgkmcnt(6)
mqa 96,240,24
tq 236,29,18432
mqa 104,240,28
tq 238,29,20480
buffer_load_dwordx4 v11, s[76:79], s45 offen offset:1024 lds
mqa 108,244,28
tq 240,16,22528
mqa 100,244,24
tq 242,16,24576
mqa 112,248,24
tq 244,17,22528
s_add_u32 m0, s56, 92160
mqa 120,248,28
tq 246,17,24576
buffer_load_dwordx4 v10, s[76:79], s42 offen offset:0 lds
mqa 124,252,28
tq 248,28,22528
mqa 116,252,24
tq 250,28,24576
s_waitcnt lgkmcnt(6)
mqa 96,224,48
tq 252,29,22528
mqa 104,224,52
tq 254,29,24576
buffer_load_dwordx4 v11, s[76:79], s42 offen offset:1024 lds
mqa 108,228,52
tq 224,8,43008
mqa 100,228,48
tq 226,8,45056
mqa 112,232,48
tq 228,9,43008
s_add_u32 m0, s56, 100352
mqa 120,232,52
tq 230,9,45056
buffer_load_dwordx4 v30, s[76:79], s42 offen offset:0 lds
mqa 124,236,52
tq 232,12,43008
mqa 116,236,48
tq 234,12,45056
s_waitcnt lgkmcnt(6)
mqa 96,240,56
tq 236,13,43008
mqa 104,240,60
tq 238,13,45056
buffer_load_dwordx4 v31, s[76:79], s42 offen offset:1024 lds
mqa 108,244,60
tq 240,8,47104
mqa 100,244,56
tq 242,8,49152
mqa 112,248,56
tq 244,9,47104
s_add_u32 m0, s56, 2048
mqa 120,248,60
tq 246,9,49152
buffer_load_dwordx4 v10, s[76:79], s44 offen offset:0 lds
mqa 124,252,60
tq 248,12,47104
mqa 116,252,56
tq 250,12,49152
s_waitcnt lgkmcnt(6)
mqa 96,224,80
tq 252,13,47104
tq 254,13,49152
mqa 104,224,84
s_waitcnt vmcnt(16)
s_barrier
mqa 108,228,84
tq 224,16,2048
mqa 100,228,80
tq 226,16,4096
buffer_load_dwordx4 v11, s[76:79], s44 offen offset:1024 lds
mqa 112,232,80
tq 228,17,2048
s_add_u32 m0, s56, 34816
mqa 120,232,84
tq 230,17,4096
buffer_load_dwordx4 v10, s[76:79], s46 offen offset:0 lds
mqa 124,236,84
tq 232,28,2048
mqa 116,236,80
tq 234,28,4096
s_waitcnt lgkmcnt(6)
mqa 96,240,88
tq 236,29,2048
mqa 104,240,92
tq 238,29,4096
buffer_load_dwordx4 v11, s[76:79], s46 offen offset:1024 lds
mqa 108,244,92
tq 240,16,6144
mqa 100,244,88
tq 242,16,8192
mqa 112,248,88
tq 244,17,6144
mqa 120,248,92
tq 246,17,8192
mqa 124,252,92
tq 248,28,6144
mqa 116,252,88
tq 250,28,8192
s_waitcnt lgkmcnt(6)
mqa 96,224,112
tq 252,29,6144
s_add_u32 m0, s56, 18432
mqa 104,224,116
tq 254,29,8192
buffer_load_dwordx4 v30, s[76:79], s46 offen offset:0 lds
mqa 108,228,116
tq 224,4,43008
mqa 100,228,112
tq 226,4,45056
buffer_load_dwordx4 v31, s[76:79], s46 offen offset:1024 lds
mqa 112,232,112
tq 228,5,43008
mqa 120,232,116
tq 230,5,45056
mqa 124,236,116
tq 232,6,43008
mqa 116,236,112
tq 234,6,45056
s_waitcnt lgkmcnt(6)
mqa 96,240,120
tq 236,7,43008
mqa 104,240,124
tq 238,7,45056
mqa 108,244,124
tq 240,4,47104
mqa 100,244,120
tq 242,4,49152
mqa 112,248,120
tq 244,5,47104
mqa 120,248,124
tq 246,5,49152
mqa 124,252,124
tq 248,6,47104
mqa 116,252,120
tq 250,6,49152
s_waitcnt lgkmcnt(6)
mqa 96,224,144
tq 252,7,47104
tq 254,7,49152
s_add_u32 m0, s56, 149504
mqa 104,224,148
buffer_load_dwordx4 v10, s[76:79], s48 offen offset:0 lds
s_waitcnt vmcnt(20)
s_barrier
mqa 108,228,148
tq 224,4,59392
mqa 100,228,144
tq 226,4,61440
buffer_load_dwordx4 v11, s[76:79], s48 offen offset:1024 lds
mqa 112,232,144
tq 228,4,63488
s_add_u32 m0, s56, 108544
mqa 120,232,148
tq 230,8,0
buffer_load_dwordx4 v10, s[76:79], s53 offen offset:0 lds
mqa 124,236,148
tq 232,4,10240
mqa 116,236,144
tq 234,4,12288
s_waitcnt lgkmcnt(6)
mqa 96,240,152
tq 236,4,14336
mqa 104,240,156
tq 238,4,16384
buffer_load_dwordx4 v11, s[76:79], s53 offen offset:1024 lds
mqa 108,244,156
tq 240,5,59392
mqa 100,244,152
tq 242,5,61440
mqa 112,248,152
tq 244,5,63488
mqa 120,248,156
tq 246,9,0
mqa 124,252,156
tq 248,5,10240
mqa 116,252,152
tq 250,5,12288
s_waitcnt lgkmcnt(6)
mqa 96,224,176
tq 252,5,14336
mqa 104,224,180
tq 254,5,16384
mqa 104,228,188
tq 224,6,59392
mqa 96,228,184
tq 226,6,61440
mqa 96,232,208
tq 228,6,63488
mqa 104,232,212
tq 230,12,0
mqa 104,236,220
tq 232,6,10240
mqa 96,236,216
tq 234,6,12288
s_waitcnt lgkmcnt(6)
mqa 100,240,176
tq 236,6,14336
mqa 108,240,180
tq 238,6,16384
mqa 108,244,188
tq 240,7,59392
v_fma_f32 v232, v168, v104, v232
v_fma_f32 v233, v169, v105, v233
mqa 100,244,184
tq 242,7,61440
v_fma_f32 v234, v170, v106, v234
mqa 100,248,208
tq 244,7,63488
v_fma_f32 v235, v171, v107, v235
v_fma_f32 v224, v160, v96, v224
mqa 108,248,212
tq 246,13,0
v_fma_f32 v225, v161, v97, v225
mqa 108,252,220
tq 248,7,10240
v_fma_f32 v226, v162, v98, v226
v_fma_f32 v227, v163, v99, v227
mqa 100,252,216
tq 250,7,12288
s_waitcnt lgkmcnt(6)
mqa 112,224,176
tq 252,7,14336
tq 254,7,16384
mqa 120,224,180
buffer_load_dword v23, v20, s[28:31], 0 offen offset:64 sc1
s_add_u32 m0, s56, 133120
s_nop 0
buffer_load_dwordx4 v10, s[76:79], s50 offen offset:0 lds
s_waitcnt vmcnt(21)
s_barrier
mqa 120,228,188
tq 224,4,26624
v_cvt_pk_bf16_f32 v52, v232, v233
v_cvt_pk_bf16_f32 v53, v234, v235
mqa 112,228,184
tq 226,4,28672
v_cvt_pk_bf16_f32 v48, v224, v225
mqa 112,232,208
tq 228,5,26624
v_cvt_pk_bf16_f32 v49, v226, v227
v_fma_f32 v236, v172, v108, v236
mqa 120,232,212
tq 230,5,28672
buffer_load_dwordx4 v11, s[76:79], s50 offen offset:1024 lds
mqa 120,236,220
tq 232,4,30720
v_fma_f32 v237, v173, v109, v237
v_fma_f32 v238, v174, v110, v238
mqa 112,236,216
tq 234,4,32768
s_waitcnt lgkmcnt(6)
mqa 116,240,176
tq 236,5,30720
v_fma_f32 v239, v175, v111, v239
v_fma_f32 v228, v164, v100, v228
mqa 124,240,180
tq 238,5,32768
v_fma_f32 v229, v165, v101, v229
mqa 124,244,188
tq 240,6,26624
v_fma_f32 v230, v166, v102, v230
v_fma_f32 v231, v167, v103, v231
mqa 116,244,184
tq 242,6,28672
v_cvt_pk_bf16_f32 v54, v236, v237
v_cvt_pk_bf16_f32 v55, v238, v239
mqa 116,248,208
tq 244,7,26624
mqa 124,248,212
tq 246,7,28672
buffer_store_dwordx4 v[52:55], v15, s[28:31], s81 offen offset:1024 sc1
v_cvt_pk_bf16_f32 v50, v228, v229
v_cvt_pk_bf16_f32 v51, v230, v231
buffer_store_dwordx4 v[48:51], v15, s[28:31], s81 offen offset:0 sc1
mqa 124,252,220
tq 248,6,30720
ds_read_b128 v[96:99], v24 offset:1280
v_fma_f32 v248, v184, v120, v248
v_fma_f32 v249, v185, v121, v249
mqa 116,252,216
tq 250,6,32768
s_waitcnt lgkmcnt(7)
mqv 64,224,32
tq 252,7,30720
s_add_u32 m0, s56, 43008
v_fma_f32 v250, v186, v122, v250
mqv 72,224,36
tq 254,7,32768
buffer_load_dwordx4 v30, s[76:79], s50 offen offset:0 lds
v_fma_f32 v251, v187, v123, v251
mqv 76,228,36
tq 224,4,51200
ds_read_b128 v[104:107], v24 offset:1280
v_fma_f32 v240, v176, v112, v240
mqv 68,228,32
tq 226,4,53248
v_fma_f32 v241, v177, v113, v241
v_fma_f32 v242, v178, v114, v242
mqv 64,232,40
tq 228,5,51200
v_fma_f32 v243, v179, v115, v243
v_cvt_pk_bf16_f32 v60, v248, v249
v_cvt_pk_bf16_f32 v61, v250, v251
mqv 72,232,44
tq 230,5,53248
buffer_load_dwordx4 v31, s[76:79], s50 offen offset:1024 lds
ds_read_b128 v[100:103], v24 offset:1344
mqv 76,236,44
tq 232,4,55296
v_cvt_pk_bf16_f32 v56, v240, v241
v_cvt_pk_bf16_f32 v57, v242, v243
ds_read_b128 v[108:111], v24 offset:1344
mqv 68,236,40
tq 234,4,57344
v_fma_f32 v252, v188, v124, v252
s_waitcnt lgkmcnt(9)
mqv 80,240,32
tq 236,5,55296
v_fma_f32 v253, v189, v125, v253
v_fma_f32 v254, v190, v126, v254
mqv 88,240,36
tq 238,5,57344
v_fma_f32 v255, v191, v127, v255
ds_read_b128 v[112:115], v24 offset:1408
mqv 92,244,36
tq 240,6,51200
v_fma_f32 v244, v180, v116, v244
v_fma_f32 v245, v181, v117, v245
mqv 84,244,32
tq 242,6,53248
v_fma_f32 v246, v182, v118, v246
v_fma_f32 v247, v183, v119, v247
mqv 80,248,40
tq 244,7,51200
v_cvt_pk_bf16_f32 v62, v252, v253
v_cvt_pk_bf16_f32 v63, v254, v255
mqv 88,248,44
tq 246,7,53248
buffer_store_dwordx4 v[60:63], v15, s[28:31], s81 offen offset:3072 sc1
ds_read_b128 v[120:123], v24 offset:1408
mqv 92,252,44
tq 248,6,55296
v_cvt_pk_bf16_f32 v58, v244, v245
v_cvt_pk_bf16_f32 v59, v246, v247
mqv 84,252,40
tq 250,6,57344
buffer_store_dwordx4 v[56:59], v15, s[28:31], s81 offen offset:2048 sc1
ds_read_b128 v[116:119], v24 offset:1472
s_waitcnt lgkmcnt(9)
mqv 96,224,32
tq 252,7,55296
tq 254,7,57344
ds_read_b128 v[124:127], v24 offset:1472
mqv 104,224,36
s_waitcnt vmcnt(8)
v_cmp_ne_u32 vcc, v23, v22
s_cbranch_vccnz L_slow2_3_%=
L_back2_3_%=:
buffer_load_dwordx4 a[0:3], v15, s[28:31], s82 offen offset:0 sc1
s_barrier
mqv 108,228,36
tq 224,8,2048
mqv 100,228,32
tq 226,8,4096
buffer_load_dwordx4 a[4:7], v15, s[28:31], s82 offen offset:1024 sc1
mqv 96,232,40
tq 228,9,2048
mqv 104,232,44
tq 230,9,4096
s_nop 0
L_hop6_3_%=:
s_cbranch_execz L_ret_3_%=
buffer_load_dwordx4 a[8:11], v15, s[28:31], s82 offen offset:2048 sc1
mqv 108,236,44
tq 232,12,2048
mqv 100,236,40
tq 234,12,4096
buffer_load_dwordx4 a[12:15], v15, s[28:31], s82 offen offset:3072 sc1
s_waitcnt lgkmcnt(6)
mqv 112,240,32
tq 236,13,2048
mqv 120,240,36
tq 238,13,4096
mqv 124,244,36
tq 240,8,6144
mqv 116,244,32
tq 242,8,8192
mqv 112,248,40
tq 244,9,6144
s_add_u32 m0, s56, 10240
mqv 120,248,44
tq 246,9,8192
buffer_load_dwordx4 v30, s[76:79], s47 offen offset:0 lds
mqv 124,252,44
tq 248,12,6144
mqv 116,252,40
tq 250,12,8192
s_waitcnt lgkmcnt(6)
mqv 64,224,48
tq 252,13,6144
mqv 72,224,52
tq 254,13,8192
buffer_load_dwordx4 v31, s[76:79], s47 offen offset:1024 lds
mqv 76,228,52
tq 224,16,10240
mqv 68,228,48
tq 226,16,12288
mqv 80,232,48
tq 228,17,10240
mqv 88,232,52
tq 230,17,12288
buffer_load_dwordx4 a[32:35], v15, s[28:31], s84 offen offset:0 sc1
mqv 92,236,52
tq 232,28,10240
mqv 84,236,48
tq 234,28,12288
s_waitcnt lgkmcnt(6)
mqv 64,240,56
tq 236,29,10240
mqv 72,240,60
tq 238,29,12288
buffer_load_dwordx4 a[36:39], v15, s[28:31], s84 offen offset:1024 sc1
mqv 76,244,60
tq 240,16,14336
mqv 68,244,56
tq 242,16,16384
mqv 80,248,56
tq 244,17,14336
mqv 88,248,60
tq 246,17,16384
buffer_load_dwordx4 a[40:43], v15, s[28:31], s84 offen offset:2048 sc1
mqv 92,252,60
tq 248,28,14336
mqv 84,252,56
tq 250,28,16384
buffer_load_dwordx4 a[44:47], v15, s[28:31], s84 offen offset:3072 sc1
s_waitcnt lgkmcnt(6)
mqv 96,224,48
tq 252,29,14336
tq 254,29,16384
mqv 104,224,52
s_barrier
mqv 108,228,52
tq 224,8,26624
mqv 100,228,48
tq 226,8,28672
mqv 112,232,48
tq 228,9,26624
mqv 120,232,52
tq 230,9,28672
buffer_load_dwordx4 a[64:67], v15, s[28:31], s86 offen offset:0 sc1
mqv 124,236,52
tq 232,12,26624
mqv 116,236,48
tq 234,12,28672
s_waitcnt lgkmcnt(6)
mqv 96,240,56
tq 236,13,26624
mqv 104,240,60
tq 238,13,28672
buffer_load_dwordx4 a[68:71], v15, s[28:31], s86 offen offset:1024 sc1
mqv 108,244,60
tq 240,8,30720
mqv 100,244,56
tq 242,8,32768
mqv 112,248,56
tq 244,9,30720
mqv 120,248,60
tq 246,9,32768
buffer_load_dwordx4 a[72:75], v15, s[28:31], s86 offen offset:2048 sc1
mqv 124,252,60
tq 248,12,30720
mqv 116,252,56
tq 250,12,32768
s_waitcnt vmcnt(13)
buffer_store_dword v22, v21, s[28:31], 0 offen offset:96 sc1
s_waitcnt lgkmcnt(6)
s_waitcnt vmcnt(10)
mqa 64,224,0
tq 252,13,30720
mqa 72,224,4
tq 254,13,32768
buffer_load_dwordx4 a[76:79], v15, s[28:31], s86 offen offset:3072 sc1
mqa 76,228,4
tq 224,8,34816
mqa 68,228,0
tq 226,8,36864
mqa 80,232,0
tq 228,9,34816
mqa 88,232,4
tq 230,9,36864
buffer_load_dwordx4 a[96:99], v15, s[28:31], s88 offen offset:0 sc1
mqa 92,236,4
tq 232,12,34816
mqa 84,236,0
tq 234,12,36864
s_waitcnt lgkmcnt(6)
mqa 64,240,8
tq 236,13,34816
mqa 72,240,12
tq 238,13,36864
buffer_load_dwordx4 a[100:103], v15, s[28:31], s88 offen offset:1024 sc1
mqa 76,244,12
tq 240,8,38912
mqa 68,244,8
tq 242,8,40960
mqa 80,248,8
tq 244,9,38912
mqa 88,248,12
tq 246,9,40960
buffer_load_dwordx4 a[104:107], v15, s[28:31], s88 offen offset:2048 sc1
mqa 92,252,12
tq 248,12,38912
mqa 84,252,8
tq 250,12,40960
s_waitcnt lgkmcnt(6)
mqa 96,224,0
tq 252,13,38912
tq 254,13,40960
mqa 104,224,4
s_barrier
mqa 108,228,4
tq 224,4,2048
mqa 100,228,0
tq 226,4,4096
buffer_load_dwordx4 a[108:111], v15, s[28:31], s88 offen offset:3072 sc1
mqa 112,232,0
tq 228,5,2048
s_add_u32 m0, s56, 26624
mqa 120,232,4
tq 230,5,4096
buffer_load_dwordx4 v30, s[76:79], s55 offen offset:0 lds
mqa 124,236,4
tq 232,6,2048
mqa 116,236,0
tq 234,6,4096
s_waitcnt lgkmcnt(6)
mqa 96,240,8
tq 236,7,2048
mqa 104,240,12
tq 238,7,4096
buffer_load_dwordx4 v31, s[76:79], s55 offen offset:1024 lds
mqa 108,244,12
tq 240,4,6144
mqa 100,244,8
tq 242,4,8192
mqa 112,248,8
tq 244,5,6144
mqa 120,248,12
tq 246,5,8192
buffer_load_dwordx4 a[128:131], v15, s[28:31], s90 offen offset:0 sc1
mqa 124,252,12
tq 248,6,6144
mqa 116,252,8
tq 250,6,8192
s_waitcnt lgkmcnt(6)
s_waitcnt vmcnt(12)
mqa 64,224,32
tq 252,7,6144
mqa 72,224,36
tq 254,7,8192
buffer_load_dwordx4 a[132:135], v15, s[28:31], s90 offen offset:1024 sc1
mqa 76,228,36
tq 224,8,51200
mqa 68,228,32
tq 226,8,53248
mqa 80,232,32
tq 228,9,51200
mqa 88,232,36
tq 230,9,53248
buffer_load_dwordx4 a[136:139], v15, s[28:31], s90 offen offset:2048 sc1
mqa 92,236,36
tq 232,12,51200
mqa 84,236,32
tq 234,12,53248
s_waitcnt lgkmcnt(6)
mqa 64,240,40
tq 236,13,51200
mqa 72,240,44
tq 238,13,53248
buffer_load_dwordx4 a[140:143], v15, s[28:31], s90 offen offset:3072 sc1
mqa 76,244,44
tq 240,8,55296
mqa 68,244,40
tq 242,8,57344
mqa 80,248,40
tq 244,9,55296
s_add_u32 m0, s56, 59392
mqa 88,248,44
tq 246,9,57344
buffer_load_dwordx4 v10, s[76:79], s52 offen offset:0 lds
mqa 92,252,44
tq 248,12,55296
mqa 84,252,40
tq 250,12,57344
s_waitcnt lgkmcnt(6)
mqa 96,224,32
tq 252,13,55296
tq 254,13,57344
mqa 104,224,36
s_barrier
mqa 108,228,36
tq 224,4,34816
mqa 100,228,32
tq 226,4,36864
buffer_load_dwordx4 v11, s[76:79], s52 offen offset:1024 lds
mqa 112,232,32
tq 228,5,34816
mqa 120,232,36
tq 230,5,36864
buffer_load_dwordx4 a[160:163], v15, s[28:31], s92 offen offset:0 sc1
mqa 124,236,36
tq 232,6,34816
mqa 116,236,32
tq 234,6,36864
s_waitcnt lgkmcnt(6)
mqa 96,240,40
tq 236,7,34816
mqa 104,240,44
tq 238,7,36864
buffer_load_dwordx4 a[164:167], v15, s[28:31], s92 offen offset:1024 sc1
mqa 108,244,44
tq 240,4,38912
mqa 100,244,40
tq 242,4,40960
mqa 112,248,40
tq 244,5,38912
mqa 120,248,44
tq 246,5,40960
buffer_load_dwordx4 a[168:171], v15, s[28:31], s92 offen offset:2048 sc1
mqa 124,252,44
tq 248,6,38912
mqa 116,252,40
tq 250,6,40960
s_waitcnt lgkmcnt(6)
s_waitcnt vmcnt(15)
mqa 64,224,64
tq 252,7,38912
mqa 72,224,68
tq 254,7,40960
buffer_load_dwordx4 a[172:175], v15, s[28:31], s92 offen offset:3072 sc1
mqa 76,228,68
tq 224,4,18432
mqa 68,228,64
tq 226,4,20480
mqa 80,232,64
tq 228,5,18432
s_add_u32 m0, s56, 51200
mqa 88,232,68
tq 230,5,20480
buffer_load_dwordx4 v10, s[76:79], s54 offen offset:0 lds
mqa 92,236,68
tq 232,6,18432
mqa 84,236,64
tq 234,6,20480
s_waitcnt lgkmcnt(6)
mqa 64,240,72
tq 236,7,18432
mqa 72,240,76
tq 238,7,20480
buffer_load_dwordx4 v11, s[76:79], s54 offen offset:1024 lds
mqa 76,244,76
tq 240,4,22528
mqa 68,244,72
tq 242,4,24576
mqa 80,248,72
tq 244,5,22528
s_add_u32 m0, s56, 67584
mqa 88,248,76
tq 246,5,24576
buffer_load_dwordx4 v30, s[76:79], s54 offen offset:0 lds
mqa 92,252,76
tq 248,6,22528
mqa 84,252,72
tq 250,6,24576
s_waitcnt lgkmcnt(6)
mqa 96,224,64
tq 252,7,22528
tq 254,7,24576
mqa 104,224,68
s_barrier
mqa 108,228,68
tq 224,16,18432
mqa 100,228,64
tq 226,16,20480
buffer_load_dwordx4 v31, s[76:79], s54 offen offset:1024 lds
mqa 112,232,64
tq 228,17,18432
mqa 120,232,68
tq 230,17,20480
buffer_load_dwordx4 a[192:195], v15, s[28:31], s94 offen offset:0 sc1
mqa 124,236,68
tq 232,28,18432
mqa 116,236,64
tq 234,28,20480
s_waitcnt lgkmcnt(6)
mqa 96,240,72
tq 236,29,18432
mqa 104,240,76
tq 238,29,20480
buffer_load_dwordx4 a[196:199], v15, s[28:31], s94 offen offset:1024 sc1
mqa 108,244,76
tq 240,16,22528
mqa 100,244,72
tq 242,16,24576
mqa 112,248,72
tq 244,17,22528
mqa 120,248,76
tq 246,17,24576
buffer_load_dwordx4 a[200:203], v15, s[28:31], s94 offen offset:2048 sc1
mqa 124,252,76
tq 248,28,22528
mqa 116,252,72
tq 250,28,24576
s_waitcnt lgkmcnt(6)
s_waitcnt vmcnt(19)
mqa 64,224,96
tq 252,29,22528
mqa 72,224,100
tq 254,29,24576
buffer_load_dwordx4 a[204:207], v15, s[28:31], s94 offen offset:3072 sc1
mqa 76,228,100
tq 224,8,18432
mqa 68,228,96
tq 226,8,20480
mqa 80,232,96
tq 228,9,18432
s_add_u32 m0, s56, 92160
mqa 88,232,100
tq 230,9,20480
buffer_load_dwordx4 v10, s[76:79], s43 offen offset:0 lds
mqa 92,236,100
tq 232,12,18432
mqa 84,236,96
tq 234,12,20480
s_waitcnt lgkmcnt(6)
mqa 64,240,104
tq 236,13,18432
mqa 72,240,108
tq 238,13,20480
buffer_load_dwordx4 v11, s[76:79], s43 offen offset:1024 lds
mqa 76,244,108
tq 240,8,22528
mqa 68,244,104
tq 242,8,24576
mqa 80,248,104
tq 244,9,22528
s_add_u32 m0, s56, 141312
mqa 88,248,108
tq 246,9,24576
buffer_load_dwordx4 v10, s[76:79], s47 offen offset:0 lds
mqa 92,252,108
tq 248,12,22528
mqa 84,252,104
tq 250,12,24576
s_waitcnt lgkmcnt(6)
mqa 96,224,96
tq 252,13,22528
tq 254,13,24576
mqa 104,224,100
buffer_load_dword v23, v20, s[28:31], 0 offen offset:96 sc1
s_barrier
mqa 108,228,100
tq 224,16,2048
mqa 100,228,96
tq 226,16,4096
buffer_load_dwordx4 v11, s[76:79], s47 offen offset:1024 lds
mqa 112,232,96
tq 228,17,2048
s_add_u32 m0, s56, 2048
mqa 120,232,100
tq 230,17,4096
buffer_load_dwordx4 v10, s[76:79], s49 offen offset:0 lds
mqa 124,236,100
tq 232,28,2048
mqa 116,236,96
tq 234,28,4096
s_waitcnt lgkmcnt(6)
mqa 96,240,104
tq 236,29,2048
mqa 104,240,108
tq 238,29,4096
buffer_load_dwordx4 v11, s[76:79], s49 offen offset:1024 lds
mqa 108,244,108
tq 240,16,6144
mqa 100,244,104
tq 242,16,8192
mqa 112,248,104
tq 244,17,6144
s_add_u32 m0, s56, 100352
mqa 120,248,108
tq 246,17,8192
buffer_load_dwordx4 v10, s[76:79], s51 offen offset:0 lds
mqa 124,252,108
tq 248,28,6144
mqa 116,252,104
tq 250,28,8192
s_waitcnt lgkmcnt(6)
s_waitcnt vmcnt(22)
mqa 64,224,128
tq 252,29,6144
mqa 72,224,132
tq 254,29,8192
buffer_load_dwordx4 v11, s[76:79], s51 offen offset:1024 lds
mqa 76,228,132
tq 224,4,43008
mqa 68,228,128
tq 226,4,45056
mqa 80,232,128
tq 228,5,43008
s_add_u32 m0, s56, 34816
mqa 88,232,132
tq 230,5,45056
buffer_load_dwordx4 v10, s[76:79], s55 offen offset:0 lds
mqa 92,236,132
tq 232,6,43008
mqa 84,236,128
tq 234,6,45056
s_waitcnt lgkmcnt(6)
mqa 64,240,136
tq 236,7,43008
mqa 72,240,140
tq 238,7,45056
buffer_load_dwordx4 v11, s[76:79], s55 offen offset:1024 lds
mqa 76,244,140
tq 240,4,47104
mqa 68,244,136
tq 242,4,49152
mqa 80,248,136
tq 244,5,47104
s_add_u32 m0, s56, 116736
mqa 88,248,140
tq 246,5,49152
buffer_load_dwordx4 v30, s[76:79], s43 offen offset:0 lds
mqa 92,252,140
tq 248,6,47104
mqa 84,252,136
tq 250,6,49152
s_waitcnt lgkmcnt(6)
mqa 96,224,128
tq 252,7,47104
tq 254,7,49152
mqa 104,224,132
s_waitcnt vmcnt(8)
v_cmp_ne_u32 vcc, v23, v22
s_cbranch_vccnz L_slow3_3_%=
L_back3_3_%=:
s_barrier
mqa 108,228,132
tq 224,4,59392
mqa 100,228,128
tq 226,4,61440
buffer_load_dwordx4 v31, s[76:79], s43 offen offset:1024 lds
mqa 112,232,128
tq 228,5,59392
mqa 120,232,132
tq 230,5,61440
buffer_load_dwordx4 a[16:19], v15, s[28:31], s83 offen offset:0 sc1
mqa 124,236,132
tq 232,6,59392
mqa 116,236,128
tq 234,6,61440
s_waitcnt lgkmcnt(6)
mqa 96,240,136
tq 236,7,59392
mqa 104,240,140
tq 238,7,61440
buffer_load_dwordx4 a[20:23], v15, s[28:31], s83 offen offset:1024 sc1
mqa 108,244,140
tq 240,4,63488
mqa 100,244,136
tq 242,8,0
mqa 112,248,136
tq 244,5,63488
mqa 120,248,140
tq 246,9,0
buffer_load_dwordx4 a[24:27], v15, s[28:31], s83 offen offset:2048 sc1
mqa 124,252,140
tq 248,6,63488
mqa 116,252,136
tq 250,12,0
s_waitcnt lgkmcnt(6)
mqa 64,224,160
tq 252,7,63488
mqa 72,224,164
tq 254,13,0
buffer_load_dwordx4 a[28:31], v15, s[28:31], s83 offen offset:3072 sc1
mqa 76,228,164
tq 224,8,59392
mqa 68,228,160
tq 226,8,61440
mqa 80,232,160
tq 228,9,59392
mqa 88,232,164
tq 230,9,61440
buffer_load_dwordx4 a[48:51], v15, s[28:31], s85 offen offset:0 sc1
mqa 92,236,164
tq 232,12,59392
mqa 84,236,160
tq 234,12,61440
s_waitcnt lgkmcnt(6)
mqa 64,240,168
tq 236,13,59392
mqa 72,240,172
tq 238,13,61440
s_nop 0
L_hop7_3_%=:
s_cbranch_execz L_ret_3_%=
buffer_load_dwordx4 a[52:55], v15, s[28:31], s85 offen offset:1024 sc1
mqa 76,244,172
tq 240,8,63488
mqa 68,244,168
tq 242,16,0
mqa 80,248,168
tq 244,9,63488
mqa 88,248,172
tq 246,17,0
buffer_load_dwordx4 a[56:59], v15, s[28:31], s85 offen offset:2048 sc1
mqa 92,252,172
tq 248,12,63488
mqa 84,252,168
tq 250,28,0
s_waitcnt lgkmcnt(6)
mqa 96,224,160
tq 252,13,63488
tq 254,29,0
mqa 104,224,164
s_barrier
mqa 108,228,164
tq 224,4,51200
mqa 100,228,160
tq 226,4,53248
buffer_load_dwordx4 a[60:63], v15, s[28:31], s85 offen offset:3072 sc1
mqa 112,232,160
tq 228,5,51200
mqa 120,232,164
tq 230,5,53248
buffer_load_dwordx4 a[80:83], v15, s[28:31], s87 offen offset:0 sc1
mqa 124,236,164
tq 232,6,51200
mqa 116,236,160
tq 234,6,53248
s_waitcnt lgkmcnt(6)
mqa 96,240,168
tq 236,7,51200
mqa 104,240,172
tq 238,7,53248
buffer_load_dwordx4 a[84:87], v15, s[28:31], s87 offen offset:1024 sc1
mqa 108,244,172
tq 240,4,55296
mqa 100,244,168
tq 242,4,57344
mqa 112,248,168
tq 244,5,55296
mqa 120,248,172
tq 246,5,57344
buffer_load_dwordx4 a[88:91], v15, s[28:31], s87 offen offset:2048 sc1
mqa 124,252,172
tq 248,6,55296
mqa 116,252,168
tq 250,6,57344
s_waitcnt lgkmcnt(6)
mqa 64,224,192
tq 252,7,55296
mqa 72,224,196
tq 254,7,57344
buffer_load_dwordx4 a[92:95], v15, s[28:31], s87 offen offset:3072 sc1
mqa 76,228,196
tq 224,8,2048
mqa 68,228,192
tq 226,8,4096
mqa 80,232,192
tq 228,9,2048
mqa 88,232,196
tq 230,9,4096
buffer_load_dwordx4 a[112:115], v15, s[28:31], s89 offen offset:0 sc1
mqa 92,236,196
tq 232,12,2048
mqa 84,236,192
tq 234,12,4096
s_waitcnt lgkmcnt(6)
mqa 64,240,200
tq 236,13,2048
mqa 72,240,204
tq 238,13,4096
buffer_load_dwordx4 a[116:119], v15, s[28:31], s89 offen offset:1024 sc1
mqa 76,244,204
tq 240,8,6144
mqa 68,244,200
tq 242,8,8192
mqa 80,248,200
tq 244,9,6144
mqa 88,248,204
tq 246,9,8192
buffer_load_dwordx4 a[120:123], v15, s[28:31], s89 offen offset:2048 sc1
mqa 92,252,204
tq 248,12,6144
mqa 84,252,200
tq 250,12,8192
s_waitcnt lgkmcnt(6)
mqa 96,224,192
tq 252,13,6144
tq 254,13,8192
mqa 104,224,196
s_barrier
mqa 108,228,196
tq 224,8,26624
mqa 100,228,192
tq 226,8,28672
buffer_load_dwordx4 a[124:127], v15, s[28:31], s89 offen offset:3072 sc1
mqa 112,232,192
tq 228,9,26624
mqa 120,232,196
tq 230,9,28672
buffer_load_dwordx4 a[144:147], v15, s[28:31], s91 offen offset:0 sc1
mqa 124,236,196
tq 232,12,26624
mqa 116,236,192
tq 234,12,28672
s_waitcnt lgkmcnt(6)
mqa 96,240,200
tq 236,13,26624
mqa 104,240,204
tq 238,13,28672
buffer_load_dwordx4 a[148:151], v15, s[28:31], s91 offen offset:1024 sc1
mqa 108,244,204
tq 240,8,30720
mqa 100,244,200
tq 242,8,32768
mqa 112,248,200
tq 244,9,30720
mqa 120,248,204
tq 246,9,32768
buffer_load_dwordx4 a[152:155], v15, s[28:31], s91 offen offset:2048 sc1
mqa 124,252,204
tq 248,12,30720
mqa 116,252,200
tq 250,12,32768
s_waitcnt lgkmcnt(6)
s_waitcnt vmcnt(15)
mqa 64,224,16
tq 252,13,30720
mqa 72,224,20
tq 254,13,32768
buffer_load_dwordx4 a[156:159], v15, s[28:31], s91 offen offset:3072 sc1
mqa 76,228,20
tq 224,8,10240
mqa 68,228,16
tq 226,8,12288
mqa 80,232,16
tq 228,9,10240
s_add_u32 m0, s56, 18432
mqa 88,232,20
tq 230,9,12288
buffer_load_dwordx4 v30, s[76:79], s45 offen offset:0 lds
mqa 92,236,20
tq 232,12,10240
mqa 84,236,16
tq 234,12,12288
s_waitcnt lgkmcnt(6)
mqa 64,240,24
tq 236,13,10240
mqa 72,240,28
tq 238,13,12288
buffer_load_dwordx4 v31, s[76:79], s45 offen offset:1024 lds
mqa 76,244,28
tq 240,8,14336
mqa 68,244,24
tq 242,8,16384
mqa 80,248,24
tq 244,9,14336
s_add_u32 m0, s56, 149504
mqa 88,248,28
tq 246,9,16384
buffer_load_dwordx4 v30, s[76:79], s49 offen offset:0 lds
mqa 92,252,28
tq 248,12,14336
mqa 84,252,24
tq 250,12,16384
s_waitcnt lgkmcnt(6)
s_waitcnt vmcnt(15)
)ASM"
R"ASM(
mqa 64,224,48
tq 252,13,14336
tq 254,13,16384
mqa 72,224,52
s_barrier
mqa 76,228,52
tq 224,16,10240
mqa 68,228,48
tq 226,16,12288
buffer_load_dwordx4 v31, s[76:79], s49 offen offset:1024 lds
mqa 80,232,48
tq 228,17,10240
mqa 88,232,52
tq 230,17,12288
buffer_load_dwordx4 a[176:179], v15, s[28:31], s93 offen offset:0 sc1
mqa 92,236,52
tq 232,28,10240
mqa 84,236,48
tq 234,28,12288
s_waitcnt lgkmcnt(6)
mqa 64,240,56
tq 236,29,10240
mqa 72,240,60
tq 238,29,12288
buffer_load_dwordx4 a[180:183], v15, s[28:31], s93 offen offset:1024 sc1
mqa 76,244,60
tq 240,16,14336
mqa 68,244,56
tq 242,16,16384
mqa 80,248,56
tq 244,17,14336
mqa 88,248,60
tq 246,17,16384
buffer_load_dwordx4 a[184:187], v15, s[28:31], s93 offen offset:2048 sc1
mqa 92,252,60
tq 248,28,14336
mqa 84,252,56
tq 250,28,16384
buffer_load_dwordx4 a[188:191], v15, s[28:31], s93 offen offset:3072 sc1
s_waitcnt lgkmcnt(6)
s_waitcnt vmcnt(16)
mqa 64,224,80
tq 252,29,14336
mqa 72,224,84
tq 254,29,16384
mqa 76,228,84
tq 224,4,2048
mqa 68,228,80
tq 226,4,4096
buffer_load_dwordx4 a[208:211], v15, s[28:31], s95 offen offset:0 sc1
mqa 80,232,80
tq 228,5,2048
mqa 88,232,84
tq 230,5,4096
buffer_load_dwordx4 a[212:215], v15, s[28:31], s95 offen offset:1024 sc1
mqa 92,236,84
tq 232,6,2048
mqa 84,236,80
tq 234,6,4096
buffer_load_dwordx4 a[216:219], v15, s[28:31], s95 offen offset:2048 sc1
s_waitcnt lgkmcnt(6)
mqa 64,240,88
tq 236,7,2048
mqa 72,240,92
tq 238,7,4096
buffer_load_dwordx4 a[220:223], v15, s[28:31], s95 offen offset:3072 sc1
mqa 76,244,92
tq 240,4,6144
mqa 68,244,88
tq 242,4,8192
mqa 80,248,88
tq 244,5,6144
mqa 88,248,92
tq 246,5,8192
mqa 92,252,92
tq 248,6,6144
mqa 84,252,88
tq 250,6,8192
s_waitcnt lgkmcnt(6)
s_waitcnt vmcnt(16)
mqa 64,224,112
tq 252,7,6144
tq 254,7,8192
mqa 72,224,116
s_barrier
mqa 76,228,116
tq 224,8,34816
mqa 68,228,112
tq 226,8,36864
mqa 80,232,112
tq 228,9,34816
s_add_u32 m0, s56, 83968
mqa 88,232,116
tq 230,9,36864
buffer_load_dwordx4 v30, s[76:79], s51 offen offset:0 lds
mqa 92,236,116
tq 232,12,34816
mqa 84,236,112
tq 234,12,36864
s_waitcnt lgkmcnt(6)
mqa 64,240,120
tq 236,13,34816
mqa 72,240,124
tq 238,13,36864
buffer_load_dwordx4 v31, s[76:79], s51 offen offset:1024 lds
mqa 76,244,124
tq 240,8,38912
mqa 68,244,120
tq 242,8,40960
mqa 80,248,120
tq 244,9,38912
s_add_u32 m0, s56, 133120
mqa 88,248,124
tq 246,9,40960
buffer_load_dwordx4 v30, s[76:79], s53 offen offset:0 lds
mqa 92,252,124
tq 248,12,38912
mqa 84,252,120
tq 250,12,40960
s_waitcnt lgkmcnt(6)
s_waitcnt vmcnt(15)
mqa 64,224,144
tq 252,13,38912
mqa 72,224,148
tq 254,13,40960
buffer_load_dwordx4 v31, s[76:79], s53 offen offset:1024 lds
mqa 76,228,148
tq 224,8,43008
mqa 68,228,144
tq 226,8,45056
mqa 80,232,144
tq 228,8,47104
mqa 88,232,148
tq 230,8,49152
mqa 92,236,148
tq 232,4,34816
mqa 84,236,144
tq 234,4,36864
s_waitcnt lgkmcnt(6)
mqa 64,240,152
tq 236,4,38912
mqa 72,240,156
tq 238,4,40960
mqa 76,244,156
tq 240,9,43008
mqa 68,244,152
tq 242,9,45056
mqa 80,248,152
tq 244,9,47104
mqa 88,248,156
tq 246,9,49152
mqa 92,252,156
tq 248,5,34816
mqa 84,252,152
tq 250,5,36864
s_waitcnt lgkmcnt(6)
s_waitcnt vmcnt(8)
mqa 64,224,176
tq 252,5,38912
tq 254,5,40960
mqa 72,224,180
s_barrier
mqa 72,228,188
tq 224,12,43008
mqa 64,228,184
tq 226,12,45056
s_waitcnt vmcnt(4)
mqa 64,232,208
tq 228,12,47104
mqa 72,232,212
tq 230,12,49152
mqa 72,236,220
tq 232,6,34816
mqa 64,236,216
tq 234,6,36864
s_waitcnt lgkmcnt(6)
mqa 68,240,176
tq 236,6,38912
mqa 76,240,180
tq 238,6,40960
mqa 76,244,188
tq 240,13,43008
v_fma_f32 v200, v136, v72, v200
mqa 68,244,184
tq 242,13,45056
v_fma_f32 v201, v137, v73, v201
mqa 68,248,208
tq 244,13,47104
v_fma_f32 v202, v138, v74, v202
mqa 76,248,212
tq 246,13,49152
v_fma_f32 v203, v139, v75, v203
mqa 76,252,220
tq 248,7,34816
v_fma_f32 v192, v128, v64, v192
mqa 68,252,216
tq 250,7,36864
s_waitcnt lgkmcnt(6)
mqa 80,224,176
tq 252,7,38912
v_fma_f32 v193, v129, v65, v193
mqa 88,224,180
tq 254,7,40960
v_fma_f32 v194, v130, v66, v194
mqa 88,228,188
tq 224,8,51200
v_fma_f32 v195, v131, v67, v195
mqa 80,228,184
tq 226,8,53248
v_cvt_pk_bf16_f32 v36, v200, v201
mqa 80,232,208
tq 228,8,55296
v_cvt_pk_bf16_f32 v37, v202, v203
mqa 88,232,212
tq 230,8,57344
v_cvt_pk_bf16_f32 v32, v192, v193
mqa 88,236,220
tq 232,4,18432
v_cvt_pk_bf16_f32 v33, v194, v195
mqa 80,236,216
tq 234,4,20480
s_waitcnt lgkmcnt(6)
mqa 84,240,176
tq 236,4,22528
v_fma_f32 v204, v140, v76, v204
mqa 92,240,180
tq 238,4,24576
v_fma_f32 v205, v141, v77, v205
mqa 92,244,188
tq 240,4,10240
v_fma_f32 v206, v142, v78, v206
mqa 84,244,184
tq 242,4,12288
v_fma_f32 v207, v143, v79, v207
mqa 84,248,208
tq 244,4,14336
v_fma_f32 v196, v132, v68, v196
mqa 92,248,212
tq 246,4,16384
v_fma_f32 v197, v133, v69, v197
mqa 92,252,220
tq 248,16,18432
v_fma_f32 v198, v134, v70, v198
mqa 84,252,216
tq 250,16,20480
s_waitcnt lgkmcnt(6)
mqa 96,224,16
tq 252,16,22528
tq 254,16,24576
mqa 104,224,20
s_waitcnt vmcnt(0)
s_barrier
mqa 104,228,28
tq 224,8,18432
v_fma_f32 v199, v135, v71, v199
v_cvt_pk_bf16_f32 v38, v204, v205
mqa 96,228,24
tq 226,8,20480
v_cvt_pk_bf16_f32 v39, v206, v207
mqa 96,232,48
tq 228,8,22528
v_permlane16_swap_b32 v36, v38
v_permlane16_swap_b32 v37, v39
mqa 104,232,52
tq 230,8,24576
buffer_store_dwordx4 v[36:39], v18, s[32:35], s70 offen offset:0 sc1
mqa 104,236,60
tq 232,16,2048
v_cvt_pk_bf16_f32 v34, v196, v197
v_cvt_pk_bf16_f32 v35, v198, v199
mqa 96,236,56
tq 234,16,4096
s_waitcnt lgkmcnt(6)
mqa 96,240,80
tq 236,16,6144
v_permlane16_swap_b32 v32, v34
mqa 104,240,84
tq 238,16,8192
v_permlane16_swap_b32 v33, v35
mqa 104,244,92
tq 240,4,26624
mqa 96,244,88
tq 242,4,28672
buffer_store_dwordx4 v[32:35], v18, s[32:35], 0 offen offset:0 sc1
v_fma_f32 v216, v152, v88, v216
mqa 96,248,112
tq 244,4,30720
v_fma_f32 v217, v153, v89, v217
mqa 104,248,116
tq 246,4,32768
v_fma_f32 v218, v154, v90, v218
mqa 104,252,124
tq 248,9,51200
v_fma_f32 v219, v155, v91, v219
mqa 96,252,120
tq 250,9,53248
s_waitcnt lgkmcnt(6)
mqa 96,224,144
tq 252,9,55296
v_fma_f32 v208, v144, v80, v208
mqa 104,224,148
tq 254,9,57344
v_fma_f32 v209, v145, v81, v209
mqa 104,228,156
tq 224,5,18432
v_fma_f32 v210, v146, v82, v210
mqa 96,228,152
tq 226,5,20480
v_fma_f32 v211, v147, v83, v211
mqa 96,232,176
tq 228,5,22528
v_cvt_pk_bf16_f32 v44, v216, v217
mqa 104,232,180
tq 230,5,24576
v_cvt_pk_bf16_f32 v45, v218, v219
mqa 104,236,188
tq 232,5,10240
v_cvt_pk_bf16_f32 v40, v208, v209
mqa 96,236,184
tq 234,5,12288
s_waitcnt lgkmcnt(6)
mqa 96,240,208
tq 236,5,14336
v_cvt_pk_bf16_f32 v41, v210, v211
mqa 104,240,212
tq 238,5,16384
v_fma_f32 v220, v156, v92, v220
mqa 104,244,220
tq 240,17,18432
v_fma_f32 v221, v157, v93, v221
mqa 96,244,216
tq 242,17,20480
v_fma_f32 v222, v158, v94, v222
mqa 100,248,16
tq 244,17,22528
v_fma_f32 v223, v159, v95, v223
mqa 108,248,20
tq 246,17,24576
v_fma_f32 v212, v148, v84, v212
mqa 108,252,28
tq 248,9,18432
v_fma_f32 v213, v149, v85, v213
mqa 100,252,24
tq 250,9,20480
s_waitcnt lgkmcnt(6)
mqa 100,224,48
tq 252,9,22528
tq 254,9,24576
mqa 108,224,52
s_barrier
mqa 108,228,60
tq 224,17,2048
v_fma_f32 v214, v150, v86, v214
mqa 100,228,56
tq 226,17,4096
v_fma_f32 v215, v151, v87, v215
mqa 100,232,80
tq 228,17,6144
v_cvt_pk_bf16_f32 v46, v220, v221
mqa 108,232,84
s_nop 0
L_hop8_3_%=:
s_cbranch_execz L_ret_3_%=
tq 230,17,8192
v_cvt_pk_bf16_f32 v47, v222, v223
mqa 108,236,92
tq 232,5,26624
v_permlane16_swap_b32 v44, v46
mqa 100,236,88
tq 234,5,28672
s_waitcnt lgkmcnt(6)
mqa 100,240,112
tq 236,5,30720
v_permlane16_swap_b32 v45, v47
mqa 108,240,116
tq 238,5,32768
buffer_store_dwordx4 v[44:47], v18, s[32:35], s70 offen offset:64 sc1
mqa 108,244,124
tq 240,12,51200
v_cvt_pk_bf16_f32 v42, v212, v213
mqa 100,244,120
tq 242,12,53248
v_cvt_pk_bf16_f32 v43, v214, v215
mqa 100,248,144
tq 244,12,55296
v_permlane16_swap_b32 v40, v42
mqa 108,248,148
tq 246,12,57344
v_permlane16_swap_b32 v41, v43
mqa 108,252,156
tq 248,6,18432
mqa 100,252,152
tq 250,6,20480
buffer_store_dwordx4 v[40:43], v18, s[32:35], 0 offen offset:64 sc1
s_waitcnt lgkmcnt(6)
mqa 100,224,176
tq 252,6,22528
v_fma_f32 v232, v168, v104, v232
mqa 108,224,180
tq 254,6,24576
v_fma_f32 v233, v169, v105, v233
mqa 108,228,188
tq 224,6,10240
v_fma_f32 v234, v170, v106, v234
mqa 100,228,184
tq 226,6,12288
v_fma_f32 v235, v171, v107, v235
mqa 100,232,208
tq 228,6,14336
v_fma_f32 v224, v160, v96, v224
mqa 108,232,212
tq 230,6,16384
v_fma_f32 v225, v161, v97, v225
mqa 108,236,220
tq 232,28,18432
v_fma_f32 v226, v162, v98, v226
mqa 100,236,216
tq 234,28,20480
s_waitcnt lgkmcnt(6)
mqa 112,240,16
tq 236,28,22528
v_fma_f32 v227, v163, v99, v227
mqa 120,240,20
tq 238,28,24576
v_cvt_pk_bf16_f32 v52, v232, v233
mqa 120,244,28
tq 240,12,18432
v_cvt_pk_bf16_f32 v53, v234, v235
mqa 112,244,24
tq 242,12,20480
v_cvt_pk_bf16_f32 v48, v224, v225
mqa 112,248,48
tq 244,12,22528
v_cvt_pk_bf16_f32 v49, v226, v227
mqa 120,248,52
tq 246,12,24576
v_fma_f32 v236, v172, v108, v236
mqa 120,252,60
tq 248,28,2048
v_fma_f32 v237, v173, v109, v237
mqa 112,252,56
tq 250,28,4096
s_waitcnt lgkmcnt(6)
mqa 112,224,80
tq 252,28,6144
tq 254,28,8192
mqa 120,224,84
s_barrier
mqa 120,228,92
tq 224,6,26624
v_fma_f32 v238, v174, v110, v238
mqa 112,228,88
tq 226,6,28672
v_fma_f32 v239, v175, v111, v239
mqa 112,232,112
tq 228,6,30720
v_fma_f32 v228, v164, v100, v228
mqa 120,232,116
tq 230,6,32768
v_fma_f32 v229, v165, v101, v229
mqa 120,236,124
tq 232,13,51200
v_fma_f32 v230, v166, v102, v230
mqa 112,236,120
tq 234,13,53248
s_waitcnt lgkmcnt(6)
mqa 112,240,144
tq 236,13,55296
v_fma_f32 v231, v167, v103, v231
mqa 120,240,148
tq 238,13,57344
v_cvt_pk_bf16_f32 v54, v236, v237
mqa 120,244,156
tq 240,7,18432
v_cvt_pk_bf16_f32 v55, v238, v239
mqa 112,244,152
tq 242,7,20480
v_permlane16_swap_b32 v52, v54
mqa 112,248,176
tq 244,7,22528
v_permlane16_swap_b32 v53, v55
mqa 120,248,180
tq 246,7,24576
buffer_store_dwordx4 v[52:55], v3, s[32:35], s70 offen offset:0 sc1
mqa 120,252,188
tq 248,7,10240
v_cvt_pk_bf16_f32 v50, v228, v229
mqa 112,252,184
tq 250,7,12288
s_waitcnt lgkmcnt(6)
mqa 112,224,208
tq 252,7,14336
v_cvt_pk_bf16_f32 v51, v230, v231
mqa 120,224,212
tq 254,7,16384
v_permlane16_swap_b32 v48, v50
mqa 120,228,220
tq 224,29,18432
v_permlane16_swap_b32 v49, v51
mqa 112,228,216
tq 226,29,20480
buffer_store_dwordx4 v[48:51], v3, s[32:35], 0 offen offset:0 sc1
mqa 116,232,16
tq 228,29,22528
mqa 124,232,20
tq 230,29,24576
mqa 124,236,28
tq 232,13,18432
v_fma_f32 v248, v184, v120, v248
mqa 116,236,24
tq 234,13,20480
s_waitcnt lgkmcnt(6)
mqa 116,240,48
tq 236,13,22528
v_fma_f32 v249, v185, v121, v249
mqa 124,240,52
tq 238,13,24576
v_fma_f32 v250, v186, v122, v250
mqa 124,244,60
tq 240,29,2048
v_fma_f32 v251, v187, v123, v251
mqa 116,244,56
tq 242,29,4096
v_fma_f32 v240, v176, v112, v240
mqa 116,248,80
tq 244,29,6144
v_fma_f32 v241, v177, v113, v241
mqa 124,248,84
tq 246,29,8192
v_fma_f32 v242, v178, v114, v242
mqa 124,252,92
tq 248,7,26624
v_fma_f32 v243, v179, v115, v243
mqa 116,252,88
tq 250,7,28672
s_waitcnt lgkmcnt(6)
mqa 124,224,116
tq 252,7,30720
v_cvt_pk_bf16_f32 v60, v248, v249
mqa 124,228,124
tq 254,7,32768
v_cvt_pk_bf16_f32 v61, v250, v251
mqa 124,232,148
v_cvt_pk_bf16_f32 v56, v240, v241
v_cvt_pk_bf16_f32 v57, v242, v243
mqa 124,236,156
s_waitcnt lgkmcnt(4)
mqa 124,240,180
mqa 124,244,188
s_waitcnt lgkmcnt(0)
mqa 124,248,212
mqa 124,252,220
mqa 116,224,112
mqa 116,228,120
mqa 116,232,144
s_nop 4
v_fma_f32 v252, v188, v124, v252
v_fma_f32 v253, v189, v125, v253
mqa 116,236,152
v_fma_f32 v254, v190, v126, v254
v_fma_f32 v255, v191, v127, v255
mqa 116,240,176
v_cvt_pk_bf16_f32 v62, v252, v253
v_cvt_pk_bf16_f32 v63, v254, v255
mqa 116,244,184
v_permlane16_swap_b32 v60, v62
v_permlane16_swap_b32 v61, v63
mqa 116,248,208
buffer_store_dwordx4 v[60:63], v3, s[32:35], s70 offen offset:64 sc1
mqa 116,252,216
s_nop 7
v_fma_f32 v244, v180, v116, v244
v_fma_f32 v245, v181, v117, v245
v_fma_f32 v246, v182, v118, v246
v_fma_f32 v247, v183, v119, v247
v_cvt_pk_bf16_f32 v58, v244, v245
v_cvt_pk_bf16_f32 v59, v246, v247
s_nop 0
v_permlane16_swap_b32 v56, v58
v_permlane16_swap_b32 v57, v59
buffer_store_dwordx4 v[56:59], v3, s[32:35], 0 offen offset:64 sc1
s_endpgm
L_slow0_3_%=:
s_mov_b32 s62, 0
L_loop0_3_%=:
s_and_b32 s59, vcc_lo, 0xff00
s_cbranch_scc0 L_norep0_3_%=
v_cndmask_b32 v22, v22, v23, s[60:61]
buffer_store_dword v22, v21, s[28:31], 0 offen offset:0 sc1
L_norep0_3_%=:
buffer_load_dword v23, v20, s[28:31], 0 offen offset:0 sc1
s_waitcnt vmcnt(0)
v_cmp_ne_u32 vcc, v23, v22
s_cbranch_vccz L_back0_3_%=
s_add_u32 s62, s62, 1
s_cmp_lt_u32 s62, 100000
s_cbranch_scc1 L_loop0_3_%=
s_branch L_back0_3_%=
L_slow1_3_%=:
s_mov_b32 s62, 0
L_loop1_3_%=:
s_and_b32 s59, vcc_lo, 0xff00
s_cbranch_scc0 L_norep1_3_%=
v_cndmask_b32 v22, v22, v23, s[60:61]
buffer_store_dword v22, v21, s[28:31], 0 offen offset:0 sc1
buffer_store_dword v22, v21, s[28:31], 0 offen offset:32 sc1
L_norep1_3_%=:
buffer_load_dword v23, v20, s[28:31], 0 offen offset:32 sc1
s_waitcnt vmcnt(0)
v_cmp_ne_u32 vcc, v23, v22
s_cbranch_vccz L_back1_3_%=
s_add_u32 s62, s62, 1
s_cmp_lt_u32 s62, 100000
s_cbranch_scc1 L_loop1_3_%=
s_branch L_back1_3_%=
L_slow2_3_%=:
s_mov_b32 s62, 0
L_loop2_3_%=:
s_and_b32 s59, vcc_lo, 0xff00
s_cbranch_scc0 L_norep2_3_%=
v_cndmask_b32 v22, v22, v23, s[60:61]
buffer_store_dword v22, v21, s[28:31], 0 offen offset:0 sc1
buffer_store_dword v22, v21, s[28:31], 0 offen offset:32 sc1
buffer_store_dword v22, v21, s[28:31], 0 offen offset:64 sc1
L_norep2_3_%=:
buffer_load_dword v23, v20, s[28:31], 0 offen offset:64 sc1
s_waitcnt vmcnt(0)
v_cmp_ne_u32 vcc, v23, v22
s_cbranch_vccz L_back2_3_%=
s_add_u32 s62, s62, 1
s_cmp_lt_u32 s62, 100000
s_cbranch_scc1 L_loop2_3_%=
s_branch L_back2_3_%=
L_slow3_3_%=:
s_mov_b32 s62, 0
L_loop3_3_%=:
s_and_b32 s59, vcc_lo, 0xff00
s_cbranch_scc0 L_norep3_3_%=
v_cndmask_b32 v22, v22, v23, s[60:61]
buffer_store_dword v22, v21, s[28:31], 0 offen offset:0 sc1
buffer_store_dword v22, v21, s[28:31], 0 offen offset:32 sc1
buffer_store_dword v22, v21, s[28:31], 0 offen offset:64 sc1
buffer_store_dword v22, v21, s[28:31], 0 offen offset:96 sc1
L_norep3_3_%=:
buffer_load_dword v23, v20, s[28:31], 0 offen offset:96 sc1
s_waitcnt vmcnt(0)
v_cmp_ne_u32 vcc, v23, v22
s_cbranch_vccz L_back3_3_%=
s_add_u32 s62, s62, 1
s_cmp_lt_u32 s62, 100000
s_cbranch_scc1 L_loop3_3_%=
s_branch L_back3_3_%=
.p2align 12
L_last_%=:
.purgem mqa
.purgem mqv
.purgem tq
)ASM"
    :
    : "{s[0:1]}"((unsigned long long)(size_t)__builtin_amdgcn_kernarg_segment_ptr()), "{s2}"(blockIdx.x), "{v0}"(threadIdx.x), "{s19}"((unsigned)(size_t)lds)
    : "v1", "v2", "v3", "v4", "v5", "v6", "v7", "v8", "v9", "v10", "v11", "v12", "v13", "v14", "v15", "v16", "v17", "v18", "v19", "v20", "v21", "v22", "v23", "v24", "v25", "v26", "v27", "v28", "v29", "v30", "v31", "v32", "v33", "v34", "v35", "v36", "v37", "v38", "v39", "v40", "v41", "v42", "v43", "v44", "v45", "v46", "v47", "v48", "v49", "v50", "v51", "v52", "v53", "v54", "v55", "v56", "v57", "v58", "v59", "v60", "v61", "v62", "v63", "v64", "v65", "v66", "v67", "v68", "v69", "v70", "v71", "v72", "v73", "v74", "v75", "v76", "v77", "v78", "v79", "v80", "v81", "v82", "v83", "v84", "v85", "v86", "v87", "v88", "v89", "v90", "v91", "v92", "v93", "v94", "v95", "v96", "v97", "v98", "v99", "v100", "v101", "v102", "v103", "v104", "v105", "v106", "v107", "v108", "v109", "v110", "v111", "v112", "v113", "v114", "v115", "v116", "v117", "v118", "v119", "v120", "v121", "v122", "v123", "v124", "v125", "v126", "v127", "v128", "v129", "v130", "v131", "v132", "v133", "v134", "v135", "v136", "v137", "v138", "v139", "v140", "v141", "v142", "v143", "v144", "v145", "v146", "v147", "v148", "v149", "v150", "v151", "v152", "v153", "v154", "v155", "v156", "v157", "v158", "v159", "v160", "v161", "v162", "v163", "v164", "v165", "v166", "v167", "v168", "v169", "v170", "v171", "v172", "v173", "v174", "v175", "v176", "v177", "v178", "v179", "v180", "v181", "v182", "v183", "v184", "v185", "v186", "v187", "v188", "v189", "v190", "v191", "v192", "v193", "v194", "v195", "v196", "v197", "v198", "v199", "v200", "v201", "v202", "v203", "v204", "v205", "v206", "v207", "v208", "v209", "v210", "v211", "v212", "v213", "v214", "v215", "v216", "v217", "v218", "v219", "v220", "v221", "v222", "v223", "v224", "v225", "v226", "v227", "v228", "v229", "v230", "v231", "v232", "v233", "v234", "v235", "v236", "v237", "v238", "v239", "v240", "v241", "v242", "v243", "v244", "v245", "v246", "v247", "v248", "v249", "v250", "v251", "v252", "v253", "v254", "v255", "a0", "a1", "a2", "a3", "a4", "a5", "a6", "a7", "a8", "a9", "a10", "a11", "a12", "a13", "a14", "a15", "a16", "a17", "a18", "a19", "a20", "a21", "a22", "a23", "a24", "a25", "a26", "a27", "a28", "a29", "a30", "a31", "a32", "a33", "a34", "a35", "a36", "a37", "a38", "a39", "a40", "a41", "a42", "a43", "a44", "a45", "a46", "a47", "a48", "a49", "a50", "a51", "a52", "a53", "a54", "a55", "a56", "a57", "a58", "a59", "a60", "a61", "a62", "a63", "a64", "a65", "a66", "a67", "a68", "a69", "a70", "a71", "a72", "a73", "a74", "a75", "a76", "a77", "a78", "a79", "a80", "a81", "a82", "a83", "a84", "a85", "a86", "a87", "a88", "a89", "a90", "a91", "a92", "a93", "a94", "a95", "a96", "a97", "a98", "a99", "a100", "a101", "a102", "a103", "a104", "a105", "a106", "a107", "a108", "a109", "a110", "a111", "a112", "a113", "a114", "a115", "a116", "a117", "a118", "a119", "a120", "a121", "a122", "a123", "a124", "a125", "a126", "a127", "a128", "a129", "a130", "a131", "a132", "a133", "a134", "a135", "a136", "a137", "a138", "a139", "a140", "a141", "a142", "a143", "a144", "a145", "a146", "a147", "a148", "a149", "a150", "a151", "a152", "a153", "a154", "a155", "a156", "a157", "a158", "a159", "a160", "a161", "a162", "a163", "a164", "a165", "a166", "a167", "a168", "a169", "a170", "a171", "a172", "a173", "a174", "a175", "a176", "a177", "a178", "a179", "a180", "a181", "a182", "a183", "a184", "a185", "a186", "a187", "a188", "a189", "a190", "a191", "a192", "a193", "a194", "a195", "a196", "a197", "a198", "a199", "a200", "a201", "a202", "a203", "a204", "a205", "a206", "a207", "a208", "a209", "a210", "a211", "a212", "a213", "a214", "a215", "a216", "a217", "a218", "a219", "a220", "a221", "a222", "a223", "a224", "a225", "a226", "a227", "a228", "a229", "a230", "a231", "a232", "a233", "a234", "a235", "a236", "a237", "a238", "a239", "a240", "a241", "a242", "a243", "a244", "a245", "a246", "a247", "a248", "a249", "a250", "a251", "a252", "a253", "a254", "a255", "s3", "s4", "s5", "s6", "s7", "s8", "s9", "s10", "s11", "s12", "s13", "s14", "s15", "s16", "s17", "s18", "s20", "s21", "s22", "s23", "s24", "s25", "s26", "s27", "s28", "s29", "s30", "s31", "s33", "s34", "s35", "s36", "s37", "s38", "s39", "s40", "s41", "s42", "s43", "s44", "s45", "s46", "s47", "s48", "s49", "s50", "s51", "s52", "s53", "s54", "s55", "s56", "s57", "s58", "s59", "s60", "s61", "s62", "s63", "s64", "s65", "s66", "s67", "s68", "s69", "s70", "s71", "s72", "s73", "s74", "s75", "s76", "s77", "s78", "s79", "s80", "s81", "s82", "s83", "s84", "s85", "s86", "s87", "s88", "s89", "s90", "s91", "s92", "s93", "s94", "s95", "s96", "s97", "vcc", "scc", "memory");
}

extern "C" void kernel_launch(void* const* d_in, const int* in_sizes, int n_in, void* d_out, int out_size, void* d_ws,
                              size_t ws_size, hipStream_t stream) {
  const unsigned char* W = (const unsigned char*)d_in[1];
  const unsigned F = 0x20000u;
  dcn_fused<<<256, 256, 0, stream>>>(W, 0x200000u, F, d_in[0], 0x800000u, F,
                                     (unsigned char*)d_ws + ((0 - (size_t)d_ws) & 1023) + (0ull << 20), 35651584u, F, d_out, 0x800000u, F,
                                     W + 0x200000, 0x200000u, F, W + 0x400000, 0x200000u, F, d_in[2], 6144u, F);
}
```
